```python
import torch

def _softmax(x, axis=-1):
    return torch.softmax(x, dim=axis)
def _gelu(x):
    return torch.nn.functional.gelu(x, approximate="tanh")
def _silu(x):
    return torch.nn.functional.silu(x)
def _sigmoid(x):
    return torch.sigmoid(x)
def _layernorm(x, axis=-1, eps=1e-5):
    m = x.mean(dim=axis, keepdim=True)
    v = x.var(dim=axis, keepdim=True, unbiased=False)
    return (x - m) / torch.sqrt(v + eps)
def _scatter_add(tgt, idx, src, axis=0):
    return tgt.clone().index_add_(axis, idx, src)
def _segment_sum(src, seg, n):
    out = torch.zeros((n,) + src.shape[1:], dtype=src.dtype)
    return out.index_add_(0, seg, src)
def _pad(x, pw):
    flat = tuple(v for pair in reversed(pw) for v in pair)
    return torch.nn.functional.pad(x, flat)

kernel_name = 'standalone__mha_block_cached__near_ridge'

def setup_inputs():

    _w = (lambda a: (lambda t: t.to(torch.bfloat16) if t.is_floating_point() else t)(torch.as_tensor(a)))
    B, Sq, D = 32, 160, 576
    H, Sk, Dh = 32, 512, 128
    return {
        "x": _w((lambda *s: torch.randn(*s, dtype=torch.bfloat16) * 0.8)(B, Sq, D)),
        "k_cache": _w((lambda *s: torch.randn(*s, dtype=torch.bfloat16) * 0.8)(B, H, Sk, Dh)),
        "v_cache": _w((lambda *s: torch.randn(*s, dtype=torch.bfloat16) * 0.05)(B, H, Sk, Dh)),
        "wq": _w((lambda *s: torch.randn(*s, dtype=torch.bfloat16) * 0.8)(D, H * Dh) * (D ** -0.5)),
        "wo": _w((lambda *s: torch.randn(*s, dtype=torch.bfloat16) * 0.05)(H * Dh, D) * ((H * Dh) ** -0.5)),
    }

def reference(x, k_cache, v_cache, wq, wo):
    B, Sq, D = x.shape
    H, Dh = 32, 128
    q = (lambda x, *d: x.permute(*d))((x @ wq).reshape(B, Sq, H, Dh), 0, 2, 1, 3)
    scores = torch.einsum("bhqd,bhkd->bhqk", q, k_cache) * (Dh ** -0.5)
    p = _softmax(scores, axis=-1)
    o = torch.einsum("bhqk,bhkd->bhqd", p, v_cache)
    return (lambda x, *d: x.permute(*d))(o, 0, 2, 1, 3).reshape(B, Sq, H * Dh) @ wo
```

```cpp
#include <hip/hip_runtime.h>
#include <hip/hip_bf16.h>
typedef __hip_bfloat16 bf16;
constexpr int B=32,SQ=160,D=576,H=32,SK=512,DH=128;
__global__ void gemm_naive(const bf16* A,const bf16* W,bf16* C,int M,int N,int K){
  __shared__ float As[16][17], Ws[16][17];
  int tx=threadIdx.x&15, ty=threadIdx.x>>4;
  int row=blockIdx.y*16+ty, col=blockIdx.x*16+tx;
  float acc=0;
  for(int k0=0;k0<K;k0+=16){
    As[ty][tx]=__bfloat162float(A[(long)row*K+k0+tx]);
    Ws[ty][tx]=__bfloat162float(W[(long)(k0+ty)*N+col]);
    __syncthreads();
    for(int k=0;k<16;++k) acc+=As[ty][k]*Ws[k][tx];
    __syncthreads();
  }
  C[(long)row*N+col]=__float2bfloat16(acc);
}
__global__ void attn_naive(const bf16* Q,const bf16* Kc,const bf16* Vc,bf16* O){
  int q=blockIdx.x%SQ, bh=blockIdx.x/SQ, h=bh%H, b=bh/H;
  __shared__ float qs[DH], p[SK], red[128];
  int t=threadIdx.x;
  qs[t]=__bfloat162float(Q[((long)(b*SQ+q))*(H*DH)+h*DH+t]);
  __syncthreads();
  const bf16* Kp=Kc+(long)bh*SK*DH; const bf16* Vp=Vc+(long)bh*SK*DH;
  float mx=-1e30f;
  for(int k=t;k<SK;k+=128){ float s=0; for(int d=0;d<DH;++d) s+=qs[d]*__bfloat162float(Kp[k*DH+d]); s*=0.08838834764831845f; p[k]=s; mx=fmaxf(mx,s);}
  red[t]=mx; __syncthreads();
  for(int o=64;o>0;o>>=1){ if(t<o) red[t]=fmaxf(red[t],red[t+o]); __syncthreads(); }
  mx=red[0]; __syncthreads();
  float sum=0;
  for(int k=t;k<SK;k+=128){ float e=__expf(p[k]-mx); p[k]=e; sum+=e; }
  red[t]=sum; __syncthreads();
  for(int o=64;o>0;o>>=1){ if(t<o) red[t]+=red[t+o]; __syncthreads(); }
  sum=red[0];
  float acc=0;
  for(int k=0;k<SK;++k) acc+=p[k]*__bfloat162float(Vp[k*DH+t]);
  O[((long)(b*SQ+q))*(H*DH)+h*DH+t]=__float2bfloat16(acc/sum);
}
extern "C" void kernel_launch(void* const* d_in, const int* in_sizes, int n_in, void* d_out, int out_size, void* d_ws, size_t ws_size, hipStream_t stream) {
  const bf16* x=(const bf16*)d_in[0]; const bf16* kc=(const bf16*)d_in[1]; const bf16* vc=(const bf16*)d_in[2];
  const bf16* wq=(const bf16*)d_in[3]; const bf16* wo=(const bf16*)d_in[4];
  bf16* Q=(bf16*)d_ws; bf16* O=Q+(long)B*SQ*H*DH;
  gemm_naive<<<dim3(H*DH/16,B*SQ/16),256,0,stream>>>(x,wq,Q,B*SQ,H*DH,D);
  attn_naive<<<B*H*SQ,128,0,stream>>>(Q,kc,vc,O);
  gemm_naive<<<dim3(D/16,B*SQ/16),256,0,stream>>>(O,wo,(bf16*)d_out,B*SQ,D,H*DH);
}
```

```cpp
#include <hip/hip_runtime.h>
#include <hip/hip_bf16.h>

typedef unsigned short u16;
typedef unsigned long long u64;
constexpr int NB = 32, SQ = 160, DM = 576, NH = 32, SK = 512, DH = 128;
constexpr int PART_BYTES = SQ * DM * 2;
constexpr size_t SYNC_OFF = 48u << 20;

__device__ __forceinline__ float bf2f(u16 v) { return __uint_as_float((unsigned)v << 16); }
__device__ __forceinline__ float rbf(float f) { return __bfloat162float(__float2bfloat16(f)); }

__global__ __launch_bounds__(256, 1) void mha_main(const char* x, const char* kc, const char* vc,
                                                   const char* wq, const char* wo, char* ws, u16* out) {
  extern __shared__ __attribute__((aligned(16))) char lds[];
  unsigned bid = blockIdx.x;
  unsigned wave = __builtin_amdgcn_readfirstlane(threadIdx.x >> 6);
  typedef __attribute__((address_space(4))) const u64 cu64;
  cu64* ka = (cu64*)__builtin_amdgcn_kernarg_segment_ptr();
  unsigned e0, e1;
  {
    const int xcd = bid & 7, idx = bid >> 3;
    const int b = 8 * (xcd >> 1) + (idx & 7), G = 4 * (xcd & 1) + (idx >> 3);
    const int ro = b & 7;
    int rq = (b & 7) + 2 * (G & 3);
    rq -= rq >= 9 ? 9 : 0;
    const char* xp = x + (size_t)b * (SQ * DM * 2) + rq * 128;
    const char* wqp = wq + G * 1024 - 1024 + (size_t)rq * (64 * 8192);
    const int rk = idx & 15;
    const char* kp = kc + (size_t)(b * NH + 4 * G) * (SK * DH * 2) - 1024 + rk * 8192 - 131072;
    const char* vp = vc + (size_t)(b * NH + 4 * G) * (SK * DH * 2) - 1024 + rk * 8192 - 131072;
    const char* wop = wo + (size_t)G * (512 * DM * 2) + ro * 128;
    char* pp = ws + (size_t)(b * 8 + G) * PART_BYTES;
    char* sy = ws + SYNC_OFF + b * 2048;
    u16* op = out + (size_t)b * (SQ * DM);
    const unsigned ldsb = (unsigned)(size_t)lds;
    asm volatile(
R"ASM(.macro LD v,r,s,o
buffer_load_dwordx4 v\v, s[\r:\r+3], s\s offen offset:\o lds
.endm
.macro Ta d,a,o
ds_read_b64_tr_b16 a[\d:\d+1], v\a offset:\o
.endm
.macro Rv d,a,o
ds_read_b128 v[\d:\d+3], v\a offset:\o
.endm
.macro WL n
s_waitcnt lgkmcnt(\n)
.endm
.macro Zvav d,a,b
v_mfma_f32_16x16x32_bf16 v[\d:\d+3], a[\a:\a+3], v[\b:\b+3], 0
.endm
.macro Tv d,a,o
ds_read_b64_tr_b16 v[\d:\d+1], v\a offset:\o
.endm
.macro Zvvv d,a,b
v_mfma_f32_16x16x32_bf16 v[\d:\d+3], v[\a:\a+3], v[\b:\b+3], 0
.endm
.macro Mvav d,a,b
v_mfma_f32_16x16x32_bf16 v[\d:\d+3], a[\a:\a+3], v[\b:\b+3], v[\d:\d+3]
.endm
.macro Mvvv d,a,b
v_mfma_f32_16x16x32_bf16 v[\d:\d+3], v[\a:\a+3], v[\b:\b+3], v[\d:\d+3]
.endm
.macro LN v,r,s,o
buffer_load_dwordx4 v\v, s[\r:\r+3], s\s offen offset:\o sc1 nt lds
.endm
.macro CV d,a,b
v_cvt_pk_bf16_f32 v\d, v\a, v\b
.endm
.macro AW d,a
v_accvgpr_write_b32 a\d, v\a
.endm
.macro Ra d,a,o
ds_read_b128 a[\d:\d+3], v\a offset:\o
.endm
.macro Zvva d,a,b
v_mfma_f32_16x16x32_bf16 v[\d:\d+3], v[\a:\a+3], a[\b:\b+3], 0
.endm
.macro Mvva d,a,b
v_mfma_f32_16x16x32_bf16 v[\d:\d+3], v[\a:\a+3], a[\b:\b+3], v[\d:\d+3]
.endm
.macro Mvaa d,a,b
v_mfma_f32_16x16x32_bf16 v[\d:\d+3], a[\a:\a+3], a[\b:\b+3], v[\d:\d+3]
.endm
.macro Nvaa d,a,b,c
v_mfma_f32_16x16x32_bf16 v[\d:\d+3], a[\a:\a+3], a[\b:\b+3], v[\c:\c+3]
.endm
.macro EX d
v_exp_f32 v\d, v\d
.endm
.macro Zavv d,a,b
v_mfma_f32_16x16x32_bf16 a[\d:\d+3], v[\a:\a+3], v[\b:\b+3], 0
.endm
.macro Zaav d,a,b
v_mfma_f32_16x16x32_bf16 a[\d:\d+3], a[\a:\a+3], v[\b:\b+3], 0
.endm
.macro Mavv d,a,b
v_mfma_f32_16x16x32_bf16 a[\d:\d+3], v[\a:\a+3], v[\b:\b+3], a[\d:\d+3]
.endm
.macro Maav d,a,b
v_mfma_f32_16x16x32_bf16 a[\d:\d+3], a[\a:\a+3], v[\b:\b+3], a[\d:\d+3]
.endm
.macro Nvva d,a,b,c
v_mfma_f32_16x16x32_bf16 v[\d:\d+3], v[\a:\a+3], a[\b:\b+3], v[\c:\c+3]
.endm
.macro Nvav d,a,b,c
v_mfma_f32_16x16x32_bf16 v[\d:\d+3], a[\a:\a+3], v[\b:\b+3], v[\c:\c+3]
.endm
.macro Nvvv d,a,b,c
v_mfma_f32_16x16x32_bf16 v[\d:\d+3], v[\a:\a+3], v[\b:\b+3], v[\c:\c+3]
.endm
.macro AR d,a
v_accvgpr_read_b32 v\d, a\a
.endm
s_cmp_ge_u32 %8,2
s_cbranch_scc0 .Lmha_p0
v_mbcnt_lo_u32_b32 v228,-1,0
v_mbcnt_hi_u32_b32 v228,-1,v228
s_and_b32 s87,%8,1
s_mov_b64 s[24:25],%2
s_and_b32 s25,s25,0xffff
s_mov_b32 s26,184320
s_mov_b32 s27,0x20000
s_mov_b64 s[28:29],%3
s_and_b32 s29,s29,0xffff
s_mov_b32 s30,4719616
s_mov_b32 s31,0x20000
s_and_b32 s80,%12,0x700
s_lshl_b32 s80,s80,2
s_sub_u32 s30,s30,s80
s_lshl_b32 s80,s87,12
s_add_u32 s76,s80,%9
s_lshl_b32 s80,s87,10
s_add_u32 s77,s80,%9
s_mul_i32 s48,s87,9216
s_add_u32 s49,s48,16384
s_add_u32 s50,s48,36864
s_add_u32 s51,s48,53248
s_add_u32 s52,s48,73728
s_add_u32 s53,s48,90112
s_add_u32 s54,s48,110592
s_add_u32 s55,s48,126976
s_add_u32 s56,s48,147456
s_add_u32 s57,s48,163840
s_lshl_b32 s82,s87,16
s_add_u32 s64,s82,1024
s_add_u32 s65,s82,128
s_add_u32 s66,s82,130048
s_add_u32 s67,s82,129152
v_lshrrev_b32 v233,3,v228
v_lshrrev_b32 v230,4,v228
s_lshl_b32 s80,s87,2
v_or_b32 v234,s80,v230
v_and_b32 v235,7,v228
v_xor_b32 v234,v234,v235
v_lshlrev_b32 v234,4,v234
v_mul_u32_u24 v235,1152,v233
v_add_u32 v0,v235,v234
v_lshl_add_u32 v1,v233,8,v234
v_and_b32 v232,3,v228
v_bfe_u32 v237,v228,2,3
v_bfe_u32 v234,v228,4,1
v_lshlrev_b32 v234,1,v234
v_xor_b32 v234,v234,v232
v_lshlrev_b32 v234,4,v234
v_lshrrev_b32 v238,5,v228
v_lshl_add_u32 v238,v238,6,v234
s_lshl_b32 s81,s87,5
v_xor_b32 v236,s81,v238
v_lshl_add_u32 v3,v237,13,v236
s_add_u32 m0,s76,73728
v_add_u32 v3,256,v3
LD 3,28,64,0
LD 3,28,65,1024
LD 3,28,66,2048
LD 3,28,67,3072
s_add_u32 m0,s77,98304
s_nop 0
LD 0,24,49,2048
s_add_u32 m0,s77,102400
s_nop 0
LD 0,24,51,2048
s_add_u32 m0,s77,106496
s_nop 0
LD 0,24,53,2048
s_add_u32 m0,s77,110592
s_nop 0
LD 0,24,55,2048
s_add_u32 m0,s77,114688
s_nop 0
LD 0,24,57,2048
s_add_u32 m0,s76,90112
v_add_u32 v3,262144,v3
LD 3,28,64,0
LD 3,28,65,1024
LD 3,28,66,2048
LD 3,28,67,3072
v_lshlrev_b32 v5,4,v228
s_bfe_u32 s88,%12,0x4000b
s_sub_u32 s88,16,s88
s_bfe_u32 s89,%12,0x40018
s_bfe_u32 s90,%12,0x4001c
s_mov_b64 s[68:69],%2
s_sub_u32 s68,s68,1152
s_subb_u32 s69,s69,0
s_mov_b64 s[70:71],%3
s_sub_u32 s70,s70,4718592
s_subb_u32 s71,s71,0
v_lshl_add_u32 v2,v237,8,v238
v_mul_u32_u24 v236,1152,v237
v_add_u32 v4,v236,v238
s_mov_b64 s[36:37],%4
s_and_b32 s37,s37,0xffff
s_mov_b32 s38,656384
s_mov_b32 s39,0x20000
s_mov_b64 s[96:97],%5
s_and_b32 s97,s97,0xffff
s_mov_b32 s98,656384
s_mov_b32 s99,0x20000
s_mov_b64 s[40:41],%6
s_and_b32 s41,s41,0xffff
s_mov_b32 s42,589824
s_mov_b32 s43,0x20000
s_mov_b64 s[44:45],%7
s_and_b32 s45,s45,0xffff
s_mov_b32 s46,184320
s_mov_b32 s47,0x20000
s_lshl_b32 s80,%8,13
s_add_u32 s78,s80,%9
s_lshl_b32 s82,s87,11
s_add_u32 s58,s82,1024
s_add_u32 s59,s82,128
s_add_u32 s60,s82,3072
s_add_u32 s61,s82,2176
s_lshl_b32 s82,s87,12
s_add_u32 s62,s82,1024
s_add_u32 s63,s82,128
s_mul_i32 s79,%8,5120
s_add_u32 s84,s79,4096
s_mov_b32 s85,0x3e0293ee
v_and_b32 v229,15,v228
v_bfe_u32 v231,v228,2,2
v_lshrrev_b32 v233,1,v229
v_xor_b32 v233,v233,v230
v_and_b32 v234,7,v229
v_lshlrev_b32 v234,7,v234
v_lshl_add_u32 v233,v233,4,v234
v_lshrrev_b32 v234,3,v229
s_add_u32 s81,%9,8192
v_lshl_add_u32 v235,v234,12,v233
v_add_u32 v6,s81,v235
v_xor_b32 v235,64,v235
v_add_u32 v7,s81,v235
s_mul_i32 s82,s87,10240
s_add_u32 s82,s82,%9
s_add_u32 s82,s82,98304
v_lshl_add_u32 v235,v234,10,v233
v_add_u32 v11,s82,v235
v_xor_b32 v235,64,v235
v_add_u32 v12,s82,v235
v_and_b32 v233,1,v230
v_lshlrev_b32 v234,6,v231
v_lshl_add_u32 v234,v233,5,v234
v_lshl_add_u32 v234,v232,3,v234
v_lshrrev_b32 v235,1,v230
s_add_u32 s83,s81,32768
v_lshlrev_b32 v236,4,v232
v_lshlrev_b32 v237,5,v233
v_xor_b32 v236,v236,v237
v_lshl_add_u32 v236,v231,6,v236
v_lshl_add_u32 v236,v235,11,v236
v_lshl_add_u32 v236,v233,12,v236
v_add_u32 v9,s83,v236
v_xor_b32 v236,32,v236
v_add_u32 v10,s83,v236
s_add_u32 s83,s81,49152
v_lshl_add_u32 v234,v233,8,v234
v_lshl_add_u32 v236,v235,11,v234
v_add_u32 v8,s83,v236
v_xor_b32 v236,32,v236
v_add_u32 v252,s83,v236
s_add_u32 s83,%9,512
v_lshl_add_u32 v236,v235,10,v234
v_add_u32 v13,s83,v236
v_xor_b32 v236,32,v236
v_add_u32 v14,s83,v236
v_add_u32 v253,0x10000,v13
v_add_u32 v254,0x10000,v14
s_mul_i32 s82,s87,10240
s_add_u32 s82,s82,%9
s_add_u32 s83,s82,86016
v_add_u32 v15,s83,v5
s_add_u32 s83,s82,65536
v_add_u32 v16,s83,v5
s_add_u32 s83,%9,159744
v_add_u32 v233,s83,v5
v_mov_b32 v240,0
v_mov_b32 v241,0
v_mov_b32 v242,0
v_mov_b32 v243,0
ds_write_b128 v233,v[240:243]
v_mov_b32 v248,0x3f803f80
v_mov_b32 v249,0x3f803f80
v_mov_b32 v250,0x3f803f80
v_mov_b32 v251,0x3f803f80
s_cmp_le_u32 s89,1
s_cselect_b64 s[24:25],s[68:69],%2
v_add_u32 v0,128,v0
s_add_u32 m0,s77,139264
s_nop 0
LD 0,24,49,2048
s_add_u32 m0,s77,143360
s_nop 0
LD 0,24,51,2048
s_add_u32 m0,s77,147456
s_nop 0
LD 0,24,53,2048
s_add_u32 m0,s77,151552
s_nop 0
LD 0,24,55,2048
s_add_u32 m0,s77,155648
s_nop 0
LD 0,24,57,2048
s_add_u32 m0,s76,40960
s_cmp_le_u32 s89,1
s_cselect_b64 s[28:29],s[70:71],%3
v_add_u32 v3,262144,v3
LD 3,28,64,0
LD 3,28,65,1024
LD 3,28,66,2048
LD 3,28,67,3072
s_branch .Lmha_r1
.Lmha_p0:
v_mbcnt_lo_u32_b32 v228,-1,0
v_mbcnt_hi_u32_b32 v228,-1,v228
s_and_b32 s87,%8,1
s_mov_b64 s[24:25],%2
s_and_b32 s25,s25,0xffff
s_mov_b32 s26,184320
s_mov_b32 s27,0x20000
s_mov_b64 s[28:29],%3
s_and_b32 s29,s29,0xffff
s_mov_b32 s30,4719616
s_mov_b32 s31,0x20000
s_and_b32 s80,%12,0x700
s_lshl_b32 s80,s80,2
s_sub_u32 s30,s30,s80
s_lshl_b32 s80,s87,12
s_add_u32 s76,s80,%9
s_lshl_b32 s80,s87,10
s_add_u32 s77,s80,%9
s_mul_i32 s48,s87,9216
s_add_u32 s49,s48,16384
s_add_u32 s50,s48,36864
s_add_u32 s51,s48,53248
s_add_u32 s52,s48,73728
s_add_u32 s53,s48,90112
s_add_u32 s54,s48,110592
s_add_u32 s55,s48,126976
s_add_u32 s56,s48,147456
s_add_u32 s57,s48,163840
s_lshl_b32 s82,s87,16
s_add_u32 s64,s82,1024
s_add_u32 s65,s82,128
s_add_u32 s66,s82,130048
s_add_u32 s67,s82,129152
v_lshrrev_b32 v233,3,v228
v_lshrrev_b32 v230,4,v228
s_lshl_b32 s80,s87,2
v_or_b32 v234,s80,v230
v_and_b32 v235,7,v228
v_xor_b32 v234,v234,v235
v_lshlrev_b32 v234,4,v234
v_mul_u32_u24 v235,1152,v233
v_add_u32 v0,v235,v234
v_lshl_add_u32 v1,v233,8,v234
v_and_b32 v232,3,v228
v_bfe_u32 v237,v228,2,3
v_bfe_u32 v234,v228,4,1
v_lshlrev_b32 v234,1,v234
v_xor_b32 v234,v234,v232
v_lshlrev_b32 v234,4,v234
v_lshrrev_b32 v238,5,v228
v_lshl_add_u32 v238,v238,6,v234
s_lshl_b32 s81,s87,5
v_xor_b32 v236,s81,v238
v_lshl_add_u32 v3,v237,13,v236
s_add_u32 m0,s76,65536
s_nop 0
LD 3,28,64,0
LD 3,28,65,1024
LD 3,28,66,2048
LD 3,28,67,3072
s_add_u32 m0,s77,98304
s_nop 0
LD 0,24,48,0
s_add_u32 m0,s77,102400
s_nop 0
LD 0,24,50,0
s_add_u32 m0,s77,106496
s_nop 0
LD 0,24,52,0
s_add_u32 m0,s77,110592
s_nop 0
LD 0,24,54,0
s_add_u32 m0,s77,114688
s_nop 0
LD 0,24,56,0
s_add_u32 m0,s76,81920
v_add_u32 v3,262144,v3
LD 3,28,64,0
LD 3,28,65,1024
LD 3,28,66,2048
LD 3,28,67,3072
v_lshlrev_b32 v5,4,v228
s_bfe_u32 s88,%12,0x4000b
s_sub_u32 s88,16,s88
s_bfe_u32 s89,%12,0x40018
s_bfe_u32 s90,%12,0x4001c
s_mov_b64 s[68:69],%2
s_sub_u32 s68,s68,1152
s_subb_u32 s69,s69,0
s_mov_b64 s[70:71],%3
s_sub_u32 s70,s70,4718592
s_subb_u32 s71,s71,0
v_lshl_add_u32 v2,v237,8,v238
v_mul_u32_u24 v236,1152,v237
v_add_u32 v4,v236,v238
s_mov_b64 s[36:37],%4
s_and_b32 s37,s37,0xffff
s_mov_b32 s38,656384
s_mov_b32 s39,0x20000
s_mov_b64 s[96:97],%5
s_and_b32 s97,s97,0xffff
s_mov_b32 s98,656384
s_mov_b32 s99,0x20000
s_mov_b64 s[40:41],%6
s_and_b32 s41,s41,0xffff
s_mov_b32 s42,589824
s_mov_b32 s43,0x20000
s_mov_b64 s[44:45],%7
s_and_b32 s45,s45,0xffff
s_mov_b32 s46,184320
s_mov_b32 s47,0x20000
s_lshl_b32 s80,%8,13
s_add_u32 s78,s80,%9
s_lshl_b32 s82,s87,11
s_add_u32 s58,s82,1024
s_add_u32 s59,s82,128
s_add_u32 s60,s82,3072
s_add_u32 s61,s82,2176
s_lshl_b32 s82,s87,12
s_add_u32 s62,s82,1024
s_add_u32 s63,s82,128
s_mul_i32 s79,%8,5120
s_add_u32 s84,s79,4096
s_mov_b32 s85,0x3e0293ee
v_and_b32 v229,15,v228
v_bfe_u32 v231,v228,2,2
v_lshrrev_b32 v233,1,v229
v_xor_b32 v233,v233,v230
v_and_b32 v234,7,v229
v_lshlrev_b32 v234,7,v234
v_lshl_add_u32 v233,v233,4,v234
v_lshrrev_b32 v234,3,v229
s_add_u32 s81,%9,0
v_lshl_add_u32 v235,v234,12,v233
v_add_u32 v6,s81,v235
v_xor_b32 v235,64,v235
v_add_u32 v7,s81,v235
s_mul_i32 s82,s87,10240
s_add_u32 s82,s82,%9
s_add_u32 s82,s82,98304
v_lshl_add_u32 v235,v234,10,v233
v_add_u32 v11,s82,v235
v_xor_b32 v235,64,v235
v_add_u32 v12,s82,v235
v_and_b32 v233,1,v230
v_lshlrev_b32 v234,6,v231
v_lshl_add_u32 v234,v233,5,v234
v_lshl_add_u32 v234,v232,3,v234
v_lshrrev_b32 v235,1,v230
s_add_u32 s83,s81,32768
v_lshlrev_b32 v236,4,v232
v_lshlrev_b32 v237,5,v233
v_xor_b32 v236,v236,v237
v_lshl_add_u32 v236,v231,6,v236
v_lshl_add_u32 v236,v235,11,v236
v_lshl_add_u32 v236,v233,12,v236
v_add_u32 v9,s83,v236
v_xor_b32 v236,32,v236
v_add_u32 v10,s83,v236
s_add_u32 s83,s81,49152
v_lshl_add_u32 v234,v233,8,v234
v_lshl_add_u32 v236,v235,11,v234
v_add_u32 v8,s83,v236
v_xor_b32 v236,32,v236
v_add_u32 v252,s83,v236
s_add_u32 s83,%9,0
v_lshl_add_u32 v236,v235,10,v234
v_add_u32 v13,s83,v236
v_xor_b32 v236,32,v236
v_add_u32 v14,s83,v236
v_add_u32 v253,0x10000,v13
v_add_u32 v254,0x10000,v14
s_mul_i32 s82,s87,10240
s_add_u32 s82,s82,%9
s_add_u32 s83,s82,65536
v_add_u32 v15,s83,v5
s_add_u32 s83,s82,86016
v_add_u32 v16,s83,v5
s_add_u32 s83,%9,159744
v_add_u32 v233,s83,v5
v_mov_b32 v240,0
v_mov_b32 v241,0
v_mov_b32 v242,0
v_mov_b32 v243,0
ds_write_b128 v233,v[240:243]
v_mov_b32 v248,0x3f803f80
v_mov_b32 v249,0x3f803f80
v_mov_b32 v250,0x3f803f80
v_mov_b32 v251,0x3f803f80
s_getpc_b64 s[92:93]
s_and_b32 s92,s92,0xffffff80
s_and_b32 s93,s93,0xffff
s_mov_b32 s94,.Lmha_end-.Lmha_b0
s_mov_b32 s95,0x20000
v_lshlrev_b32 v17,2,v5
s_bfe_u32 s86,%12,0x6000f
s_add_u32 s86,s86,s87
s_lshl_b32 s86,s86,12
buffer_load_dword v255,v17,s[92:95],s86 offen
s_mul_i32 s91,s87,.Lmha_r1-.Lmha_b0
s_cmp_le_u32 s89,1
s_cselect_b64 s[24:25],s[68:69],%2
v_add_u32 v0,128,v0
s_add_u32 m0,s77,139264
s_nop 0
LD 0,24,48,0
s_add_u32 m0,s77,143360
s_nop 0
LD 0,24,50,0
s_add_u32 m0,s77,147456
s_nop 0
LD 0,24,52,0
s_add_u32 m0,s77,151552
s_nop 0
LD 0,24,54,0
s_add_u32 m0,s77,155648
s_nop 0
LD 0,24,56,0
s_add_u32 m0,s76,32768
s_cmp_le_u32 s89,1
s_cselect_b64 s[28:29],s[70:71],%3
v_add_u32 v3,262144,v3
LD 3,28,64,0
LD 3,28,65,1024
LD 3,28,66,2048
LD 3,28,67,3072
s_branch .Lmha_b0
.p2align 12
.Lmha_b0:
s_waitcnt vmcnt(14)
s_barrier
Ta 240,9,32768
Ta 242,10,33024
Ta 244,9,32776
Ta 246,10,33032
Rv 180,11,0
Rv 184,11,2048
Rv 188,11,4096
Rv 192,11,6144
Rv 196,11,8192
WL 4
Zvav 20,240,180
WL 3
s_add_u32 m0,s76,49152
v_add_u32 v3,262144,v3
Zvav 24,240,184
WL 2
Zvav 28,240,188
WL 1
Ta 248,9,33280
Zvav 32,240,192
WL 1
Ta 250,10,33536
Ta 252,9,33288
Zvav 36,240,196
s_nop 0
Ta 254,10,33544
Rv 200,12,0
Zvav 100,244,180
Tv 220,9,33792
Zvav 104,244,184
s_nop 0
Tv 222,10,34048
Rv 204,12,2048
Zvav 108,244,188
Rv 208,12,4096
Zvav 112,244,192
Rv 212,12,6144
Zvav 116,244,196
WL 8
Rv 216,12,8192
Zvav 40,248,180
Tv 224,9,33800
Zvav 44,248,184
Tv 226,10,34056
Zvav 48,248,188
Zvav 52,248,192
Zvav 56,248,196
WL 5
Zvav 120,252,180
s_nop 0
Ta 240,9,34304
Ta 242,10,34560
Zvav 124,252,184
LD 3,28,64,0
Zvav 128,252,188
Zvav 132,252,192
Zvav 136,252,196
s_nop 0
Ta 244,9,34312
Ta 246,10,34568
Zvvv 60,220,180
LD 3,28,65,1024
Zvvv 64,220,184
Zvvv 68,220,188
Zvvv 72,220,192
Zvvv 76,220,196
WL 2
LD 3,28,66,2048
Zvvv 140,224,180
Zvvv 144,224,184
Zvvv 148,224,188
Zvvv 152,224,192
LD 3,28,67,3072
Zvvv 156,224,196
s_waitcnt vmcnt(8) lgkmcnt(0)
s_barrier
Zvav 80,240,180
s_nop 0
Ta 248,9,49152
Ta 250,10,49408
Zvav 84,240,184
s_nop 0
Ta 252,9,49160
Ta 254,10,49416
Zvav 88,240,188
s_cmp_le_u32 s89,2
s_cselect_b64 s[24:25],s[68:69],%2
v_add_u32 v0,128,v0
Zvav 92,240,192
s_add_u32 m0,s77,118784
Zvav 96,240,196
LD 0,24,48,0
Zvav 160,244,180
s_add_u32 m0,s77,122880
Tv 220,9,49664
Tv 222,10,49920
Zvav 164,244,184
Zvav 168,244,188
Zvav 172,244,192
LD 0,24,50,0
Zvav 176,244,196
WL 2
s_add_u32 m0,s77,126976
Rv 180,11,40960
Mvav 20,248,200
s_nop 0
Tv 224,9,49672
Tv 226,10,49928
Mvav 24,248,204
s_nop 0
Rv 184,11,43008
Rv 188,11,45056
Mvav 28,248,208
LD 0,24,52,0
Mvav 32,248,212
s_add_u32 m0,s77,131072
Rv 192,11,47104
Rv 196,11,49152
Mvav 36,248,216
Mvav 100,252,200
s_nop 0
Ta 240,9,50176
Ta 242,10,50432
Mvav 104,252,204
LD 0,24,54,0
Mvav 108,252,208
s_add_u32 m0,s77,135168
Mvav 112,252,212
Mvav 116,252,216
WL 4
Mvvv 40,220,200
s_nop 0
Ta 244,9,50184
Ta 246,10,50440
Mvvv 44,220,204
LD 0,24,56,0
Mvvv 48,220,208
s_add_u32 m0,s76,65536
s_cmp_le_u32 s89,2
s_cselect_b64 s[28:29],s[70:71],%3
Mvvv 52,220,212
v_add_u32 v3,262144,v3
Mvvv 56,220,216
Mvvv 120,224,200
s_nop 0
Ta 248,9,50688
Ta 250,10,50944
Mvvv 124,224,204
LD 3,28,64,0
Mvvv 128,224,208
Mvvv 132,224,212
Mvvv 136,224,216
WL 2
Ta 252,9,50696
Ta 254,10,50952
Mvav 60,240,200
LD 3,28,65,1024
Mvav 64,240,204
Mvav 68,240,208
Mvav 72,240,212
Mvav 76,240,216
LD 3,28,66,2048
Mvav 140,244,200
Mvav 144,244,204
Mvav 148,244,208
Mvav 152,244,212
LD 3,28,67,3072
Mvav 156,244,216
s_waitcnt vmcnt(13) lgkmcnt(0)
s_barrier
Mvav 80,248,200
s_nop 0
Tv 220,9,0
Tv 222,10,256
Mvav 84,248,204
s_nop 0
Tv 224,9,8
Tv 226,10,264
Mvav 88,248,208
s_cmp_le_u32 s89,3
s_cselect_b64 s[24:25],s[68:69],%2
v_add_u32 v0,128,v0
Mvav 92,248,212
s_add_u32 m0,s77,98304
Mvav 96,248,216
LD 0,24,48,0
Mvav 160,252,200
s_add_u32 m0,s77,102400
Ta 240,9,512
Ta 242,10,768
Mvav 164,252,204
s_nop 0
Mvav 168,252,208
Mvav 172,252,212
LD 0,24,50,0
Mvav 176,252,216
WL 2
s_add_u32 m0,s77,106496
Rv 200,12,40960
Mvvv 20,220,180
s_nop 0
Ta 244,9,520
Ta 246,10,776
Mvvv 24,220,184
s_nop 0
Rv 204,12,43008
Rv 208,12,45056
Mvvv 28,220,188
LD 0,24,52,0
Mvvv 32,220,192
s_add_u32 m0,s77,110592
Rv 212,12,47104
Rv 216,12,49152
Mvvv 36,220,196
Mvvv 100,224,180
s_nop 0
Ta 248,9,1024
Ta 250,10,1280
Mvvv 104,224,184
LD 0,24,54,0
Mvvv 108,224,188
s_add_u32 m0,s77,114688
Mvvv 112,224,192
Mvvv 116,224,196
WL 4
Mvav 40,240,180
s_nop 0
Ta 252,9,1032
Ta 254,10,1288
Mvav 44,240,184
LD 0,24,56,0
Mvav 48,240,188
s_add_u32 m0,s76,0
v_add_u32 v1,131072,v1
Mvav 52,240,192
Mvav 56,240,196
Mvav 120,244,180
s_nop 0
Tv 220,9,1536
Tv 222,10,1792
Mvav 124,244,184
LN 1,36,58,0
Mvav 128,244,188
Mvav 132,244,192
Mvav 136,244,196
WL 2
LN 1,36,59,1024
LN 1,36,60,2048
LN 1,36,61,3072
Mvav 60,248,180
s_add_u32 m0,s76,81920
Tv 224,9,1544
Tv 226,10,1800
Mvav 64,248,184
v_add_u32 v3,262144,v3
Mvav 68,248,188
Mvav 72,248,192
Mvav 76,248,196
Mvav 140,252,180
Mvav 144,252,184
Mvav 148,252,188
Mvav 152,252,192
Mvav 156,252,196
s_waitcnt vmcnt(13) lgkmcnt(0)
s_barrier
Mvvv 80,220,180
s_nop 0
Ta 240,9,16384
Ta 242,10,16640
Mvvv 84,220,184
s_nop 0
Ta 244,9,16392
Ta 246,10,16648
Mvvv 88,220,188
LD 3,28,64,0
Mvvv 92,220,192
Mvvv 96,220,196
Mvvv 160,224,180
s_nop 0
Ta 248,9,16896
Ta 250,10,17152
Mvvv 164,224,184
LD 3,28,65,1024
Mvvv 168,224,188
Mvvv 172,224,192
Mvvv 176,224,196
WL 2
Rv 180,11,20480
Rv 184,11,22528
Mvav 20,240,200
Ta 252,9,16904
Mvav 24,240,204
s_nop 0
Ta 254,10,17160
Rv 188,11,24576
Mvav 28,240,208
LD 3,28,66,2048
Mvav 32,240,212
s_nop 0
Rv 192,11,26624
Rv 196,11,28672
Mvav 36,240,216
Mvav 100,244,200
s_nop 0
Tv 220,9,17408
Tv 222,10,17664
Mvav 104,244,204
LD 3,28,67,3072
Mvav 108,244,208
s_add_u32 m0,s76,32768
s_cmp_le_u32 s89,3
s_cselect_b64 s[28:29],s[70:71],%3
Mvav 112,244,212
v_add_u32 v3,262144,v3
Mvav 116,244,216
WL 4
Mvav 40,248,200
s_nop 0
Tv 224,9,17416
Tv 226,10,17672
Mvav 44,248,204
LD 3,28,64,0
Mvav 48,248,208
Mvav 52,248,212
Mvav 56,248,216
Mvav 120,252,200
s_nop 0
Ta 240,9,17920
Ta 242,10,18176
Mvav 124,252,204
LD 3,28,65,1024
Mvav 128,252,208
Mvav 132,252,212
Mvav 136,252,216
WL 2
Ta 244,9,17928
Ta 246,10,18184
Mvvv 60,220,200
LD 3,28,66,2048
Mvvv 64,220,204
Mvvv 68,220,208
Mvvv 72,220,212
Mvvv 76,220,216
LD 3,28,67,3072
Mvvv 140,224,200
s_cmp_le_u32 s89,4
s_cselect_b64 s[24:25],s[68:69],%2
v_add_u32 v0,128,v0
Mvvv 144,224,204
s_add_u32 m0,s77,139264
Mvvv 148,224,208
Mvvv 152,224,212
LD 0,24,48,0
Mvvv 156,224,216
s_waitcnt vmcnt(18) lgkmcnt(0)
s_barrier
Mvav 80,240,200
s_add_u32 m0,s77,143360
Ta 248,9,32768
Ta 250,10,33024
Mvav 84,240,204
s_nop 0
Ta 252,9,32776
Ta 254,10,33032
Mvav 88,240,208
LD 0,24,50,0
Mvav 92,240,212
s_add_u32 m0,s77,147456
Mvav 96,240,216
Mvav 160,244,200
s_nop 0
Tv 220,9,33280
Tv 222,10,33536
Mvav 164,244,204
LD 0,24,52,0
Mvav 168,244,208
s_add_u32 m0,s77,151552
Mvav 172,244,212
Mvav 176,244,216
WL 2
Rv 200,12,20480
Rv 204,12,22528
Mvav 20,248,180
Tv 224,9,33288
Mvav 24,248,184
s_nop 0
Tv 226,10,33544
Rv 208,12,24576
Mvav 28,248,188
LD 0,24,54,0
Mvav 32,248,192
s_add_u32 m0,s77,155648
Rv 212,12,26624
Rv 216,12,28672
Mvav 36,248,196
Mvav 100,252,180
s_nop 0
Ta 240,9,33792
Ta 242,10,34048
Mvav 104,252,184
LD 0,24,56,0
Mvav 108,252,188
v_mov_b32 v238,0
Mvav 112,252,192
Mvav 116,252,196
WL 4
Mvvv 40,220,180
s_nop 0
Ta 244,9,33800
Ta 246,10,34056
Mvvv 44,220,184
global_load_dwordx2 v[240:241],v238,%11 sc1
Mvvv 48,220,188
s_add_u32 m0,s76,49152
v_add_u32 v3,262144,v3
Mvvv 52,220,192
Mvvv 56,220,196
Mvvv 120,224,180
s_nop 0
Ta 248,9,34304
Ta 250,10,34560
Mvvv 124,224,184
Mvvv 128,224,188
Mvvv 132,224,192
Mvvv 136,224,196
WL 2
Ta 252,9,34312
Ta 254,10,34568
Mvav 60,240,180
LD 3,28,64,0
Mvav 64,240,184
Mvav 68,240,188
Mvav 72,240,192
Mvav 76,240,196
LD 3,28,65,1024
Mvav 140,244,180
Mvav 144,244,184
Mvav 148,244,188
Mvav 152,244,192
LD 3,28,66,2048
Mvav 156,244,196
s_waitcnt vmcnt(13) lgkmcnt(0)
s_barrier
Mvav 80,248,180
s_nop 0
Tv 220,9,49152
Tv 222,10,49408
Mvav 84,248,184
s_nop 0
Tv 224,9,49160
Tv 226,10,49416
Mvav 88,248,188
LD 3,28,67,3072
Mvav 92,248,192
s_add_u32 m0,s76,16384
s_cmp_eq_u32 s88,1
s_cselect_b32 s86,0x20000,0
Mvav 96,248,196
v_subrev_u32 v1,s86,v1
v_add_u32 v1,8192,v1
Mvav 160,252,180
s_nop 0
Ta 240,9,49664
Ta 242,10,49920
Mvav 164,252,184
LN 1,36,58,0
Mvav 168,252,188
Mvav 172,252,192
Mvav 176,252,196
WL 2
Rv 180,11,0
Rv 184,11,2048
Mvvv 20,220,200
Ta 244,9,49672
Mvvv 24,220,204
s_nop 0
Ta 246,10,49928
Rv 188,11,4096
Mvvv 28,220,208
LN 1,36,59,1024
Mvvv 32,220,212
s_nop 0
Rv 192,11,6144
Rv 196,11,8192
Mvvv 36,220,216
Mvvv 100,224,200
s_nop 0
Ta 248,9,50176
Ta 250,10,50432
Mvvv 104,224,204
LN 1,36,60,2048
Mvvv 108,224,208
Mvvv 112,224,212
Mvvv 116,224,216
WL 4
Mvav 40,240,200
s_nop 0
Ta 252,9,50184
Ta 254,10,50440
Mvav 44,240,204
LN 1,36,61,3072
Mvav 48,240,208
s_and_b32 s86,%12,458752
s_cmp_eq_u32 s86,196608
s_cselect_b32 s86,s91,0x40000000
Mvav 52,240,212
s_add_u32 s86,s86,12288
Mvav 56,240,216
Mvav 120,244,200
s_nop 0
Tv 220,9,50688
Tv 222,10,50944
Mvav 124,244,204
buffer_load_dword v255,v17,s[92:95],s86 offen
Mvav 128,244,208
s_add_u32 m0,s76,65536
s_cmp_le_u32 s89,4
s_cselect_b64 s[28:29],s[70:71],%3
Mvav 132,244,212
v_add_u32 v3,262144,v3
Mvav 136,244,216
WL 2
Tv 224,9,50696
Tv 226,10,50952
Mvav 60,248,200
Mvav 64,248,204
Mvav 68,248,208
Mvav 72,248,212
Mvav 76,248,216
LD 3,28,64,0
Mvav 140,252,200
Mvav 144,252,204
Mvav 148,252,208
Mvav 152,252,212
LD 3,28,65,1024
Mvav 156,252,216
s_waitcnt vmcnt(17) lgkmcnt(0)
s_barrier
Mvvv 80,220,200
s_nop 0
Ta 240,9,0
Ta 242,10,256
Mvvv 84,220,204
s_nop 0
Ta 244,9,8
Ta 246,10,264
Mvvv 88,220,208
LD 3,28,66,2048
Mvvv 92,220,212
Mvvv 96,220,216
Mvvv 160,224,200
s_nop 0
Ta 248,9,512
Ta 250,10,768
Mvvv 164,224,204
LD 3,28,67,3072
Mvvv 168,224,208
s_cmp_le_u32 s89,5
s_cselect_b64 s[24:25],s[68:69],%2
v_add_u32 v0,128,v0
Mvvv 172,224,212
s_add_u32 m0,s77,118784
Mvvv 176,224,216
WL 2
Rv 200,12,0
Rv 204,12,2048
Mvav 20,240,180
Ta 252,9,520
Mvav 24,240,184
s_nop 0
Ta 254,10,776
Rv 208,12,4096
Mvav 28,240,188
LD 0,24,48,0
Mvav 32,240,192
s_add_u32 m0,s77,122880
Rv 212,12,6144
Rv 216,12,8192
Mvav 36,240,196
Mvav 100,244,180
s_nop 0
Tv 220,9,1024
Tv 222,10,1280
Mvav 104,244,184
LD 0,24,50,0
Mvav 108,244,188
s_add_u32 m0,s77,126976
Mvav 112,244,192
Mvav 116,244,196
WL 4
Mvav 40,248,180
s_nop 0
Tv 224,9,1032
Tv 226,10,1288
Mvav 44,248,184
LD 0,24,52,0
Mvav 48,248,188
s_add_u32 m0,s77,131072
Mvav 52,248,192
Mvav 56,248,196
Mvav 120,252,180
s_nop 0
Ta 240,9,1536
Ta 242,10,1792
Mvav 124,252,184
LD 0,24,54,0
Mvav 128,252,188
s_add_u32 m0,s77,135168
Mvav 132,252,192
Mvav 136,252,196
WL 2
LD 0,24,56,0
Mvvv 60,220,180
s_add_u32 m0,s76,81920
Ta 244,9,1544
Ta 246,10,1800
Mvvv 64,220,184
v_add_u32 v3,262144,v3
Mvvv 68,220,188
Mvvv 72,220,192
Mvvv 76,220,196
LD 3,28,64,0
Mvvv 140,224,180
Mvvv 144,224,184
Mvvv 148,224,188
Mvvv 152,224,192
LD 3,28,65,1024
Mvvv 156,224,196
s_waitcnt vmcnt(16) lgkmcnt(0)
s_barrier
Mvav 80,240,180
s_nop 0
Ta 248,9,16384
Ta 250,10,16640
Mvav 84,240,184
s_nop 0
Ta 252,9,16392
Ta 254,10,16648
Mvav 88,240,188
LD 3,28,66,2048
Mvav 92,240,192
s_nop 0
Mvav 96,240,196
Mvav 160,244,180
s_nop 0
Tv 220,9,16896
Tv 222,10,17152
Mvav 164,244,184
LD 3,28,67,3072
Mvav 168,244,188
s_add_u32 m0,s76,32768
s_cmp_le_u32 s89,5
s_cselect_b64 s[28:29],s[70:71],%3
Mvav 172,244,192
s_nop 0
v_add_u32 v3,262144,v3
Mvav 176,244,196
WL 2
Rv 180,11,40960
Rv 184,11,43008
Mvav 20,248,200
s_nop 0
Tv 224,9,16904
Mvav 24,248,204
s_nop 0
Tv 226,10,17160
Rv 188,11,45056
Mvav 28,248,208
LD 3,28,64,0
Mvav 32,248,212
s_nop 0
Rv 192,11,47104
Rv 196,11,49152
Mvav 36,248,216
Mvav 100,252,200
s_nop 0
Ta 240,9,17408
Ta 242,10,17664
Mvav 104,252,204
LD 3,28,65,1024
Mvav 108,252,208
Mvav 112,252,212
Mvav 116,252,216
WL 4
Mvvv 40,220,200
s_nop 0
Ta 244,9,17416
Ta 246,10,17672
Mvvv 44,220,204
LD 3,28,66,2048
Mvvv 48,220,208
Mvvv 52,220,212
Mvvv 56,220,216
Mvvv 120,224,200
s_nop 0
Ta 248,9,17920
Ta 250,10,18176
Mvvv 124,224,204
LD 3,28,67,3072
Mvvv 128,224,208
s_cmp_le_u32 s89,6
s_cselect_b64 s[24:25],s[68:69],%2
v_add_u32 v0,128,v0
Mvvv 132,224,212
s_add_u32 m0,s77,98304
Mvvv 136,224,216
WL 2
Ta 252,9,17928
Ta 254,10,18184
Mvav 60,240,200
LD 0,24,48,0
Mvav 64,240,204
s_add_u32 m0,s77,102400
Mvav 68,240,208
Mvav 72,240,212
Mvav 76,240,216
LD 0,24,50,0
Mvav 140,244,200
s_add_u32 m0,s77,106496
Mvav 144,244,204
Mvav 148,244,208
Mvav 152,244,212
LD 0,24,52,0
Mvav 156,244,216
s_waitcnt vmcnt(16) lgkmcnt(0)
s_barrier
Mvav 80,248,200
s_add_u32 m0,s77,110592
Tv 220,9,32768
Tv 222,10,33024
Mvav 84,248,204
s_nop 0
Tv 224,9,32776
Tv 226,10,33032
Mvav 88,248,208
LD 0,24,54,0
Mvav 92,248,212
s_add_u32 m0,s77,114688
Mvav 96,248,216
Mvav 160,252,200
s_nop 0
Ta 240,9,33280
Ta 242,10,33536
Mvav 164,252,204
LD 0,24,56,0
Mvav 168,252,208
s_add_u32 m0,s76,49152
v_add_u32 v3,262144,v3
Mvav 172,252,212
Mvav 176,252,216
WL 2
Rv 200,12,40960
Rv 204,12,43008
Mvvv 20,220,180
Ta 244,9,33288
Mvvv 24,220,184
s_nop 0
Ta 246,10,33544
Rv 208,12,45056
Mvvv 28,220,188
LD 3,28,64,0
Mvvv 32,220,192
s_nop 0
Rv 212,12,47104
Rv 216,12,49152
Mvvv 36,220,196
Mvvv 100,224,180
s_nop 0
Ta 248,9,33792
Ta 250,10,34048
Mvvv 104,224,184
LD 3,28,65,1024
Mvvv 108,224,188
Mvvv 112,224,192
Mvvv 116,224,196
WL 4
Mvav 40,240,180
s_nop 0
Ta 252,9,33800
Ta 254,10,34056
Mvav 44,240,184
LD 3,28,66,2048
Mvav 48,240,188
Mvav 52,240,192
Mvav 56,240,196
Mvav 120,244,180
s_nop 0
Tv 220,9,34304
Tv 222,10,34560
Mvav 124,244,184
LD 3,28,67,3072
Mvav 128,244,188
Mvav 132,244,192
Mvav 136,244,196
WL 2
Tv 224,9,34312
Tv 226,10,34568
Mvav 60,248,180
Mvav 64,248,184
Mvav 68,248,188
Mvav 72,248,192
Mvav 76,248,196
Mvav 140,252,180
Mvav 144,252,184
Mvav 148,252,188
Mvav 152,252,192
Mvav 156,252,196
s_waitcnt vmcnt(13) lgkmcnt(0)
s_barrier
Mvvv 80,220,180
s_add_u32 m0,s76,65536
Ta 240,9,49152
Ta 242,10,49408
Mvvv 84,220,184
s_nop 0
Ta 244,9,49160
Ta 246,10,49416
Mvvv 88,220,188
s_cmp_le_u32 s89,6
s_cselect_b64 s[28:29],s[70:71],%3
v_add_u32 v3,262144,v3
Mvvv 92,220,192
LD 3,28,64,0
Mvvv 96,220,196
Mvvv 160,224,180
s_nop 0
Ta 248,9,49664
Ta 250,10,49920
Mvvv 164,224,184
Mvvv 168,224,188
LD 3,28,65,1024
Mvvv 172,224,192
Mvvv 176,224,196
WL 2
Rv 180,11,20480
Rv 184,11,22528
Mvav 20,240,200
Ta 252,9,49672
Mvav 24,240,204
s_nop 0
Ta 254,10,49928
Rv 188,11,24576
Mvav 28,240,208
LD 3,28,66,2048
Mvav 32,240,212
s_nop 0
Rv 192,11,26624
Rv 196,11,28672
Mvav 36,240,216
Mvav 100,244,200
s_nop 0
Tv 220,9,50176
Tv 222,10,50432
Mvav 104,244,204
LD 3,28,67,3072
Mvav 108,244,208
s_cmp_le_u32 s89,7
s_cselect_b64 s[24:25],s[68:69],%2
v_add_u32 v0,128,v0
Mvav 112,244,212
s_add_u32 m0,s77,139264
Mvav 116,244,216
WL 4
Mvav 40,248,200
s_nop 0
Tv 224,9,50184
Tv 226,10,50440
Mvav 44,248,204
LD 0,24,48,0
Mvav 48,248,208
s_add_u32 m0,s77,143360
Mvav 52,248,212
Mvav 56,248,216
Mvav 120,252,200
s_nop 0
Ta 240,9,50688
Ta 242,10,50944
Mvav 124,252,204
LD 0,24,50,0
Mvav 128,252,208
s_add_u32 m0,s77,147456
Mvav 132,252,212
Mvav 136,252,216
WL 2
Ta 244,9,50696
Ta 246,10,50952
Mvvv 60,220,200
LD 0,24,52,0
Mvvv 64,220,204
s_add_u32 m0,s77,151552
Mvvv 68,220,208
Mvvv 72,220,212
Mvvv 76,220,216
LD 0,24,54,0
Mvvv 140,224,200
s_add_u32 m0,s77,155648
Mvvv 144,224,204
Mvvv 148,224,208
Mvvv 152,224,212
LD 0,24,56,0
Mvvv 156,224,216
s_waitcnt vmcnt(18) lgkmcnt(0)
s_barrier
Mvav 80,240,200
s_add_u32 m0,s76,81920
Ta 248,9,0
Ta 250,10,256
Mvav 84,240,204
s_nop 0
Ta 252,9,8
Ta 254,10,264
Mvav 88,240,208
v_add_u32 v3,262144,v3
Mvav 92,240,212
LD 3,28,64,0
Mvav 96,240,216
Mvav 160,244,200
s_nop 0
Tv 220,9,512
Tv 222,10,768
Mvav 164,244,204
Mvav 168,244,208
LD 3,28,65,1024
Mvav 172,244,212
Mvav 176,244,216
WL 2
Rv 200,12,20480
Rv 204,12,22528
Mvav 20,248,180
Tv 224,9,520
Mvav 24,248,184
s_nop 0
Tv 226,10,776
Rv 208,12,24576
Mvav 28,248,188
LD 3,28,66,2048
Mvav 32,248,192
s_nop 0
Rv 212,12,26624
Rv 216,12,28672
Mvav 36,248,196
Mvav 100,252,180
s_nop 0
Ta 240,9,1024
Ta 242,10,1280
Mvav 104,252,184
LD 3,28,67,3072
Mvav 108,252,188
s_and_b32 s86,%12,458752
s_cmp_eq_u32 s86,262144
s_cselect_b32 s86,s91,0x40000000
Mvav 112,252,192
s_add_u32 s86,s86,16384
Mvav 116,252,196
WL 4
Mvvv 40,220,180
s_nop 0
Ta 244,9,1032
Ta 246,10,1288
Mvvv 44,220,184
buffer_load_dword v255,v17,s[92:95],s86 offen
Mvvv 48,220,188
Mvvv 52,220,192
Mvvv 56,220,196
Mvvv 120,224,180
s_nop 0
Ta 248,9,1536
Ta 250,10,1792
Mvvv 124,224,184
Mvvv 128,224,188
Mvvv 132,224,192
Mvvv 136,224,196
WL 2
Ta 252,9,1544
Ta 254,10,1800
Mvav 60,240,180
Mvav 64,240,184
Mvav 68,240,188
Mvav 72,240,192
Mvav 76,240,196
Mvav 140,244,180
Mvav 144,244,184
Mvav 148,244,188
Mvav 152,244,192
Mvav 156,244,196
s_waitcnt vmcnt(14) lgkmcnt(0)
s_barrier
Mvav 80,248,180
s_add_u32 m0,s76,32768
Tv 220,9,16384
Tv 222,10,16640
Mvav 84,248,184
s_nop 0
Tv 224,9,16392
Tv 226,10,16648
Mvav 88,248,188
s_cmp_le_u32 s89,7
s_cselect_b64 s[28:29],s[70:71],%3
v_add_u32 v3,262144,v3
Mvav 92,248,192
LD 3,28,64,0
Mvav 96,248,196
Mvav 160,252,180
s_nop 0
Ta 240,9,16896
Ta 242,10,17152
Mvav 164,252,184
Mvav 168,252,188
LD 3,28,65,1024
Mvav 172,252,192
Mvav 176,252,196
WL 2
Rv 180,11,0
Rv 184,11,2048
Mvvv 20,220,200
Ta 244,9,16904
Mvvv 24,220,204
s_nop 0
Ta 246,10,17160
Rv 188,11,4096
Mvvv 28,220,208
LD 3,28,66,2048
Mvvv 32,220,212
s_nop 0
Rv 192,11,6144
Rv 196,11,8192
Mvvv 36,220,216
Mvvv 100,224,200
s_nop 0
Ta 248,9,17408
Ta 250,10,17664
Mvvv 104,224,204
LD 3,28,67,3072
Mvvv 108,224,208
s_cmp_le_u32 s89,8
s_cselect_b64 s[24:25],s[68:69],%2
v_add_u32 v0,128,v0
Mvvv 112,224,212
s_add_u32 m0,s77,118784
Mvvv 116,224,216
WL 4
Mvav 40,240,200
s_nop 0
Ta 252,9,17416
Ta 254,10,17672
Mvav 44,240,204
LD 0,24,48,0
Mvav 48,240,208
s_add_u32 m0,s77,122880
Mvav 52,240,212
Mvav 56,240,216
Mvav 120,244,200
s_nop 0
Tv 220,9,17920
Tv 222,10,18176
Mvav 124,244,204
LD 0,24,50,0
Mvav 128,244,208
s_add_u32 m0,s77,126976
Mvav 132,244,212
Mvav 136,244,216
WL 2
Tv 224,9,17928
Tv 226,10,18184
Mvav 60,248,200
LD 0,24,52,0
Mvav 64,248,204
s_add_u32 m0,s77,131072
Mvav 68,248,208
Mvav 72,248,212
Mvav 76,248,216
LD 0,24,54,0
Mvav 140,252,200
s_add_u32 m0,s77,135168
Mvav 144,252,204
Mvav 148,252,208
Mvav 152,252,212
LD 0,24,56,0
Mvav 156,252,216
s_waitcnt vmcnt(19) lgkmcnt(0)
s_barrier
Mvvv 80,220,200
s_add_u32 m0,s76,49152
Ta 240,9,32768
Ta 242,10,33024
Mvvv 84,220,204
s_nop 0
Ta 244,9,32776
Ta 246,10,33032
Mvvv 88,220,208
v_add_u32 v3,262144,v3
Mvvv 92,220,212
LD 3,28,64,0
Mvvv 96,220,216
Mvvv 160,224,200
s_nop 0
Ta 248,9,33280
Ta 250,10,33536
Mvvv 164,224,204
Mvvv 168,224,208
LD 3,28,65,1024
Mvvv 172,224,212
Mvvv 176,224,216
WL 2
Rv 200,12,0
Rv 204,12,2048
Mvav 20,240,180
Ta 252,9,33288
Mvav 24,240,184
s_nop 0
Ta 254,10,33544
Rv 208,12,4096
Mvav 28,240,188
LD 3,28,66,2048
Mvav 32,240,192
s_nop 0
Rv 212,12,6144
Rv 216,12,8192
Mvav 36,240,196
Mvav 100,244,180
s_nop 0
Tv 220,9,33792
Tv 222,10,34048
Mvav 104,244,184
LD 3,28,67,3072
Mvav 108,244,188
Mvav 112,244,192
Mvav 116,244,196
WL 4
Mvav 40,248,180
s_nop 0
Tv 224,9,33800
Tv 226,10,34056
Mvav 44,248,184
Mvav 48,248,188
Mvav 52,248,192
Mvav 56,248,196
Mvav 120,252,180
s_nop 0
Ta 240,9,34304
Ta 242,10,34560
Mvav 124,252,184
Mvav 128,252,188
Mvav 132,252,192
Mvav 136,252,196
WL 2
Ta 244,9,34312
Ta 246,10,34568
Mvvv 60,220,180
Mvvv 64,220,184
Mvvv 68,220,188
Mvvv 72,220,192
Mvvv 76,220,196
Mvvv 140,224,180
Mvvv 144,224,184
Mvvv 148,224,188
Mvvv 152,224,192
Mvvv 156,224,196
s_waitcnt vmcnt(14) lgkmcnt(0)
s_barrier
Mvav 80,240,180
s_add_u32 m0,s76,65536
Ta 248,9,49152
Ta 250,10,49408
Mvav 84,240,184
s_nop 0
Ta 252,9,49160
Ta 254,10,49416
Mvav 88,240,188
s_cmp_le_u32 s89,8
s_cselect_b64 s[28:29],s[70:71],%3
v_add_u32 v3,262144,v3
Mvav 92,240,192
LD 3,28,64,0
Mvav 96,240,196
Mvav 160,244,180
s_nop 0
Tv 220,9,49664
Tv 222,10,49920
Mvav 164,244,184
Mvav 168,244,188
LD 3,28,65,1024
Mvav 172,244,192
Mvav 176,244,196
WL 2
Rv 180,11,40960
Rv 184,11,43008
Mvav 20,248,200
Tv 224,9,49672
Mvav 24,248,204
s_nop 0
Tv 226,10,49928
Rv 188,11,45056
Mvav 28,248,208
LD 3,28,66,2048
Mvav 32,248,212
s_nop 0
Rv 192,11,47104
Rv 196,11,49152
Mvav 36,248,216
Mvav 100,252,200
s_nop 0
Ta 240,9,50176
Ta 242,10,50432
Mvav 104,252,204
LD 3,28,67,3072
Mvav 108,252,208
s_add_u32 m0,s76,98304
v_add_u32 v2,131072,v2
Mvav 112,252,212
Mvav 116,252,216
WL 4
Mvvv 40,220,200
s_nop 0
Ta 244,9,50184
Ta 246,10,50440
Mvvv 44,220,204
LN 2,96,62,0
Mvvv 48,220,208
Mvvv 52,220,212
Mvvv 56,220,216
Mvvv 120,224,200
s_nop 0
Ta 248,9,50688
Ta 250,10,50944
Mvvv 124,224,204
LN 2,96,63,1024
Mvvv 128,224,208
Mvvv 132,224,212
Mvvv 136,224,216
WL 2
Ta 252,9,50696
Ta 254,10,50952
Mvav 60,240,200
LN 2,96,62,2048
Mvav 64,240,204
Mvav 68,240,208
Mvav 72,240,212
Mvav 76,240,216
LN 2,96,63,3072
Mvav 140,244,200
s_cmp_le_u32 s89,0
s_cselect_b64 s[28:29],s[70:71],%3
v_lshrrev_b32 v17,5,v5
Mvav 144,244,204
v_mul_u32_u24 v17,7680,v17
v_lshl_add_u32 v17,v5,4,v17
Mvav 148,244,208
s_lshl_b32 s86,s87,18
s_add_u32 s86,s86,1664
Mvav 152,244,212
buffer_load_dword v255,v17,s[28:31],s86 offen
Mvav 156,244,216
s_waitcnt vmcnt(18) lgkmcnt(0)
s_barrier
Mvav 80,248,200
s_nop 0
Tv 220,9,0
Tv 222,10,256
Mvav 84,248,204
s_nop 0
Tv 224,9,8
Tv 226,10,264
Mvav 88,248,208
v_lshlrev_b32 v17,2,v5
Mvav 92,248,212
s_add_u32 m0,s76,81920
s_cmp_le_u32 s89,8
s_cselect_b64 s[28:29],s[70:71],%3
Mvav 96,248,216
v_add_u32 v3,262144,v3
Mvav 160,252,200
s_nop 0
Ta 240,9,512
Ta 242,10,768
Mvav 164,252,204
LD 3,28,64,0
Mvav 168,252,208
Mvav 172,252,212
Mvav 176,252,216
WL 2
Rv 200,12,40960
Rv 204,12,43008
Mvvv 20,220,180
Ta 244,9,520
Mvvv 24,220,184
s_nop 0
Ta 246,10,776
Rv 208,12,45056
Mvvv 28,220,188
LD 3,28,65,1024
Mvvv 32,220,192
s_nop 0
Rv 212,12,47104
Rv 216,12,49152
Mvvv 36,220,196
Mvvv 100,224,180
s_nop 0
Ta 248,9,1024
Ta 250,10,1280
Mvvv 104,224,184
LD 3,28,66,2048
Mvvv 108,224,188
Mvvv 112,224,192
Mvvv 116,224,196
WL 4
Mvav 40,240,180
s_nop 0
Ta 252,9,1032
Ta 254,10,1288
Mvav 44,240,184
LD 3,28,67,3072
Mvav 48,240,188
Mvav 52,240,192
Mvav 56,240,196
Mvav 120,244,180
s_nop 0
Tv 220,9,1536
Tv 222,10,1792
Mvav 124,244,184
Mvav 128,244,188
Mvav 132,244,192
Mvav 136,244,196
WL 2
Tv 224,9,1544
Tv 226,10,1800
Mvav 60,248,180
Mvav 64,248,184
Mvav 68,248,188
Mvav 72,248,192
Mvav 76,248,196
Mvav 140,252,180
Mvav 144,252,184
Mvav 148,252,188
Mvav 152,252,192
Mvav 156,252,196
s_waitcnt vmcnt(13) lgkmcnt(0)
s_barrier
Mvvv 80,220,180
s_add_u32 m0,s76,32768
Ta 240,9,16384
Ta 242,10,16640
Mvvv 84,220,184
s_nop 0
Ta 244,9,16392
Ta 246,10,16648
Mvvv 88,220,188
s_cmp_eq_u32 s88,2
s_cselect_b32 s86,0x20000,0
v_subrev_u32 v1,s86,v1
Mvvv 92,220,192
v_add_u32 v1,8192,v1
Mvvv 96,220,196
LN 1,36,58,0
Mvvv 160,224,180
s_nop 0
Ta 248,9,16896
Ta 250,10,17152
Mvvv 164,224,184
Mvvv 168,224,188
Mvvv 172,224,192
LN 1,36,59,1024
Mvvv 176,224,196
WL 2
Rv 180,11,20480
Rv 184,11,22528
Mvav 20,240,200
Ta 252,9,16904
Mvav 24,240,204
s_nop 0
Ta 254,10,17160
Rv 188,11,24576
Mvav 28,240,208
LN 1,36,60,2048
Mvav 32,240,212
s_nop 0
Rv 192,11,26624
Rv 196,11,28672
Mvav 36,240,216
Mvav 100,244,200
s_nop 0
Tv 220,9,17408
Tv 222,10,17664
Mvav 104,244,204
LN 1,36,61,3072
Mvav 108,244,208
Mvav 112,244,212
Mvav 116,244,216
WL 4
Mvav 40,248,200
s_nop 0
Tv 224,9,17416
Tv 226,10,17672
Mvav 44,248,204
Mvav 48,248,208
Mvav 52,248,212
Mvav 56,248,216
Mvav 120,252,200
s_nop 0
Ta 240,9,17920
Ta 242,10,18176
Mvav 124,252,204
Mvav 128,252,208
Mvav 132,252,212
Mvav 136,252,216
WL 2
Ta 244,9,17928
Ta 246,10,18184
Mvvv 60,220,200
Mvvv 64,220,204
Mvvv 68,220,208
Mvvv 72,220,212
Mvvv 76,220,216
Mvvv 140,224,200
Mvvv 144,224,204
Mvvv 148,224,208
Mvvv 152,224,212
Mvvv 156,224,216
s_waitcnt vmcnt(13) lgkmcnt(0)
s_barrier
Mvav 80,240,200
s_add_u32 m0,s76,49152
Ta 248,9,32768
Ta 250,10,33024
Mvav 84,240,204
s_nop 0
Ta 252,9,32776
Ta 254,10,33032
Mvav 88,240,208
v_add_u32 v3,512,v3
Mvav 92,240,212
LD 3,28,64,0
Mvav 96,240,216
Mvav 160,244,200
s_nop 0
Tv 220,9,33280
Tv 222,10,33536
Mvav 164,244,204
Mvav 168,244,208
LD 3,28,66,2048
Mvav 172,244,212
s_add_u32 m0,s76,50176
v_subrev_u32 v3,262144,v3
Mvav 176,244,216
WL 2
Rv 200,12,20480
Rv 204,12,22528
Mvav 20,248,180
Tv 224,9,33288
Mvav 24,248,184
s_nop 0
Tv 226,10,33544
Rv 208,12,24576
Mvav 28,248,188
LD 3,28,64,0
Mvav 32,248,192
s_nop 0
Rv 212,12,26624
Rv 216,12,28672
Mvav 36,248,196
Mvav 100,252,180
s_nop 0
Ta 240,9,33792
Ta 242,10,34048
Mvav 104,252,184
LD 3,28,66,2048
Mvav 108,252,188
Mvav 112,252,192
Mvav 116,252,196
WL 4
Mvvv 40,220,180
s_nop 0
Ta 244,9,33800
Ta 246,10,34056
Mvvv 44,220,184
Mvvv 48,220,188
Mvvv 52,220,192
Mvvv 56,220,196
Mvvv 120,224,180
s_nop 0
Ta 248,9,34304
Ta 250,10,34560
Mvvv 124,224,184
Mvvv 128,224,188
Mvvv 132,224,192
Mvvv 136,224,196
WL 2
Ta 252,9,34312
Ta 254,10,34568
Mvav 60,240,180
Mvav 64,240,184
Mvav 68,240,188
Mvav 72,240,192
Mvav 76,240,196
Mvav 140,244,180
Mvav 144,244,184
Mvav 148,244,188
Mvav 152,244,192
)ASM"
R"ASM(Mvav 156,244,196
s_waitcnt vmcnt(8) lgkmcnt(0)
s_barrier
Mvav 80,248,180
s_add_u32 m0,s76,65536
Tv 220,9,49152
Tv 222,10,49408
Mvav 84,248,184
s_nop 0
Tv 224,9,49160
Tv 226,10,49416
Mvav 88,248,188
s_cmp_le_u32 s89,7
s_cselect_b64 s[28:29],s[70:71],%3
v_subrev_u32 v3,262144,v3
Mvav 92,248,192
LD 3,28,64,0
Mvav 96,248,196
Mvav 160,252,180
s_nop 0
Ta 240,9,49664
Ta 242,10,49920
Mvav 164,252,184
Mvav 168,252,188
LD 3,28,66,2048
Mvav 172,252,192
s_add_u32 m0,s76,66560
v_subrev_u32 v3,262144,v3
Mvav 176,252,196
WL 2
Mvvv 20,220,200
s_nop 0
Ta 244,9,49672
Ta 246,10,49928
Mvvv 24,220,204
LD 3,28,64,0
Mvvv 28,220,208
Mvvv 32,220,212
Mvvv 36,220,216
Mvvv 100,224,200
s_nop 0
Ta 248,9,50176
Ta 250,10,50432
Mvvv 104,224,204
LD 3,28,66,2048
Mvvv 108,224,208
s_cmp_le_u32 s89,6
s_cselect_b64 s[24:25],s[68:69],%2
v_subrev_u32 v0,256,v0
Mvvv 112,224,212
s_add_u32 m0,s77,118784
Mvvv 116,224,216
WL 2
v_mul_f32 v20,s85,v20
v_mul_f32 v21,s85,v21
Mvav 40,240,200
s_nop 0
Ta 252,9,50184
Ta 254,10,50440
Mvav 44,240,204
LD 0,24,48,0
Mvav 48,240,208
s_add_u32 m0,s77,122880
v_mul_f32 v22,s85,v22
v_mul_f32 v23,s85,v23
Mvav 52,240,212
v_mul_f32 v100,s85,v100
v_mul_f32 v101,s85,v101
Mvav 56,240,216
v_mul_f32 v102,s85,v102
v_mul_f32 v103,s85,v103
Mvav 120,244,200
s_nop 0
Tv 220,9,50688
Tv 222,10,50944
Mvav 124,244,204
LD 0,24,50,0
Mvav 128,244,208
s_add_u32 m0,s77,126976
CV 20,20,21
CV 21,22,23
Mvav 132,244,212
CV 22,100,101
CV 23,102,103
Mvav 136,244,216
WL 2
Tv 224,9,50696
Tv 226,10,50952
Mvav 60,248,200
LD 0,24,52,0
Mvav 64,248,204
s_add_u32 m0,s77,131072
v_mul_f32 v24,s85,v24
v_mul_f32 v25,s85,v25
Mvav 68,248,208
v_mul_f32 v26,s85,v26
v_mul_f32 v27,s85,v27
Mvav 72,248,212
v_mul_f32 v104,s85,v104
v_mul_f32 v105,s85,v105
Mvav 76,248,216
LD 0,24,54,0
Mvav 140,252,200
s_add_u32 m0,s77,135168
v_mul_f32 v106,s85,v106
v_mul_f32 v107,s85,v107
Mvav 144,252,204
CV 24,24,25
CV 25,26,27
Mvav 148,252,208
CV 26,104,105
CV 27,106,107
Mvav 152,252,212
LD 0,24,56,0
Mvav 156,252,216
s_waitcnt vmcnt(9) lgkmcnt(0)
s_barrier
Mvvv 80,220,200
s_nop 0
Ta 240,9,16384
Ta 242,10,16640
Mvvv 84,220,204
s_nop 0
Ta 244,9,16392
Ta 246,10,16648
Mvvv 88,220,208
s_and_b32 s86,%12,458752
s_cmp_eq_u32 s86,327680
s_cselect_b32 s86,s91,0x40000000
Mvvv 92,220,212
s_add_u32 s86,s86,20480
v_mul_f32 v28,s85,v28
v_mul_f32 v29,s85,v29
Mvvv 96,220,216
buffer_load_dword v255,v17,s[92:95],s86 offen
Mvvv 160,224,200
s_add_u32 m0,s76,81920
Ta 248,9,16896
Ta 250,10,17152
Mvvv 164,224,204
s_cmp_eq_u32 s88,1
s_cselect_b32 s86,0x20000,0
v_subrev_u32 v2,s86,v2
Mvvv 168,224,208
v_add_u32 v2,8192,v2
v_mul_f32 v30,s85,v30
Mvvv 172,224,212
v_mul_f32 v31,s85,v31
v_mul_f32 v108,s85,v108
v_mul_f32 v109,s85,v109
v_mul_f32 v110,s85,v110
v_mul_f32 v111,s85,v111
CV 28,28,29
CV 29,30,31
CV 30,108,109
CV 31,110,111
v_mul_f32 v32,s85,v32
v_mul_f32 v33,s85,v33
v_mul_f32 v34,s85,v34
v_mul_f32 v35,s85,v35
v_mul_f32 v112,s85,v112
v_mul_f32 v113,s85,v113
v_mul_f32 v114,s85,v114
v_mul_f32 v115,s85,v115
CV 32,32,33
CV 33,34,35
CV 34,112,113
CV 35,114,115
v_mul_f32 v36,s85,v36
v_mul_f32 v37,s85,v37
v_mul_f32 v38,s85,v38
v_mul_f32 v39,s85,v39
v_mul_f32 v116,s85,v116
v_mul_f32 v117,s85,v117
v_mul_f32 v118,s85,v118
v_mul_f32 v119,s85,v119
CV 36,36,37
CV 37,38,39
CV 38,116,117
CV 39,118,119
AW 160,20
AW 161,21
AW 162,22
AW 163,23
AW 164,24
AW 165,25
AW 166,26
AW 167,27
AW 168,28
AW 169,29
AW 170,30
AW 171,31
AW 172,32
AW 173,33
AW 174,34
AW 175,35
AW 176,36
AW 177,37
AW 178,38
AW 179,39
Mvvv 176,224,216
WL 2
v_mul_f32 v40,s85,v40
v_mul_f32 v41,s85,v41
Zvav 20,240,200
s_nop 0
Ta 252,9,16904
Ta 254,10,17160
Zvav 24,240,204
v_mul_f32 v42,s85,v42
v_mul_f32 v43,s85,v43
Zvav 28,240,208
LN 2,96,62,0
Zvav 32,240,212
v_mul_f32 v120,s85,v120
v_mul_f32 v121,s85,v121
Zvav 36,240,216
v_mul_f32 v122,s85,v122
v_mul_f32 v123,s85,v123
Zvav 100,244,200
s_nop 0
Tv 220,9,17408
Tv 222,10,17664
Zvav 104,244,204
LN 2,96,63,1024
Zvav 108,244,208
CV 40,40,41
CV 41,42,43
Zvav 112,244,212
CV 42,120,121
CV 43,122,123
v_mul_f32 v44,s85,v44
v_mul_f32 v45,s85,v45
v_mul_f32 v46,s85,v46
v_mul_f32 v47,s85,v47
v_mul_f32 v124,s85,v124
v_mul_f32 v125,s85,v125
v_mul_f32 v126,s85,v126
v_mul_f32 v127,s85,v127
CV 44,44,45
CV 45,46,47
CV 46,124,125
CV 47,126,127
v_mul_f32 v48,s85,v48
v_mul_f32 v49,s85,v49
v_mul_f32 v50,s85,v50
v_mul_f32 v51,s85,v51
v_mul_f32 v128,s85,v128
v_mul_f32 v129,s85,v129
v_mul_f32 v130,s85,v130
v_mul_f32 v131,s85,v131
CV 48,48,49
CV 49,50,51
CV 50,128,129
CV 51,130,131
v_mul_f32 v52,s85,v52
v_mul_f32 v53,s85,v53
v_mul_f32 v54,s85,v54
v_mul_f32 v55,s85,v55
v_mul_f32 v132,s85,v132
v_mul_f32 v133,s85,v133
v_mul_f32 v134,s85,v134
v_mul_f32 v135,s85,v135
CV 52,52,53
CV 53,54,55
CV 54,132,133
CV 55,134,135
v_mul_f32 v56,s85,v56
v_mul_f32 v57,s85,v57
v_mul_f32 v58,s85,v58
v_mul_f32 v59,s85,v59
v_mul_f32 v136,s85,v136
v_mul_f32 v137,s85,v137
v_mul_f32 v138,s85,v138
v_mul_f32 v139,s85,v139
CV 56,56,57
CV 57,58,59
CV 58,136,137
CV 59,138,139
AW 180,40
AW 181,41
AW 182,42
AW 183,43
AW 184,44
AW 185,45
AW 186,46
AW 187,47
AW 188,48
AW 189,49
AW 190,50
AW 191,51
AW 192,52
AW 193,53
AW 194,54
AW 195,55
AW 196,56
AW 197,57
AW 198,58
AW 199,59
Zvav 116,244,216
WL 2
v_mul_f32 v60,s85,v60
v_mul_f32 v61,s85,v61
Zvav 40,248,200
s_nop 0
Tv 224,9,17416
Tv 226,10,17672
Zvav 44,248,204
LN 2,96,62,2048
Zvav 48,248,208
v_mul_f32 v62,s85,v62
v_mul_f32 v63,s85,v63
Zvav 52,248,212
v_mul_f32 v140,s85,v140
v_mul_f32 v141,s85,v141
Zvav 56,248,216
v_mul_f32 v142,s85,v142
v_mul_f32 v143,s85,v143
Zvav 120,252,200
s_nop 0
Ta 240,9,17920
Ta 242,10,18176
Zvav 124,252,204
LN 2,96,63,3072
Zvav 128,252,208
CV 60,60,61
CV 61,62,63
Zvav 132,252,212
CV 62,140,141
CV 63,142,143
Zvav 136,252,216
WL 2
Ta 244,9,17928
Ta 246,10,18184
Mvvv 20,220,180
s_nop 0
Rv 200,12,40960
Rv 204,12,43008
Mvvv 24,220,184
Rv 208,12,45056
v_mul_f32 v64,s85,v64
Mvvv 28,220,188
Rv 212,12,47104
v_mul_f32 v65,s85,v65
Mvvv 32,220,192
Rv 216,12,49152
v_mul_f32 v66,s85,v66
Mvvv 36,220,196
v_mul_f32 v67,s85,v67
v_mul_f32 v144,s85,v144
Mvvv 100,224,180
v_mul_f32 v145,s85,v145
v_mul_f32 v146,s85,v146
Mvvv 104,224,184
v_mul_f32 v147,s85,v147
CV 64,64,65
Mvvv 108,224,188
CV 65,66,67
CV 66,144,145
Mvvv 112,224,192
CV 67,146,147
v_mul_f32 v68,s85,v68
Mvvv 116,224,196
s_waitcnt vmcnt(10) lgkmcnt(2)
s_barrier
Mvav 40,240,180
s_add_u32 m0,s76,49152
Ta 248,9,32768
Ta 250,10,33024
Mvav 44,240,184
s_nop 0
Ta 252,9,32776
Ta 254,10,33032
Mvav 48,240,188
s_cmp_le_u32 s89,6
s_cselect_b64 s[28:29],s[70:71],%3
v_subrev_u32 v3,262144,v3
Mvav 52,240,192
LD 3,28,64,0
Mvav 56,240,196
s_nop 0
v_mul_f32 v69,s85,v69
v_mul_f32 v70,s85,v70
Mvav 120,244,180
s_nop 0
Tv 220,9,33280
Tv 222,10,33536
Mvav 124,244,184
s_nop 0
v_mul_f32 v71,s85,v71
v_mul_f32 v148,s85,v148
Mvav 128,244,188
LD 3,28,66,2048
Mvav 132,244,192
s_add_u32 m0,s76,50176
v_subrev_u32 v3,262144,v3
v_mul_f32 v149,s85,v149
Mvav 136,244,196
WL 2
Rv 180,11,40960
Rv 184,11,43008
Mvav 20,248,200
s_nop 0
Tv 224,9,33288
v_mul_f32 v150,s85,v150
Mvav 24,248,204
s_nop 0
Tv 226,10,33544
Rv 188,11,45056
Mvav 28,248,208
LD 3,28,64,0
Mvav 32,248,212
s_nop 0
Rv 192,11,47104
Rv 196,11,49152
Mvav 36,248,216
s_nop 0
v_mul_f32 v151,s85,v151
CV 68,68,69
Mvav 100,252,200
s_nop 0
Ta 240,9,33792
Ta 242,10,34048
Mvav 104,252,204
LD 3,28,66,2048
Mvav 108,252,208
CV 69,70,71
CV 70,148,149
Mvav 112,252,212
CV 71,150,151
v_mul_f32 v72,s85,v72
Mvav 116,252,216
WL 4
v_mul_f32 v73,s85,v73
v_mul_f32 v74,s85,v74
Mvvv 40,220,200
s_nop 0
Ta 244,9,33800
Ta 246,10,34056
Mvvv 44,220,204
v_mul_f32 v75,s85,v75
v_mul_f32 v152,s85,v152
Mvvv 48,220,208
v_mul_f32 v153,s85,v153
v_mul_f32 v154,s85,v154
Mvvv 52,220,212
v_mul_f32 v155,s85,v155
CV 72,72,73
Mvvv 56,220,216
CV 73,74,75
CV 74,152,153
Mvvv 120,224,200
s_nop 0
Ta 248,9,34304
Ta 250,10,34560
Mvvv 124,224,204
CV 75,154,155
v_mul_f32 v76,s85,v76
Mvvv 128,224,208
v_mul_f32 v77,s85,v77
v_mul_f32 v78,s85,v78
Mvvv 132,224,212
v_mul_f32 v79,s85,v79
v_mul_f32 v156,s85,v156
Mvvv 136,224,216
WL 2
Ta 252,9,34312
Ta 254,10,34568
Mvav 20,240,180
v_mul_f32 v157,s85,v157
v_mul_f32 v158,s85,v158
Mvav 24,240,184
v_mul_f32 v159,s85,v159
CV 76,76,77
Mvav 28,240,188
CV 77,78,79
CV 78,156,157
Mvav 32,240,192
CV 79,158,159
AW 200,60
Mvav 36,240,196
AW 201,61
AW 202,62
Mvav 100,244,180
AW 203,63
AW 204,64
Mvav 104,244,184
AW 205,65
AW 206,66
Mvav 108,244,188
AW 207,67
AW 208,68
Mvav 112,244,192
AW 209,69
AW 210,70
Mvav 116,244,196
s_waitcnt vmcnt(2) lgkmcnt(0)
s_barrier
Mvav 40,248,180
s_nop 0
Rv 220,6,0
Rv 224,7,0
Mvav 44,248,184
s_cmp_le_u32 s89,5
s_cselect_b64 s[24:25],s[68:69],%2
Ra 240,6,1024
Mvav 48,248,188
Ra 244,7,1024
v_subrev_u32 v0,128,v0
Mvav 52,248,192
s_add_u32 m0,s77,139264
Rv 200,12,20480
AW 211,71
Mvav 56,248,196
LD 0,24,48,0
Mvav 120,252,180
s_add_u32 m0,s77,143360
Rv 204,12,22528
Rv 208,12,24576
Mvav 124,252,184
Rv 212,12,26624
AW 212,72
Mvav 128,252,188
AW 213,73
AW 214,74
AW 215,75
AW 216,76
AW 217,77
AW 218,78
AW 219,79
v_mul_f32 v80,s85,v80
v_mul_f32 v81,s85,v81
v_mul_f32 v82,s85,v82
v_mul_f32 v83,s85,v83
v_mul_f32 v160,s85,v160
v_mul_f32 v161,s85,v161
v_mul_f32 v162,s85,v162
v_mul_f32 v163,s85,v163
CV 80,80,81
CV 81,82,83
CV 82,160,161
CV 83,162,163
v_mul_f32 v84,s85,v84
v_mul_f32 v85,s85,v85
v_mul_f32 v86,s85,v86
v_mul_f32 v87,s85,v87
v_mul_f32 v164,s85,v164
v_mul_f32 v165,s85,v165
v_mul_f32 v166,s85,v166
v_mul_f32 v167,s85,v167
CV 84,84,85
CV 85,86,87
CV 86,164,165
CV 87,166,167
v_mul_f32 v88,s85,v88
v_mul_f32 v89,s85,v89
v_mul_f32 v90,s85,v90
v_mul_f32 v91,s85,v91
v_mul_f32 v168,s85,v168
v_mul_f32 v169,s85,v169
v_mul_f32 v170,s85,v170
v_mul_f32 v171,s85,v171
CV 88,88,89
CV 89,90,91
CV 90,168,169
CV 91,170,171
v_mul_f32 v92,s85,v92
v_mul_f32 v93,s85,v93
v_mul_f32 v94,s85,v94
v_mul_f32 v95,s85,v95
v_mul_f32 v172,s85,v172
v_mul_f32 v173,s85,v173
v_mul_f32 v174,s85,v174
v_mul_f32 v175,s85,v175
CV 92,92,93
CV 93,94,95
CV 94,172,173
CV 95,174,175
v_mul_f32 v96,s85,v96
v_mul_f32 v97,s85,v97
v_mul_f32 v98,s85,v98
v_mul_f32 v99,s85,v99
v_mul_f32 v176,s85,v176
v_mul_f32 v177,s85,v177
v_mul_f32 v178,s85,v178
v_mul_f32 v179,s85,v179
CV 96,96,97
CV 97,98,99
CV 98,176,177
CV 99,178,179
AW 220,80
AW 221,81
AW 222,82
AW 223,83
AW 224,84
AW 225,85
AW 226,86
AW 227,87
AW 228,88
AW 229,89
AW 230,90
AW 231,91
AW 232,92
AW 233,93
AW 234,94
AW 235,95
AW 236,96
AW 237,97
AW 238,98
AW 239,99
Mvav 132,252,192
LD 0,24,50,0
Mvav 136,252,196
WL 5
s_add_u32 m0,s77,147456
Rv 216,12,28672
Zvva 140,220,160
s_nop 0
Rv 180,11,20480
Rv 184,11,22528
Mvva 140,224,180
Rv 188,11,24576
Mvaa 140,240,200
WL 7
LD 0,24,52,0
Mvaa 140,244,220
s_add_u32 m0,s77,151552
Rv 192,11,26624
Rv 196,11,28672
Zvva 144,220,164
Mvva 144,224,184
s_nop 0
Ta 248,9,16384
Ta 250,10,16640
Mvaa 144,240,204
LD 0,24,54,0
Mvaa 144,244,224
s_add_u32 m0,s77,155648
Zvva 148,220,168
Mvva 148,224,188
Mvaa 148,240,208
s_nop 0
Ta 252,9,16392
Ta 254,10,16648
Mvaa 148,244,228
LD 0,24,56,0
Zvva 152,220,172
s_add_u32 m0,s76,65536
s_cmp_eq_u32 s88,2
s_cselect_b32 s86,0x20000,0
Mvva 152,224,192
v_subrev_u32 v2,s86,v2
v_add_u32 v2,8192,v2
Mvaa 152,240,212
Mvaa 152,244,232
LN 2,96,62,0
Zvva 156,220,176
s_nop 0
Tv 220,9,16896
Tv 222,10,17152
Mvva 156,224,196
Mvaa 156,240,216
Mvaa 156,244,236
WL 2
Tv 224,9,16904
Tv 226,10,17160
Mvav 20,248,200
LN 2,96,63,1024
Mvav 24,248,204
Mvav 28,248,208
v_max3_f32 v80,v140,v141,v142
v_max3_f32 v84,v144,v145,v146
Mvav 32,248,212
v_max3_f32 v88,v148,v149,v150
v_max3_f32 v92,v152,v153,v154
Mvav 36,248,216
LN 2,96,62,2048
Mvav 100,252,200
Ra 240,6,2048
v_max3_f32 v96,v156,v157,v158
Mvav 104,252,204
Ra 244,7,2048
v_max_f32 v81,v80,v143
Mvav 108,252,208
Ra 248,6,3072
v_max_f32 v80,v80,v143
Mvav 112,252,212
LN 2,96,63,3072
Mvav 116,252,216
s_waitcnt vmcnt(9) lgkmcnt(3)
s_barrier
Mvvv 40,220,200
Ra 252,7,3072
v_max_f32 v85,v84,v147
Mvvv 44,220,204
s_and_b32 s86,%12,458752
s_cmp_eq_u32 s86,393216
s_cselect_b32 s86,s91,0x40000000
Mvvv 48,220,208
s_add_u32 s86,s86,24576
v_max_f32 v84,v84,v147
v_max_f32 v89,v88,v151
Mvvv 52,220,212
buffer_load_dword v255,v17,s[92:95],s86 offen
Mvvv 56,220,216
s_add_u32 m0,s76,49152
v_max_f32 v88,v88,v151
Mvvv 120,224,200
s_cmp_le_u32 s89,5
s_cselect_b64 s[28:29],s[70:71],%3
v_subrev_u32 v3,262144,v3
Mvvv 124,224,204
v_max_f32 v93,v92,v155
v_max_f32 v92,v92,v155
Mvvv 128,224,208
v_max_f32 v97,v96,v159
v_max_f32 v96,v96,v159
Mvvv 132,224,212
v_permlane32_swap_b32 v80,v81
v_permlane32_swap_b32 v84,v85
v_permlane32_swap_b32 v88,v89
v_permlane32_swap_b32 v92,v93
v_permlane32_swap_b32 v96,v97
v_max_f32 v82,v80,v81
v_max_f32 v80,v80,v81
v_max_f32 v86,v84,v85
v_max_f32 v84,v84,v85
v_max_f32 v90,v88,v89
v_max_f32 v88,v88,v89
v_max_f32 v94,v92,v93
v_max_f32 v92,v92,v93
v_max_f32 v98,v96,v97
v_max_f32 v96,v96,v97
v_permlane16_swap_b32 v80,v82
v_permlane16_swap_b32 v84,v86
v_permlane16_swap_b32 v88,v90
v_permlane16_swap_b32 v92,v94
v_permlane16_swap_b32 v96,v98
v_min_f32_e64 v81,-v80,-v82
v_min_f32_e64 v85,-v84,-v86
v_min_f32_e64 v89,-v88,-v90
v_min_f32_e64 v93,-v92,-v94
v_min_f32_e64 v97,-v96,-v98
v_min_f32_e64 v83,-v80,-v82
v_min_f32_e64 v87,-v84,-v86
v_min_f32_e64 v91,-v88,-v90
v_min_f32_e64 v95,-v92,-v94
v_min_f32_e64 v99,-v96,-v98
v_min_f32_e64 v80,-v80,-v82
v_min_f32_e64 v84,-v84,-v86
v_min_f32_e64 v88,-v88,-v90
v_min_f32_e64 v92,-v92,-v94
v_min_f32_e64 v96,-v96,-v98
v_mov_b32 v82,v80
v_mov_b32 v86,v84
v_mov_b32 v90,v88
v_mov_b32 v94,v92
v_mov_b32 v98,v96
Mvvv 136,224,216
WL 0
v_add_f32 v140,v140,v80
v_add_f32 v141,v141,v80
Nvaa 160,240,160,80
v_add_f32 v142,v142,v80
v_add_f32 v143,v143,v80
Mvaa 160,244,180
LD 3,28,64,0
Mvaa 160,248,200
EX 140
Mvaa 160,252,220
EX 141
Nvaa 164,240,164,84
EX 142
Mvaa 164,244,184
s_nop 0
Tv 220,9,17408
Tv 222,10,17664
Mvaa 164,248,204
LD 3,28,66,2048
Mvaa 164,252,224
s_cmp_le_u32 s89,4
s_cselect_b64 s[24:25],s[68:69],%2
v_subrev_u32 v0,128,v0
Nvaa 168,240,168,88
s_add_u32 m0,s77,118784
EX 143
Mvaa 168,244,188
CV 140,140,141
CV 141,142,143
Mvaa 168,248,208
s_nop 0
Tv 224,9,17416
Tv 226,10,17672
Mvaa 168,252,228
LD 0,24,48,0
Nvaa 172,240,172,92
s_add_u32 m0,s77,122880
v_add_f32 v144,v144,v84
v_add_f32 v145,v145,v84
Mvaa 172,244,192
v_add_f32 v146,v146,v84
v_add_f32 v147,v147,v84
Mvaa 172,248,212
EX 144
Mvaa 172,252,232
LD 0,24,50,0
Nvaa 176,240,176,96
s_add_u32 m0,s77,126976
Ta 240,9,17920
Ta 242,10,18176
Mvaa 176,244,196
EX 145
Mvaa 176,248,216
EX 146
Mvaa 176,252,236
WL 2
Ta 244,9,17928
Ta 246,10,18184
Mvvv 20,220,180
LD 0,24,52,0
Mvvv 24,220,184
s_add_u32 m0,s77,131072
EX 147
Mvvv 28,220,188
CV 144,144,145
CV 145,146,147
Mvvv 32,220,192
v_add_f32 v148,v148,v88
v_add_f32 v149,v149,v88
Mvvv 36,220,196
LD 0,24,54,0
Mvvv 100,224,180
s_add_u32 m0,s77,135168
v_add_f32 v150,v150,v88
v_add_f32 v151,v151,v88
Mvvv 104,224,184
EX 148
Mvvv 108,224,188
EX 149
Mvvv 112,224,192
LD 0,24,56,0
Mvvv 116,224,196
s_waitcnt vmcnt(5) lgkmcnt(0)
s_barrier
Mvav 40,240,180
s_add_u32 m0,s76,50176
Ra 248,6,16384
Ra 252,7,16384
Mvav 44,240,184
Rv 220,6,17408
v_subrev_u32 v3,262144,v3
Mvav 48,240,188
Rv 224,7,17408
Mvav 52,240,192
LD 3,28,64,0
Mvav 56,240,196
s_nop 0
Rv 200,12,40960
Rv 204,12,43008
Mvav 120,244,180
Rv 208,12,45056
Mvav 124,244,184
Rv 212,12,47104
Mvav 128,244,188
LD 3,28,66,2048
Mvav 132,244,192
EX 150
EX 151
CV 148,148,149
CV 149,150,151
v_add_f32 v152,v152,v92
v_add_f32 v153,v153,v92
v_add_f32 v154,v154,v92
v_add_f32 v155,v155,v92
EX 152
EX 153
EX 154
EX 155
CV 152,152,153
CV 153,154,155
v_add_f32 v156,v156,v96
v_add_f32 v157,v157,v96
v_add_f32 v158,v158,v96
v_add_f32 v159,v159,v96
EX 156
EX 157
EX 158
EX 159
CV 156,156,157
CV 157,158,159
EX 160
EX 161
EX 162
EX 163
CV 142,160,161
CV 143,162,163
Mvav 136,244,196
WL 5
s_add_u32 m0,s76,0
Rv 216,12,49152
Nvaa 160,248,160,80
s_cmp_eq_u32 s88,3
s_cselect_b32 s86,0x20000,0
Mvaa 160,252,180
v_subrev_u32 v1,s86,v1
v_add_u32 v1,8192,v1
Mvva 160,220,200
EX 164
EX 165
EX 166
EX 167
CV 146,164,165
CV 147,166,167
WL 3
Mvva 160,224,220
LN 1,36,58,0
Nvaa 164,248,164,84
s_nop 0
Rv 180,11,40960
Rv 184,11,43008
Mvaa 164,252,184
Ta 240,9,16384
v_readfirstlane_b32 %0,v240
Mvva 164,220,204
EX 168
EX 169
EX 170
EX 171
CV 150,168,169
CV 151,170,171
Mvva 164,224,224
s_nop 0
Ta 242,10,16640
Rv 188,11,45056
Nvaa 168,248,168,88
LN 1,36,59,1024
Mvaa 168,252,188
Rv 192,11,47104
v_readfirstlane_b32 %1,v241
Mvva 168,220,208
EX 172
EX 173
EX 174
EX 175
CV 154,172,173
CV 155,174,175
Mvva 168,224,228
s_nop 0
Ta 244,9,16392
Ta 246,10,16648
Nvaa 172,248,172,92
LN 1,36,60,2048
Mvaa 172,252,192
Rv 196,11,49152
Mvva 172,220,212
EX 176
EX 177
EX 178
EX 179
CV 158,176,177
CV 159,178,179
Mvva 172,224,232
EX 160
Nvaa 176,248,176,96
s_nop 0
Ta 248,9,16896
Ta 250,10,17152
Mvaa 176,252,196
LN 1,36,61,3072
Mvva 176,220,216
EX 161
Mvva 176,224,236
WL 3
Ta 252,9,16904
Ta 254,10,17160
Mvav 20,240,200
EX 162
Mvav 24,240,204
EX 163
Mvav 28,240,208
CV 160,160,161
CV 161,162,163
Mvav 32,240,212
EX 164
Mvav 36,240,216
EX 165
Mvav 100,244,200
EX 166
Mvav 104,244,204
EX 167
Mvav 108,244,208
CV 164,164,165
CV 165,166,167
Mvav 112,244,212
EX 168
Mvav 116,244,216
WL 0
s_barrier
Mvav 40,248,200
s_add_u32 m0,s76,49152
Tv 220,8,49152
Tv 222,8,53248
Mvav 44,248,204
s_cmp_le_u32 s89,4
s_cselect_b64 s[28:29],s[70:71],%3
v_subrev_u32 v3,262144,v3
Mvav 48,248,208
LD 3,28,64,0
Mvav 52,248,212
EX 169
Mvav 56,248,216
EX 170
Mvav 120,252,200
s_nop 0
Tv 224,252,49152
Tv 226,252,53248
Mvav 124,252,204
LD 3,28,66,2048
Mvav 128,252,208
s_cmp_le_u32 s89,3
s_cselect_b64 s[24:25],s[68:69],%2
v_subrev_u32 v0,128,v0
Mvav 132,252,212
s_add_u32 m0,s77,139264
EX 171
Mvav 136,252,216
CV 168,168,169
CV 169,170,171
Zvvv 228,248,140
s_nop 0
Ta 240,8,49664
Ta 242,8,53760
Zvvv 232,248,144
LD 0,24,48,0
Zvvv 236,248,148
s_add_u32 m0,s77,143360
EX 172
Zvvv 240,248,152
EX 173
Zvvv 244,248,156
WL 2
EX 174
Zavv 0,220,140
s_nop 0
Ta 244,252,49664
Ta 246,252,53760
Zavv 4,220,144
LD 0,24,50,0
Zavv 8,220,148
s_add_u32 m0,s77,147456
EX 175
Zavv 12,220,152
CV 172,172,173
CV 173,174,175
Zavv 16,220,156
EX 176
Zavv 20,224,140
s_nop 0
Ta 248,8,50176
Ta 250,8,54272
Zavv 24,224,144
LD 0,24,52,0
Zavv 28,224,148
s_add_u32 m0,s77,151552
EX 177
Zavv 32,224,152
EX 178
Zavv 36,224,156
WL 2
EX 179
Zaav 40,240,140
s_nop 0
Ta 252,252,50176
Ta 254,252,54272
Zaav 44,240,144
LD 0,24,54,0
Zaav 48,240,148
s_add_u32 m0,s77,155648
CV 176,176,177
CV 177,178,179
Zaav 52,240,152
Zaav 56,240,156
Zaav 60,244,140
s_nop 0
Tv 220,8,50688
Tv 222,8,54784
Zaav 64,244,144
LD 0,24,56,0
Zaav 68,244,148
Zaav 72,244,152
Zaav 76,244,156
WL 2
Tv 224,252,50688
Tv 226,252,54784
Zaav 80,248,140
Zaav 84,248,144
Zaav 88,248,148
Zaav 92,248,152
Zaav 96,248,156
Zaav 100,252,140
Ra 240,6,18432
Zaav 104,252,144
Ra 244,7,18432
Zaav 108,252,148
Ra 248,6,19456
Zaav 112,252,152
Zaav 116,252,156
s_waitcnt vmcnt(11) lgkmcnt(3)
s_barrier
Zavv 120,220,140
s_add_u32 m0,s76,98304
Ra 252,7,19456
Zavv 124,220,144
s_cmp_eq_u32 s88,3
s_cselect_b32 s86,0x20000,0
v_subrev_u32 v2,s86,v2
Zavv 128,220,148
v_add_u32 v2,8192,v2
Zavv 132,220,152
LN 2,96,62,0
Zavv 136,220,156
Zavv 140,224,140
Zavv 144,224,144
Zavv 148,224,148
LN 2,96,63,1024
Zavv 152,224,152
Zavv 156,224,156
WL 0
Nvaa 140,240,160,80
Mvaa 140,244,180
LN 2,96,62,2048
Mvaa 140,248,200
Mvaa 140,252,220
Nvaa 144,240,164,84
Mvaa 144,244,184
s_nop 0
Tv 220,9,17408
Tv 222,10,17664
Mvaa 144,248,204
LN 2,96,63,3072
Mvaa 144,252,224
EX 140
Nvaa 148,240,168,88
EX 141
Mvaa 148,244,188
EX 142
Mvaa 148,248,208
s_nop 0
Tv 224,9,17416
Tv 226,10,17672
Mvaa 148,252,228
EX 143
Nvaa 152,240,172,92
CV 162,140,141
CV 163,142,143
Mvaa 152,244,192
EX 144
Mvaa 152,248,212
EX 145
Mvaa 152,252,232
EX 146
Nvaa 156,240,176,96
s_nop 0
Ta 240,9,17920
Ta 242,10,18176
Mvaa 156,244,196
EX 147
Mvaa 156,248,216
CV 166,144,145
CV 167,146,147
Mvaa 156,252,236
WL 2
Ta 244,9,17928
Ta 246,10,18184
Mvvv 20,220,180
EX 148
Mvvv 24,220,184
EX 149
Mvvv 28,220,188
EX 150
Mvvv 32,220,192
EX 151
Mvvv 36,220,196
CV 170,148,149
CV 171,150,151
Mvvv 100,224,180
EX 152
Mvvv 104,224,184
EX 153
Mvvv 108,224,188
EX 154
Mvvv 112,224,192
EX 155
Mvvv 116,224,196
s_waitcnt vmcnt(9) lgkmcnt(0)
s_barrier
Mvav 40,240,180
s_add_u32 m0,s76,50176
Ra 248,6,32768
Ra 252,7,32768
Mvav 44,240,184
Rv 220,6,33792
v_subrev_u32 v3,262144,v3
Mvav 48,240,188
Rv 224,7,33792
CV 174,152,153
Mvav 52,240,192
LD 3,28,64,0
Mvav 56,240,196
s_nop 0
Rv 200,12,20480
Rv 204,12,22528
Mvav 120,244,180
Rv 208,12,24576
CV 175,154,155
Mvav 124,244,184
Rv 212,12,26624
Mvav 128,244,188
LD 3,28,66,2048
Mvav 132,244,192
s_add_u32 m0,s76,16384
s_cmp_eq_u32 s88,4
s_cselect_b32 s86,0x20000,0
Mvav 136,244,196
WL 5
v_subrev_u32 v1,s86,v1
v_add_u32 v1,8192,v1
Nvaa 140,248,160,80
s_nop 0
Rv 216,12,28672
Rv 180,11,20480
Mvaa 140,252,180
LN 1,36,58,0
Mvva 140,220,200
WL 4
Rv 184,11,22528
Rv 188,11,24576
Mvva 140,224,220
Rv 192,11,26624
Nvaa 144,248,164,84
Rv 196,11,28672
Mvaa 144,252,184
Ta 240,9,16384
Mvva 144,220,204
Ta 242,10,16640
Mvva 144,224,224
LN 1,36,59,1024
Nvaa 148,248,168,88
EX 156
Mvaa 148,252,188
EX 157
Mvva 148,220,208
s_nop 0
Ta 244,9,16392
Ta 246,10,16648
Mvva 148,224,228
LN 1,36,60,2048
Nvaa 152,248,172,92
EX 158
Mvaa 152,252,192
EX 159
Mvva 152,220,212
CV 178,156,157
CV 179,158,159
Mvva 152,224,232
LN 1,36,61,3072
Nvaa 156,248,176,96
s_nop 0
Ta 248,9,16896
Ta 250,10,17152
Mvaa 156,252,196
s_and_b32 s86,%12,458752
s_cmp_eq_u32 s86,458752
s_cselect_b32 s86,s91,0x40000000
Mvva 156,220,216
s_add_u32 s86,s86,28672
EX 140
Mvva 156,224,236
WL 2
Ta 252,9,16904
Ta 254,10,17160
Mvav 20,240,200
buffer_load_dword v255,v17,s[92:95],s86 offen
Mvav 24,240,204
EX 141
Mvav 28,240,208
EX 142
Mvav 32,240,212
EX 143
Mvav 36,240,216
CV 140,140,141
CV 141,142,143
Mvav 100,244,200
EX 144
Mvav 104,244,204
EX 145
Mvav 108,244,208
EX 146
Mvav 112,244,212
EX 147
Mvav 116,244,216
WL 0
s_barrier
Mvav 40,248,200
s_add_u32 m0,s76,49152
Tv 220,8,32768
Tv 222,8,36864
Mvav 44,248,204
s_cmp_le_u32 s89,3
s_cselect_b64 s[28:29],s[70:71],%3
v_subrev_u32 v3,262144,v3
Mvav 48,248,208
LD 3,28,64,0
Mvav 52,248,212
CV 144,144,145
CV 145,146,147
Mvav 56,248,216
EX 148
Mvav 120,252,200
s_nop 0
Tv 224,252,32768
Tv 226,252,36864
Mvav 124,252,204
LD 3,28,66,2048
Mvav 128,252,208
s_cmp_le_u32 s89,2
s_cselect_b64 s[24:25],s[68:69],%2
v_subrev_u32 v0,128,v0
Mvav 132,252,212
s_add_u32 m0,s77,118784
EX 149
Mvav 136,252,216
EX 150
Mvvv 228,248,160
s_nop 0
Ta 240,8,33280
Ta 242,8,37376
Mvvv 232,248,164
LD 0,24,48,0
Mvvv 236,248,168
s_add_u32 m0,s77,122880
EX 151
Mvvv 240,248,172
CV 148,148,149
CV 149,150,151
Mvvv 244,248,176
WL 2
EX 152
Mavv 0,220,160
s_nop 0
Ta 244,252,33280
Ta 246,252,37376
Mavv 4,220,164
LD 0,24,50,0
Mavv 8,220,168
s_add_u32 m0,s77,126976
EX 153
Mavv 12,220,172
EX 154
Mavv 16,220,176
EX 155
Mavv 20,224,160
s_nop 0
Ta 248,8,33792
Ta 250,8,37888
Mavv 24,224,164
LD 0,24,52,0
Mavv 28,224,168
s_add_u32 m0,s77,131072
CV 152,152,153
CV 153,154,155
Mavv 32,224,172
EX 156
Mavv 36,224,176
WL 2
EX 157
Maav 40,240,160
s_nop 0
Ta 252,252,33792
Ta 254,252,37888
Maav 44,240,164
LD 0,24,54,0
Maav 48,240,168
s_add_u32 m0,s77,135168
EX 158
Maav 52,240,172
EX 159
Maav 56,240,176
CV 156,156,157
CV 157,158,159
Maav 60,244,160
s_nop 0
Tv 220,8,34304
Tv 222,8,38400
Maav 64,244,164
LD 0,24,56,0
Maav 68,244,168
Maav 72,244,172
Maav 76,244,176
WL 2
Tv 224,252,34304
Tv 226,252,38400
Maav 80,248,160
Maav 84,248,164
Maav 88,248,168
Maav 92,248,172
Maav 96,248,176
Maav 100,252,160
Ra 240,6,34816
Maav 104,252,164
Ra 244,7,34816
Maav 108,252,168
Ra 248,6,35840
Maav 112,252,172
Maav 116,252,176
s_waitcnt vmcnt(12) lgkmcnt(3)
s_barrier
Mavv 120,220,160
s_add_u32 m0,s76,81920
Ra 252,7,35840
Mavv 124,220,164
s_cmp_eq_u32 s88,4
s_cselect_b32 s86,0x20000,0
v_subrev_u32 v2,s86,v2
Mavv 128,220,168
v_add_u32 v2,8192,v2
Mavv 132,220,172
LN 2,96,62,0
Mavv 136,220,176
Mavv 140,224,160
Mavv 144,224,164
Mavv 148,224,168
LN 2,96,63,1024
Mavv 152,224,172
Mavv 156,224,176
WL 0
Nvaa 160,240,160,80
Mvaa 160,244,180
LN 2,96,62,2048
Mvaa 160,248,200
Mvaa 160,252,220
Nvaa 164,240,164,84
Mvaa 164,244,184
s_nop 0
Tv 220,9,17408
Tv 222,10,17664
Mvaa 164,248,204
LN 2,96,63,3072
Mvaa 164,252,224
EX 160
Nvaa 168,240,168,88
EX 161
Mvaa 168,244,188
EX 162
Mvaa 168,248,208
s_nop 0
Tv 224,9,17416
Tv 226,10,17672
Mvaa 168,252,228
EX 163
Nvaa 172,240,172,92
CV 142,160,161
CV 143,162,163
Mvaa 172,244,192
EX 164
Mvaa 172,248,212
EX 165
Mvaa 172,252,232
EX 166
Nvaa 176,240,176,96
s_nop 0
Ta 240,9,17920
Ta 242,10,18176
Mvaa 176,244,196
EX 167
Mvaa 176,248,216
CV 146,164,165
CV 147,166,167
Mvaa 176,252,236
WL 2
Ta 244,9,17928
Ta 246,10,18184
Mvvv 20,220,180
EX 168
Mvvv 24,220,184
EX 169
Mvvv 28,220,188
EX 170
Mvvv 32,220,192
EX 171
Mvvv 36,220,196
CV 150,168,169
CV 151,170,171
Mvvv 100,224,180
EX 172
Mvvv 104,224,184
EX 173
Mvvv 108,224,188
EX 174
Mvvv 112,224,192
EX 175
Mvvv 116,224,196
s_waitcnt vmcnt(9) lgkmcnt(0)
s_barrier
Mvav 40,240,180
s_add_u32 m0,s76,50176
Ra 248,6,0
Ra 252,7,0
Mvav 44,240,184
Rv 220,6,1024
v_subrev_u32 v3,262144,v3
Mvav 48,240,188
Rv 224,7,1024
CV 154,172,173
Mvav 52,240,192
LD 3,28,64,0
Mvav 56,240,196
s_nop 0
Rv 200,12,40960
Rv 204,12,43008
Mvav 120,244,180
Rv 208,12,45056
CV 155,174,175
Mvav 124,244,184
Rv 212,12,47104
Mvav 128,244,188
LD 3,28,66,2048
Mvav 132,244,192
s_add_u32 m0,s76,32768
s_cmp_eq_u32 s88,5
s_cselect_b32 s86,0x20000,0
Mvav 136,244,196
WL 5
v_subrev_u32 v1,s86,v1
v_add_u32 v1,8192,v1
Nvaa 160,248,160,80
s_nop 0
Rv 216,12,49152
Rv 180,11,40960
Mvaa 160,252,180
LN 1,36,58,0
Mvva 160,220,200
WL 4
Rv 184,11,43008
Rv 188,11,45056
Mvva 160,224,220
Rv 192,11,47104
Nvaa 164,248,164,84
Rv 196,11,49152
Mvaa 164,252,184
Ta 240,9,16384
Mvva 164,220,204
Ta 242,10,16640
Mvva 164,224,224
LN 1,36,59,1024
Nvaa 168,248,168,88
EX 176
Mvaa 168,252,188
EX 177
Mvva 168,220,208
s_nop 0
Ta 244,9,16392
Ta 246,10,16648
Mvva 168,224,228
LN 1,36,60,2048
Nvaa 172,248,172,92
EX 178
Mvaa 172,252,192
EX 179
Mvva 172,220,212
CV 158,176,177
CV 159,178,179
Mvva 172,224,232
LN 1,36,61,3072
Nvaa 176,248,176,96
s_nop 0
Ta 248,9,16896
Ta 250,10,17152
Mvaa 176,252,196
EX 160
Mvva 176,220,216
EX 161
Mvva 176,224,236
WL 2
Ta 252,9,16904
Ta 254,10,17160
Mvav 20,240,200
EX 162
Mvav 24,240,204
EX 163
Mvav 28,240,208
CV 160,160,161
CV 161,162,163
Mvav 32,240,212
EX 164
Mvav 36,240,216
EX 165
Mvav 100,244,200
EX 166
Mvav 104,244,204
EX 167
Mvav 108,244,208
CV 164,164,165
CV 165,166,167
Mvav 112,244,212
EX 168
Mvav 116,244,216
WL 0
s_barrier
Mvav 40,248,200
s_add_u32 m0,s76,49152
Tv 220,8,16384
Tv 222,8,20480
Mvav 44,248,204
s_cmp_le_u32 s89,2
s_cselect_b64 s[28:29],s[70:71],%3
v_subrev_u32 v3,262144,v3
Mvav 48,248,208
LD 3,28,64,0
Mvav 52,248,212
EX 169
Mvav 56,248,216
EX 170
Mvav 120,252,200
s_nop 0
Tv 224,252,16384
Tv 226,252,20480
Mvav 124,252,204
LD 3,28,66,2048
Mvav 128,252,208
s_cmp_le_u32 s89,1
s_cselect_b64 s[24:25],s[68:69],%2
v_subrev_u32 v0,128,v0
Mvav 132,252,212
s_add_u32 m0,s77,139264
EX 171
Mvav 136,252,216
CV 168,168,169
CV 169,170,171
Mvvv 228,248,140
s_nop 0
Ta 240,8,16896
Ta 242,8,20992
Mvvv 232,248,144
LD 0,24,48,0
Mvvv 236,248,148
s_add_u32 m0,s77,143360
EX 172
Mvvv 240,248,152
EX 173
Mvvv 244,248,156
WL 2
EX 174
Mavv 0,220,140
s_nop 0
Ta 244,252,16896
Ta 246,252,20992
Mavv 4,220,144
LD 0,24,50,0
Mavv 8,220,148
s_add_u32 m0,s77,147456
EX 175
Mavv 12,220,152
CV 172,172,173
CV 173,174,175
Mavv 16,220,156
EX 176
Mavv 20,224,140
s_nop 0
Ta 248,8,17408
Ta 250,8,21504
Mavv 24,224,144
LD 0,24,52,0
Mavv 28,224,148
s_add_u32 m0,s77,151552
EX 177
Mavv 32,224,152
EX 178
Mavv 36,224,156
WL 2
EX 179
Maav 40,240,140
s_nop 0
Ta 252,252,17408
Ta 254,252,21504
Maav 44,240,144
LD 0,24,54,0
Maav 48,240,148
s_add_u32 m0,s77,155648
CV 176,176,177
CV 177,178,179
Maav 52,240,152
Maav 56,240,156
Maav 60,244,140
s_nop 0
Tv 220,8,17920
Tv 222,8,22016
Maav 64,244,144
LD 0,24,56,0
Maav 68,244,148
Maav 72,244,152
Maav 76,244,156
WL 2
Tv 224,252,17920
Tv 226,252,22016
Maav 80,248,140
Maav 84,248,144
Maav 88,248,148
Maav 92,248,152
Maav 96,248,156
Maav 100,252,140
Ra 240,6,2048
Maav 104,252,144
Ra 244,7,2048
Maav 108,252,148
Ra 248,6,3072
Maav 112,252,152
Maav 116,252,156
s_waitcnt vmcnt(11) lgkmcnt(3)
s_barrier
Mavv 120,220,140
s_add_u32 m0,s76,65536
Ra 252,7,3072
Mavv 124,220,144
s_cmp_eq_u32 s88,5
s_cselect_b32 s86,0x20000,0
v_subrev_u32 v2,s86,v2
Mavv 128,220,148
v_add_u32 v2,8192,v2
Mavv 132,220,152
LN 2,96,62,0
Mavv 136,220,156
Mavv 140,224,140
Mavv 144,224,144
Mavv 148,224,148
LN 2,96,63,1024
Mavv 152,224,152
Mavv 156,224,156
WL 0
Nvaa 140,240,160,80
Mvaa 140,244,180
LN 2,96,62,2048
Mvaa 140,248,200
Mvaa 140,252,220
Nvaa 144,240,164,84
Mvaa 144,244,184
s_nop 0
Tv 220,9,17408
Tv 222,10,17664
Mvaa 144,248,204
LN 2,96,63,3072
Mvaa 144,252,224
EX 140
Nvaa 148,240,168,88
EX 141
Mvaa 148,244,188
EX 142
Mvaa 148,248,208
s_nop 0
Tv 224,9,17416
Tv 226,10,17672
Mvaa 148,252,228
EX 143
Nvaa 152,240,172,92
CV 162,140,141
CV 163,142,143
Mvaa 152,244,192
EX 144
Mvaa 152,248,212
EX 145
Mvaa 152,252,232
EX 146
Nvaa 156,240,176,96
s_nop 0
Ta 240,9,17920
Ta 242,10,18176
Mvaa 156,244,196
EX 147
Mvaa 156,248,216
CV 166,144,145
CV 167,146,147
Mvaa 156,252,236
WL 2
Ta 244,9,17928
Ta 246,10,18184
Mvvv 20,220,180
EX 148
Mvvv 24,220,184
EX 149
Mvvv 28,220,188
EX 150
Mvvv 32,220,192
EX 151
Mvvv 36,220,196
CV 170,148,149
CV 171,150,151
Mvvv 100,224,180
EX 152
Mvvv 104,224,184
EX 153
Mvvv 108,224,188
EX 154
Mvvv 112,224,192
EX 155
Mvvv 116,224,196
s_waitcnt vmcnt(9) lgkmcnt(0)
s_barrier
Mvav 40,240,180
s_add_u32 m0,s76,50176
Ra 248,6,16384
Ra 252,7,16384
Mvav 44,240,184
Rv 220,6,17408
v_subrev_u32 v3,262144,v3
Mvav 48,240,188
Rv 224,7,17408
CV 174,152,153
Mvav 52,240,192
LD 3,28,64,0
Mvav 56,240,196
s_nop 0
Rv 200,12,20480
Rv 204,12,22528
Mvav 120,244,180
Rv 208,12,24576
CV 175,154,155
Mvav 124,244,184
Rv 212,12,26624
Mvav 128,244,188
LD 3,28,66,2048
Mvav 132,244,192
s_add_u32 m0,s76,0
s_cmp_eq_u32 s88,6
s_cselect_b32 s86,0x20000,0
Mvav 136,244,196
WL 5
v_subrev_u32 v1,s86,v1
v_add_u32 v1,8192,v1
Nvaa 140,248,160,80
s_nop 0
Rv 216,12,28672
Rv 180,11,20480
Mvaa 140,252,180
LN 1,36,58,0
Mvva 140,220,200
WL 4
Rv 184,11,22528
Rv 188,11,24576
Mvva 140,224,220
Rv 192,11,26624
Nvaa 144,248,164,84
Rv 196,11,28672
Mvaa 144,252,184
Ta 240,9,16384
Mvva 144,220,204
Ta 242,10,16640
Mvva 144,224,224
LN 1,36,59,1024
Nvaa 148,248,168,88
EX 156
Mvaa 148,252,188
EX 157
Mvva 148,220,208
s_nop 0
Ta 244,9,16392
Ta 246,10,16648
Mvva 148,224,228
LN 1,36,60,2048
Nvaa 152,248,172,92
EX 158
Mvaa 152,252,192
EX 159
Mvva 152,220,212
CV 178,156,157
CV 179,158,159
Mvva 152,224,232
LN 1,36,61,3072
Nvaa 156,248,176,96
s_nop 0
Ta 248,9,16896
Ta 250,10,17152
Mvaa 156,252,196
s_and_b32 s86,%12,458752
s_cmp_eq_u32 s86,0
s_cselect_b32 s86,s91,0x40000000
Mvva 156,220,216
s_add_u32 s86,s86,32768
EX 140
Mvva 156,224,236
WL 2
Ta 252,9,16904
Ta 254,10,17160
Mvav 20,240,200
buffer_load_dword v255,v17,s[92:95],s86 offen
Mvav 24,240,204
EX 141
Mvav 28,240,208
EX 142
Mvav 32,240,212
EX 143
Mvav 36,240,216
CV 140,140,141
CV 141,142,143
Mvav 100,244,200
EX 144
Mvav 104,244,204
EX 145
Mvav 108,244,208
EX 146
Mvav 112,244,212
EX 147
Mvav 116,244,216
WL 0
s_barrier
Mvav 40,248,200
s_add_u32 m0,s76,49152
Tv 220,8,49152
Tv 222,8,53248
Mvav 44,248,204
s_cmp_le_u32 s89,1
s_cselect_b64 s[28:29],s[70:71],%3
v_subrev_u32 v3,262144,v3
Mvav 48,248,208
LD 3,28,64,0
Mvav 52,248,212
CV 144,144,145
CV 145,146,147
Mvav 56,248,216
EX 148
Mvav 120,252,200
s_nop 0
Tv 224,252,49152
Tv 226,252,53248
Mvav 124,252,204
LD 3,28,66,2048
Mvav 128,252,208
s_cmp_le_u32 s89,0
s_cselect_b64 s[24:25],s[68:69],%2
v_subrev_u32 v0,128,v0
Mvav 132,252,212
s_add_u32 m0,s77,118784
EX 149
Mvav 136,252,216
EX 150
Mvvv 228,248,160
s_nop 0
Ta 240,8,49664
Ta 242,8,53760
Mvvv 232,248,164
LD 0,24,48,0
Mvvv 236,248,168
s_add_u32 m0,s77,122880
EX 151
Mvvv 240,248,172
CV 148,148,149
CV 149,150,151
Mvvv 244,248,176
WL 2
EX 152
Mavv 0,220,160
s_nop 0
Ta 244,252,49664
Ta 246,252,53760
Mavv 4,220,164
LD 0,24,50,0
Mavv 8,220,168
s_add_u32 m0,s77,126976
EX 153
Mavv 12,220,172
EX 154
Mavv 16,220,176
EX 155
Mavv 20,224,160
s_nop 0
Ta 248,8,50176
Ta 250,8,54272
Mavv 24,224,164
LD 0,24,52,0
Mavv 28,224,168
s_add_u32 m0,s77,131072
CV 152,152,153
CV 153,154,155
Mavv 32,224,172
EX 156
Mavv 36,224,176
WL 2
EX 157
Maav 40,240,160
s_nop 0
Ta 252,252,50176
Ta 254,252,54272
Maav 44,240,164
LD 0,24,54,0
Maav 48,240,168
s_add_u32 m0,s77,135168
EX 158
Maav 52,240,172
EX 159
Maav 56,240,176
CV 156,156,157
CV 157,158,159
Maav 60,244,160
s_nop 0
Tv 220,8,50688
Tv 222,8,54784
Maav 64,244,164
LD 0,24,56,0
Maav 68,244,168
Maav 72,244,172
Maav 76,244,176
WL 2
Tv 224,252,50688
Tv 226,252,54784
Maav 80,248,160
Maav 84,248,164
Maav 88,248,168
Maav 92,248,172
Maav 96,248,176
Maav 100,252,160
Ra 240,6,18432
Maav 104,252,164
Ra 244,7,18432
Maav 108,252,168
Ra 248,6,19456
Maav 112,252,172
Maav 116,252,176
s_waitcnt vmcnt(12) lgkmcnt(3)
s_barrier
Mavv 120,220,160
s_add_u32 m0,s76,98304
Ra 252,7,19456
)ASM"
R"ASM(Mavv 124,220,164
s_cmp_eq_u32 s88,6
s_cselect_b32 s86,0x20000,0
v_subrev_u32 v2,s86,v2
Mavv 128,220,168
v_add_u32 v2,8192,v2
Mavv 132,220,172
LN 2,96,62,0
Mavv 136,220,176
Mavv 140,224,160
Mavv 144,224,164
Mavv 148,224,168
LN 2,96,63,1024
Mavv 152,224,172
Mavv 156,224,176
WL 0
Nvaa 160,240,160,80
Mvaa 160,244,180
LN 2,96,62,2048
Mvaa 160,248,200
Mvaa 160,252,220
Nvaa 164,240,164,84
Mvaa 164,244,184
s_nop 0
Tv 220,9,17408
Tv 222,10,17664
Mvaa 164,248,204
LN 2,96,63,3072
Mvaa 164,252,224
EX 160
Nvaa 168,240,168,88
EX 161
Mvaa 168,244,188
EX 162
Mvaa 168,248,208
s_nop 0
Tv 224,9,17416
Tv 226,10,17672
Mvaa 168,252,228
EX 163
Nvaa 172,240,172,92
CV 142,160,161
CV 143,162,163
Mvaa 172,244,192
EX 164
Mvaa 172,248,212
EX 165
Mvaa 172,252,232
EX 166
Nvaa 176,240,176,96
s_nop 0
Ta 240,9,17920
Ta 242,10,18176
Mvaa 176,244,196
EX 167
Mvaa 176,248,216
CV 146,164,165
CV 147,166,167
Mvaa 176,252,236
WL 2
Ta 244,9,17928
Ta 246,10,18184
Mvvv 20,220,180
EX 168
Mvvv 24,220,184
EX 169
Mvvv 28,220,188
EX 170
Mvvv 32,220,192
EX 171
Mvvv 36,220,196
CV 150,168,169
CV 151,170,171
Mvvv 100,224,180
EX 172
Mvvv 104,224,184
EX 173
Mvvv 108,224,188
EX 174
Mvvv 112,224,192
EX 175
Mvvv 116,224,196
s_waitcnt vmcnt(9) lgkmcnt(0)
s_barrier
Mvav 40,240,180
s_add_u32 m0,s76,50176
Ra 248,6,32768
Ra 252,7,32768
Mvav 44,240,184
Rv 220,6,33792
v_subrev_u32 v3,262144,v3
Mvav 48,240,188
Rv 224,7,33792
CV 154,172,173
Mvav 52,240,192
LD 3,28,64,0
Mvav 56,240,196
s_nop 0
Rv 200,12,40960
Rv 204,12,43008
Mvav 120,244,180
Rv 208,12,45056
CV 155,174,175
Mvav 124,244,184
Rv 212,12,47104
Mvav 128,244,188
LD 3,28,66,2048
Mvav 132,244,192
s_add_u32 m0,s76,16384
s_cmp_eq_u32 s88,7
s_cselect_b32 s86,0x20000,0
Mvav 136,244,196
WL 5
v_subrev_u32 v1,s86,v1
v_add_u32 v1,8192,v1
Nvaa 160,248,160,80
Rv 216,12,49152
Mvaa 160,252,180
LN 1,36,58,0
Mvva 160,220,200
WL 3
EX 176
Mvva 160,224,220
EX 177
Nvaa 164,248,164,84
EX 178
Mvaa 164,252,184
s_nop 0
Ta 240,9,16384
Ta 242,10,16640
Mvva 164,220,204
LN 1,36,59,1024
Mvva 164,224,224
EX 179
Nvaa 168,248,168,88
CV 158,176,177
CV 159,178,179
Mvaa 168,252,188
EX 160
Mvva 168,220,208
s_nop 0
Ta 244,9,16392
Ta 246,10,16648
Mvva 168,224,228
LN 1,36,60,2048
Nvaa 172,248,172,92
EX 161
Mvaa 172,252,192
EX 162
Mvva 172,220,212
EX 163
Mvva 172,224,232
LN 1,36,61,3072
Nvaa 176,248,176,96
s_nop 0
Ta 248,9,16896
Ta 250,10,17152
Mvaa 176,252,196
CV 160,160,161
CV 161,162,163
Mvva 176,220,216
EX 164
Mvva 176,224,236
WL 2
Ta 252,9,16904
Ta 254,10,17160
Mvav 20,240,200
EX 165
Mvav 24,240,204
EX 166
Mvav 28,240,208
EX 167
Mvav 32,240,212
CV 164,164,165
CV 165,166,167
Mvav 36,240,216
EX 168
Mvav 100,244,200
EX 169
Mvav 104,244,204
EX 170
Mvav 108,244,208
EX 171
Mvav 112,244,212
CV 168,168,169
CV 169,170,171
Mvav 116,244,216
WL 0
s_barrier
Mvav 40,248,200
s_add_u32 m0,s76,49152
Tv 220,8,32768
Tv 222,8,36864
Mvav 44,248,204
s_cmp_le_u32 s89,0
s_cselect_b64 s[28:29],s[70:71],%3
v_subrev_u32 v3,262144,v3
Mvav 48,248,208
LD 3,28,64,0
Mvav 52,248,212
EX 172
Mvav 56,248,216
EX 173
Mvav 120,252,200
s_nop 0
Tv 224,252,32768
Tv 226,252,36864
Mvav 124,252,204
LD 3,28,66,2048
Mvav 128,252,208
EX 174
Mvav 132,252,212
EX 175
Mvav 136,252,216
CV 172,172,173
CV 173,174,175
Mvvv 228,248,140
s_nop 0
Ta 240,8,33280
Ta 242,8,37376
Mvvv 232,248,144
EX 176
Mvvv 236,248,148
EX 177
Mvvv 240,248,152
EX 178
Mvvv 244,248,156
WL 2
EX 179
Mavv 0,220,140
s_nop 0
Ta 244,252,33280
Ta 246,252,37376
Mavv 4,220,144
CV 176,176,177
CV 177,178,179
Mavv 8,220,148
Mavv 12,220,152
Mavv 16,220,156
Mavv 20,224,140
s_nop 0
Ta 248,8,33792
Ta 250,8,37888
Mavv 24,224,144
Mavv 28,224,148
Mavv 32,224,152
Mavv 36,224,156
WL 2
Maav 40,240,140
s_nop 0
Ta 252,252,33792
Ta 254,252,37888
Maav 44,240,144
Maav 48,240,148
Maav 52,240,152
Maav 56,240,156
Maav 60,244,140
s_nop 0
Tv 220,8,34304
Tv 222,8,38400
Maav 64,244,144
Maav 68,244,148
Maav 72,244,152
Maav 76,244,156
WL 2
Tv 224,252,34304
Tv 226,252,38400
Maav 80,248,140
s_nop 0
Rv 180,11,40960
Rv 184,11,43008
Maav 84,248,144
Rv 188,11,45056
Maav 88,248,148
Rv 192,11,47104
Maav 92,248,152
Rv 196,11,49152
Maav 96,248,156
Maav 100,252,140
Ra 240,6,34816
Maav 104,252,144
Ra 244,7,34816
Maav 108,252,148
Ra 248,6,35840
Maav 112,252,152
Maav 116,252,156
s_waitcnt vmcnt(6) lgkmcnt(5)
s_barrier
Mavv 120,220,140
s_add_u32 m0,s76,81920
Ra 252,7,35840
Mavv 124,220,144
s_cmp_eq_u32 s88,7
s_cselect_b32 s86,0x20000,0
v_subrev_u32 v2,s86,v2
Mavv 128,220,148
v_add_u32 v2,8192,v2
Mavv 132,220,152
LN 2,96,62,0
Mavv 136,220,156
Mavv 140,224,140
Mavv 144,224,144
Mavv 148,224,148
LN 2,96,63,1024
Mavv 152,224,152
Mavv 156,224,156
WL 0
Nvaa 140,240,160,80
Mvaa 140,244,180
LN 2,96,62,2048
Mvaa 140,248,200
Mvaa 140,252,220
Nvaa 144,240,164,84
Mvaa 144,244,184
s_nop 0
Tv 220,9,17408
Tv 222,10,17664
Mvaa 144,248,204
LN 2,96,63,3072
Mvaa 144,252,224
EX 140
Nvaa 148,240,168,88
EX 141
Mvaa 148,244,188
EX 142
Mvaa 148,248,208
s_nop 0
Tv 224,9,17416
Tv 226,10,17672
Mvaa 148,252,228
EX 143
Nvaa 152,240,172,92
CV 162,140,141
CV 163,142,143
Mvaa 152,244,192
EX 144
Mvaa 152,248,212
EX 145
Mvaa 152,252,232
EX 146
Nvaa 156,240,176,96
s_nop 0
Ta 240,9,17920
Ta 242,10,18176
Mvaa 156,244,196
EX 147
Mvaa 156,248,216
CV 166,144,145
CV 167,146,147
Mvaa 156,252,236
WL 2
Ta 244,9,17928
Ta 246,10,18184
Mvvv 20,220,180
EX 148
Mvvv 24,220,184
EX 149
Mvvv 28,220,188
EX 150
Mvvv 32,220,192
EX 151
Mvvv 36,220,196
CV 170,148,149
CV 171,150,151
Mvvv 100,224,180
EX 152
Mvvv 104,224,184
EX 153
Mvvv 108,224,188
EX 154
Mvvv 112,224,192
EX 155
Mvvv 116,224,196
s_waitcnt vmcnt(4) lgkmcnt(0)
s_barrier
Mvav 40,240,180
s_add_u32 m0,s76,50176
Ra 248,6,0
Ra 252,7,0
Mvav 44,240,184
Rv 220,6,1024
v_subrev_u32 v3,262144,v3
Mvav 48,240,188
Rv 224,7,1024
CV 174,152,153
Mvav 52,240,192
LD 3,28,64,0
Mvav 56,240,196
s_nop 0
Rv 200,12,20480
Rv 204,12,22528
Mvav 120,244,180
Rv 208,12,24576
CV 175,154,155
Mvav 124,244,184
Rv 212,12,26624
Mvav 128,244,188
LD 3,28,66,2048
Mvav 132,244,192
s_add_u32 m0,s76,32768
s_cmp_eq_u32 s88,8
s_cselect_b32 s86,0x20000,0
Mvav 136,244,196
WL 5
v_subrev_u32 v1,s86,v1
v_add_u32 v1,8192,v1
Nvaa 140,248,160,80
Rv 216,12,28672
Mvaa 140,252,180
LN 1,36,58,0
Mvva 140,220,200
WL 3
EX 156
Mvva 140,224,220
EX 157
Nvaa 144,248,164,84
EX 158
Mvaa 144,252,184
s_nop 0
Ta 240,9,16384
Ta 242,10,16640
Mvva 144,220,204
LN 1,36,59,1024
Mvva 144,224,224
EX 159
Nvaa 148,248,168,88
CV 178,156,157
CV 179,158,159
Mvaa 148,252,188
EX 140
Mvva 148,220,208
s_nop 0
Ta 244,9,16392
Ta 246,10,16648
Mvva 148,224,228
LN 1,36,60,2048
Nvaa 152,248,172,92
EX 141
Mvaa 152,252,192
EX 142
Mvva 152,220,212
EX 143
Mvva 152,224,232
LN 1,36,61,3072
Nvaa 156,248,176,96
s_nop 0
Ta 248,9,16896
Ta 250,10,17152
Mvaa 156,252,196
s_and_b32 s86,%12,458752
s_cmp_eq_u32 s86,65536
s_cselect_b32 s86,s91,0x40000000
Mvva 156,220,216
s_add_u32 s86,s86,36864
CV 140,140,141
CV 141,142,143
Mvva 156,224,236
WL 2
Ta 252,9,16904
Ta 254,10,17160
Mvav 20,240,200
buffer_load_dword v255,v17,s[92:95],s86 offen
Mvav 24,240,204
EX 144
Mvav 28,240,208
EX 145
Mvav 32,240,212
EX 146
Mvav 36,240,216
EX 147
Mvav 100,244,200
CV 144,144,145
CV 145,146,147
Mvav 104,244,204
EX 148
Mvav 108,244,208
EX 149
Mvav 112,244,212
EX 150
Mvav 116,244,216
WL 0
s_barrier
Mvav 40,248,200
s_add_u32 m0,s76,49152
Tv 220,8,16384
Tv 222,8,20480
Mvav 44,248,204
v_add_u32 v3,128,v3
Mvav 48,248,208
LD 3,28,64,0
Mvav 52,248,212
EX 151
Mvav 56,248,216
CV 148,148,149
CV 149,150,151
Mvav 120,252,200
s_nop 0
Tv 224,252,16384
Tv 226,252,20480
Mvav 124,252,204
LD 3,28,66,2048
Mvav 128,252,208
EX 152
Mvav 132,252,212
EX 153
Mvav 136,252,216
EX 154
Mvvv 228,248,160
s_nop 0
Ta 240,8,16896
Ta 242,8,20992
Mvvv 232,248,164
EX 155
Mvvv 236,248,168
CV 152,152,153
CV 153,154,155
Mvvv 240,248,172
EX 156
Mvvv 244,248,176
WL 2
EX 157
Mavv 0,220,160
s_nop 0
Ta 244,252,16896
Ta 246,252,20992
Mavv 4,220,164
EX 158
Mavv 8,220,168
EX 159
Mavv 12,220,172
CV 156,156,157
CV 157,158,159
Mavv 16,220,176
Mavv 20,224,160
s_nop 0
Ta 248,8,17408
Ta 250,8,21504
Mavv 24,224,164
Mavv 28,224,168
Mavv 32,224,172
Mavv 36,224,176
WL 2
Maav 40,240,160
s_nop 0
Ta 252,252,17408
Ta 254,252,21504
Maav 44,240,164
Maav 48,240,168
Maav 52,240,172
Maav 56,240,176
Maav 60,244,160
s_nop 0
Tv 220,8,17920
Tv 222,8,22016
Maav 64,244,164
Maav 68,244,168
Maav 72,244,172
Maav 76,244,176
WL 2
Tv 224,252,17920
Tv 226,252,22016
Maav 80,248,160
s_nop 0
Rv 180,11,20480
Rv 184,11,22528
Maav 84,248,164
Rv 188,11,24576
Maav 88,248,168
Rv 192,11,26624
Maav 92,248,172
Rv 196,11,28672
Maav 96,248,176
Maav 100,252,160
Ra 240,6,2048
Maav 104,252,164
Ra 244,7,2048
Maav 108,252,168
Ra 248,6,3072
Maav 112,252,172
Maav 116,252,176
s_waitcnt vmcnt(7) lgkmcnt(5)
s_barrier
Mavv 120,220,160
s_add_u32 m0,s76,65536
Ra 252,7,3072
Mavv 124,220,164
s_cmp_eq_u32 s88,8
s_cselect_b32 s86,0x20000,0
v_subrev_u32 v2,s86,v2
Mavv 128,220,168
v_add_u32 v2,8192,v2
Mavv 132,220,172
LN 2,96,62,0
Mavv 136,220,176
Mavv 140,224,160
Mavv 144,224,164
Mavv 148,224,168
LN 2,96,63,1024
Mavv 152,224,172
Mavv 156,224,176
WL 0
Nvaa 160,240,160,80
Mvaa 160,244,180
LN 2,96,62,2048
Mvaa 160,248,200
Mvaa 160,252,220
Nvaa 164,240,164,84
Mvaa 164,244,184
s_nop 0
Tv 220,9,17408
Tv 222,10,17664
Mvaa 164,248,204
LN 2,96,63,3072
Mvaa 164,252,224
EX 160
Nvaa 168,240,168,88
EX 161
Mvaa 168,244,188
EX 162
Mvaa 168,248,208
s_nop 0
Tv 224,9,17416
Tv 226,10,17672
Mvaa 168,252,228
EX 163
Nvaa 172,240,172,92
CV 142,160,161
CV 143,162,163
Mvaa 172,244,192
EX 164
Mvaa 172,248,212
EX 165
Mvaa 172,252,232
EX 166
Nvaa 176,240,176,96
s_nop 0
Ta 240,9,17920
Ta 242,10,18176
Mvaa 176,244,196
EX 167
Mvaa 176,248,216
CV 146,164,165
CV 147,166,167
Mvaa 176,252,236
WL 2
Ta 244,9,17928
Ta 246,10,18184
Mvvv 20,220,180
EX 168
Mvvv 24,220,184
EX 169
Mvvv 28,220,188
EX 170
Mvvv 32,220,192
EX 171
Mvvv 36,220,196
CV 150,168,169
CV 151,170,171
Mvvv 100,224,180
EX 172
Mvvv 104,224,184
EX 173
Mvvv 108,224,188
EX 174
Mvvv 112,224,192
EX 175
Mvvv 116,224,196
WL 0
s_barrier
Mvav 40,240,180
s_add_u32 m0,s76,50176
Ra 248,6,16384
Ra 252,7,16384
Mvav 44,240,184
s_nop 0
Rv 220,6,17408
v_add_u32 v3,262144,v3
Mvav 48,240,188
Rv 224,7,17408
CV 154,172,173
Mvav 52,240,192
LD 3,28,64,0
Mvav 56,240,196
CV 155,174,175
v_mul_f32 v20,s85,v20
Mvav 120,244,180
EX 176
Mvav 124,244,184
EX 177
Mvav 128,244,188
LD 3,28,66,2048
Mvav 132,244,192
s_add_u32 m0,s76,0
s_cmp_eq_u32 s88,9
s_cselect_b32 s86,0x20000,0
Mvav 136,244,196
WL 1
v_subrev_u32 v1,s86,v1
v_add_u32 v1,8192,v1
Nvaa 160,248,160,80
EX 178
Mvaa 160,252,180
LN 1,36,58,0
Mvva 160,220,200
WL 0
EX 179
Mvva 160,224,220
CV 158,176,177
CV 159,178,179
Nvaa 164,248,164,84
v_mul_f32 v21,s85,v21
v_mul_f32 v22,s85,v22
Mvaa 164,252,184
LN 1,36,59,1024
Mvva 164,220,204
v_mul_f32 v23,s85,v23
v_mul_f32 v100,s85,v100
Mvva 164,224,224
v_mul_f32 v101,s85,v101
v_mul_f32 v102,s85,v102
Nvaa 168,248,168,88
v_mul_f32 v103,s85,v103
CV 20,20,21
Mvaa 168,252,188
s_barrier
Mvva 168,220,208
s_nop 0
Ta 240,8,49152
Ta 242,8,53248
Mvva 168,224,228
LN 1,36,60,2048
Nvaa 172,248,172,92
CV 21,22,23
CV 22,100,101
Mvaa 172,252,192
CV 23,102,103
v_mul_f32 v24,s85,v24
Mvva 172,220,212
v_mul_f32 v25,s85,v25
v_mul_f32 v26,s85,v26
Mvva 172,224,232
s_nop 0
Ta 244,252,49152
Ta 246,252,53248
Nvaa 176,248,176,96
LN 1,36,61,3072
Mvaa 176,252,196
v_mul_f32 v27,s85,v27
v_mul_f32 v104,s85,v104
Mvva 176,220,216
v_mul_f32 v105,s85,v105
v_mul_f32 v106,s85,v106
Mvva 176,224,236
v_mul_f32 v107,s85,v107
CV 24,24,25
Mvvv 228,248,140
s_nop 0
Ta 248,8,49664
Ta 250,8,53760
Mvvv 232,248,144
CV 25,26,27
CV 26,104,105
Mvvv 236,248,148
CV 27,106,107
v_mul_f32 v28,s85,v28
Mvvv 240,248,152
v_mul_f32 v29,s85,v29
v_mul_f32 v30,s85,v30
Mvvv 244,248,156
WL 2
v_mul_f32 v31,s85,v31
v_mul_f32 v108,s85,v108
Maav 0,240,140
s_nop 0
Ta 252,252,49664
Ta 254,252,53760
Maav 4,240,144
v_mul_f32 v109,s85,v109
v_mul_f32 v110,s85,v110
Maav 8,240,148
v_mul_f32 v111,s85,v111
CV 28,28,29
Maav 12,240,152
CV 29,30,31
CV 30,108,109
Maav 16,240,156
CV 31,110,111
v_mul_f32 v32,s85,v32
Maav 20,244,140
s_nop 0
Tv 220,8,50176
Tv 222,8,54272
Maav 24,244,144
v_mul_f32 v33,s85,v33
v_mul_f32 v34,s85,v34
Maav 28,244,148
v_mul_f32 v35,s85,v35
v_mul_f32 v112,s85,v112
Maav 32,244,152
v_mul_f32 v113,s85,v113
v_mul_f32 v114,s85,v114
Maav 36,244,156
WL 2
v_mul_f32 v115,s85,v115
CV 32,32,33
Maav 40,248,140
s_nop 0
Tv 224,252,50176
Tv 226,252,54272
Maav 44,248,144
CV 33,34,35
CV 34,112,113
Maav 48,248,148
CV 35,114,115
v_mul_f32 v36,s85,v36
Maav 52,248,152
v_mul_f32 v37,s85,v37
v_mul_f32 v38,s85,v38
Maav 56,248,156
v_mul_f32 v39,s85,v39
v_mul_f32 v116,s85,v116
Maav 60,252,140
s_nop 0
Ta 240,8,50688
Ta 242,8,54784
Maav 64,252,144
v_mul_f32 v117,s85,v117
v_mul_f32 v118,s85,v118
Maav 68,252,148
v_mul_f32 v119,s85,v119
CV 36,36,37
Maav 72,252,152
CV 37,38,39
CV 38,116,117
Maav 76,252,156
WL 2
Ta 244,252,50688
Ta 246,252,54784
Mavv 80,220,140
CV 39,118,119
v_mul_f32 v40,s85,v40
Mavv 84,220,144
v_mul_f32 v41,s85,v41
v_mul_f32 v42,s85,v42
Mavv 88,220,148
v_mul_f32 v43,s85,v43
v_mul_f32 v120,s85,v120
Mavv 92,220,152
v_mul_f32 v121,s85,v121
v_mul_f32 v122,s85,v122
Mavv 96,220,156
v_mul_f32 v123,s85,v123
CV 40,40,41
Mavv 100,224,140
Ra 248,6,18432
CV 41,42,43
Mavv 104,224,144
Ra 252,7,18432
CV 42,120,121
Mavv 108,224,148
Rv 220,6,19456
CV 43,122,123
Mavv 112,224,152
v_mul_f32 v44,s85,v44
v_mul_f32 v45,s85,v45
Mavv 116,224,156
s_waitcnt vmcnt(10) lgkmcnt(3)
s_barrier
Maav 120,240,140
s_add_u32 m0,s76,98304
Rv 224,7,19456
v_mul_f32 v46,s85,v46
Maav 124,240,144
s_cmp_eq_u32 s88,9
s_cselect_b32 s86,0x20000,0
v_subrev_u32 v2,s86,v2
Maav 128,240,148
v_add_u32 v2,8192,v2
v_mul_f32 v47,s85,v47
Maav 132,240,152
LN 2,96,62,0
Maav 136,240,156
v_mul_f32 v124,s85,v124
v_mul_f32 v125,s85,v125
Maav 140,244,140
v_mul_f32 v126,s85,v126
v_mul_f32 v127,s85,v127
Maav 144,244,144
CV 44,44,45
CV 45,46,47
Maav 148,244,148
LN 2,96,63,1024
Maav 152,244,152
CV 46,124,125
CV 47,126,127
Maav 156,244,156
WL 0
v_mul_f32 v48,s85,v48
v_mul_f32 v49,s85,v49
Nvaa 140,248,160,80
v_mul_f32 v50,s85,v50
v_mul_f32 v51,s85,v51
Mvaa 140,252,180
LN 2,96,62,2048
Mvva 140,220,200
v_mul_f32 v128,s85,v128
v_mul_f32 v129,s85,v129
Mvva 140,224,220
v_mul_f32 v130,s85,v130
v_mul_f32 v131,s85,v131
Nvaa 144,248,164,84
CV 48,48,49
CV 49,50,51
Mvaa 144,252,184
s_nop 0
Ta 240,9,16384
Ta 242,10,16640
Mvva 144,220,204
LN 2,96,63,3072
Mvva 144,224,224
CV 50,128,129
CV 51,130,131
Nvaa 148,248,168,88
v_mul_f32 v52,s85,v52
v_mul_f32 v53,s85,v53
Mvaa 148,252,188
v_mul_f32 v54,s85,v54
v_mul_f32 v55,s85,v55
Mvva 148,220,208
s_nop 0
Ta 244,9,16896
Ta 246,10,17152
Mvva 148,224,228
v_mul_f32 v132,s85,v132
v_mul_f32 v133,s85,v133
Nvaa 152,248,172,92
v_mul_f32 v134,s85,v134
v_mul_f32 v135,s85,v135
Mvaa 152,252,192
CV 52,52,53
CV 53,54,55
Mvva 152,220,212
CV 54,132,133
CV 55,134,135
Mvva 152,224,232
v_mul_f32 v56,s85,v56
v_mul_f32 v57,s85,v57
Nvaa 156,248,176,96
s_nop 0
Ta 248,9,16392
Ta 250,10,16648
Mvaa 156,252,196
v_mul_f32 v58,s85,v58
v_mul_f32 v59,s85,v59
Mvva 156,220,216
v_mul_f32 v136,s85,v136
v_mul_f32 v137,s85,v137
Mvva 156,224,236
WL 2
Ta 252,9,16904
Ta 254,10,17160
Zvav 60,240,180
v_mul_f32 v138,s85,v138
v_mul_f32 v139,s85,v139
Zvav 64,240,184
CV 56,56,57
CV 57,58,59
Zvav 68,240,188
CV 58,136,137
CV 59,138,139
Zvav 72,240,192
EX 160
Zvav 76,240,196
EX 161
Zvav 200,244,180
EX 162
Zvav 204,244,184
EX 163
Zvav 208,244,188
CV 160,160,161
CV 161,162,163
Zvav 212,244,192
EX 164
Zvav 216,244,196
s_waitcnt vmcnt(8) lgkmcnt(0)
s_barrier
Zvav 100,248,180
s_add_u32 m0,s76,49152
Rv 220,6,32768
Rv 224,7,32768
Zvav 104,248,184
s_cmp_le_u32 s89,1
s_cselect_b64 s[28:29],s[70:71],%3
Ra 240,6,33792
Zvav 108,248,188
Ra 244,7,33792
v_add_u32 v3,262144,v3
Zvav 112,248,192
LD 3,28,64,0
Zvav 116,248,196
EX 165
Zvav 120,252,180
EX 166
Zvav 124,252,184
EX 167
Zvav 128,252,188
LD 3,28,66,2048
Zvav 132,252,192
CV 164,164,165
CV 165,166,167
EX 168
EX 169
EX 170
EX 171
CV 168,168,169
CV 169,170,171
EX 172
EX 173
EX 174
EX 175
CV 172,172,173
CV 173,174,175
EX 176
EX 177
EX 178
EX 179
CV 176,176,177
CV 177,178,179
EX 140
EX 141
EX 142
EX 143
CV 162,140,141
CV 163,142,143
Zvav 136,252,196
WL 1
s_add_u32 m0,s76,16384
Rv 180,12,20480
Nvva 140,220,160,80
s_cmp_eq_u32 s88,10
s_cselect_b32 s86,0x20000,0
Mvva 140,224,180
v_subrev_u32 v1,s86,v1
v_add_u32 v1,8192,v1
Mvaa 140,240,200
EX 144
EX 145
EX 146
EX 147
CV 166,144,145
CV 167,146,147
WL 1
Mvaa 140,244,220
LN 1,36,58,0
Nvva 144,220,164,84
s_nop 0
Rv 184,12,22528
Rv 188,12,24576
Mvva 144,224,184
Ta 248,9,17408
Mvaa 144,240,204
EX 148
EX 149
EX 150
EX 151
CV 170,148,149
CV 171,150,151
Mvaa 144,244,224
s_nop 0
Ta 250,10,17664
Rv 192,12,26624
Nvva 148,220,168,88
LN 1,36,59,1024
Mvva 148,224,188
Rv 196,12,28672
Mvaa 148,240,208
EX 152
EX 153
EX 154
EX 155
CV 174,152,153
CV 175,154,155
Mvaa 148,244,228
s_nop 0
Ta 252,9,17920
Ta 254,10,18176
Nvva 152,220,172,92
LN 1,36,60,2048
Mvva 152,224,192
EX 156
Mvaa 152,240,212
EX 157
EX 158
EX 159
CV 178,156,157
CV 179,158,159
Mvaa 152,244,232
EX 140
Nvva 156,220,176,96
s_nop 0
Tv 220,9,17416
Tv 222,10,17672
Mvva 156,224,196
LN 1,36,61,3072
Mvaa 156,240,216
s_and_b32 s86,%12,458752
s_cmp_eq_u32 s86,131072
s_cselect_b32 s86,s91,0x40000000
Mvaa 156,244,236
WL 2
Tv 224,9,17928
Tv 226,10,18184
Mvav 60,248,180
s_add_u32 s86,s86,40960
EX 141
Mvav 64,248,184
buffer_load_dword v255,v17,s[92:95],s86 offen
Mvav 68,248,188
EX 142
Mvav 72,248,192
EX 143
Mvav 76,248,196
CV 140,140,141
CV 141,142,143
Mvav 200,252,180
EX 144
Mvav 204,252,184
EX 145
Mvav 208,252,188
EX 146
Mvav 212,252,192
EX 147
Mvav 216,252,196
WL 0
s_barrier
Mvvv 100,220,180
s_add_u32 m0,s76,50176
Ta 240,8,32768
Ta 242,8,36864
Mvvv 104,220,184
v_add_u32 v3,262144,v3
CV 144,144,145
Mvvv 108,220,188
LD 3,28,64,0
Mvvv 112,220,192
CV 145,146,147
Mvvv 116,220,196
EX 148
Mvvv 120,224,180
s_nop 0
Ta 244,252,32768
Ta 246,252,36864
Mvvv 124,224,184
LD 3,28,66,2048
Mvvv 128,224,188
s_cmp_le_u32 s89,2
s_cselect_b64 s[24:25],s[68:69],%2
v_add_u32 v0,256,v0
Mvvv 132,224,192
s_add_u32 m0,s77,118784
EX 149
Mvvv 136,224,196
EX 150
Mvvv 228,248,160
s_nop 0
Ta 248,8,33280
Ta 250,8,37376
Mvvv 232,248,164
LD 0,24,48,0
Mvvv 236,248,168
s_add_u32 m0,s77,122880
EX 151
Mvvv 240,248,172
CV 148,148,149
CV 149,150,151
Mvvv 244,248,176
WL 2
EX 152
Maav 0,240,160
s_nop 0
Ta 252,252,33280
Ta 254,252,37376
Maav 4,240,164
LD 0,24,50,0
Maav 8,240,168
s_add_u32 m0,s77,126976
EX 153
Maav 12,240,172
EX 154
Maav 16,240,176
EX 155
Maav 20,244,160
s_nop 0
Tv 220,8,33792
Tv 222,8,37888
Maav 24,244,164
LD 0,24,52,0
Maav 28,244,168
s_add_u32 m0,s77,131072
CV 152,152,153
CV 153,154,155
Maav 32,244,172
EX 156
Maav 36,244,176
WL 2
EX 157
Maav 40,248,160
s_nop 0
Tv 224,252,33792
Tv 226,252,37888
Maav 44,248,164
LD 0,24,54,0
Maav 48,248,168
s_add_u32 m0,s77,135168
EX 158
Maav 52,248,172
EX 159
Maav 56,248,176
CV 156,156,157
CV 157,158,159
Maav 60,252,160
s_nop 0
Ta 240,8,34304
Ta 242,8,38400
Maav 64,252,164
LD 0,24,56,0
Maav 68,252,168
Maav 72,252,172
Maav 76,252,176
WL 2
Ta 244,252,34304
Ta 246,252,38400
Mavv 80,220,160
s_nop 0
Rv 180,11,40960
Rv 184,11,43008
Mavv 84,220,164
Rv 188,11,45056
Mavv 88,220,168
Rv 192,11,47104
Mavv 92,220,172
Rv 196,11,49152
Mavv 96,220,176
Mavv 100,224,160
Ra 248,6,34816
Mavv 104,224,164
Ra 252,7,34816
Mavv 108,224,168
Rv 220,6,35840
Mavv 112,224,172
Mavv 116,224,176
s_waitcnt vmcnt(12) lgkmcnt(5)
s_barrier
Maav 120,240,160
s_add_u32 m0,s76,81920
Rv 224,7,35840
Maav 124,240,164
s_cmp_eq_u32 s88,10
s_cselect_b32 s86,0x20000,0
v_subrev_u32 v2,s86,v2
Maav 128,240,168
v_add_u32 v2,8192,v2
Maav 132,240,172
LN 2,96,62,0
Maav 136,240,176
Maav 140,244,160
Maav 144,244,164
Maav 148,244,168
LN 2,96,63,1024
Maav 152,244,172
Maav 156,244,176
WL 0
Nvaa 160,248,160,80
Mvaa 160,252,180
LN 2,96,62,2048
Mvva 160,220,200
Mvva 160,224,220
Nvaa 164,248,164,84
Mvaa 164,252,184
s_nop 0
Ta 240,9,16384
Ta 242,10,16640
Mvva 164,220,204
LN 2,96,63,3072
Mvva 164,224,224
EX 160
Nvaa 168,248,168,88
EX 161
Mvaa 168,252,188
EX 162
Mvva 168,220,208
s_nop 0
Ta 244,9,16896
Ta 246,10,17152
Mvva 168,224,228
EX 163
Nvaa 172,248,172,92
CV 142,160,161
CV 143,162,163
Mvaa 172,252,192
EX 164
Mvva 172,220,212
EX 165
Mvva 172,224,232
EX 166
Nvaa 176,248,176,96
s_nop 0
Ta 248,9,16392
Ta 250,10,16648
Mvaa 176,252,196
EX 167
Mvva 176,220,216
CV 146,164,165
CV 147,166,167
Mvva 176,224,236
WL 2
Ta 252,9,16904
Ta 254,10,17160
Mvav 60,240,180
EX 168
Mvav 64,240,184
EX 169
Mvav 68,240,188
EX 170
Mvav 72,240,192
EX 171
Mvav 76,240,196
CV 150,168,169
CV 151,170,171
Mvav 200,244,180
EX 172
Mvav 204,244,184
EX 173
Mvav 208,244,188
EX 174
Mvav 212,244,192
EX 175
Mvav 216,244,196
s_waitcnt vmcnt(9) lgkmcnt(0)
s_barrier
Mvav 100,248,180
s_add_u32 m0,s76,49152
Rv 220,6,0
Rv 224,7,0
Mvav 104,248,184
s_cmp_le_u32 s89,2
s_cselect_b64 s[28:29],s[70:71],%3
Ra 240,6,1024
Mvav 108,248,188
Ra 244,7,1024
v_add_u32 v3,262144,v3
Mvav 112,248,192
LD 3,28,64,0
Mvav 116,248,196
CV 154,172,173
CV 155,174,175
Mvav 120,252,180
EX 176
Mvav 124,252,184
EX 177
Mvav 128,252,188
LD 3,28,66,2048
Mvav 132,252,192
s_add_u32 m0,s76,32768
s_cmp_eq_u32 s88,11
s_cselect_b32 s86,0x20000,0
Mvav 136,252,196
WL 1
v_subrev_u32 v1,s86,v1
v_add_u32 v1,8192,v1
Nvva 160,220,160,80
s_nop 0
Rv 180,12,40960
Rv 184,12,43008
Mvva 160,224,180
LN 1,36,58,0
Mvaa 160,240,200
WL 2
Rv 188,12,45056
Rv 192,12,47104
Mvaa 160,244,220
Rv 196,12,49152
Nvva 164,220,164,84
EX 178
Mvva 164,224,184
s_nop 0
Ta 248,9,17408
Ta 250,10,17664
Mvaa 164,240,204
LN 1,36,59,1024
Mvaa 164,244,224
EX 179
Nvva 168,220,168,88
CV 158,176,177
CV 159,178,179
Mvva 168,224,188
EX 160
Mvaa 168,240,208
s_nop 0
Ta 252,9,17920
Ta 254,10,18176
Mvaa 168,244,228
LN 1,36,60,2048
Nvva 172,220,172,92
EX 161
Mvva 172,224,192
EX 162
Mvaa 172,240,212
EX 163
Mvaa 172,244,232
LN 1,36,61,3072
Nvva 176,220,176,96
s_nop 0
Tv 220,9,17416
Tv 222,10,17672
Mvva 176,224,196
CV 160,160,161
CV 161,162,163
Mvaa 176,240,216
EX 164
Mvaa 176,244,236
WL 2
Tv 224,9,17928
Tv 226,10,18184
Mvav 60,248,180
EX 165
Mvav 64,248,184
EX 166
Mvav 68,248,188
EX 167
Mvav 72,248,192
CV 164,164,165
CV 165,166,167
Mvav 76,248,196
EX 168
Mvav 200,252,180
EX 169
Mvav 204,252,184
EX 170
Mvav 208,252,188
EX 171
Mvav 212,252,192
CV 168,168,169
CV 169,170,171
Mvav 216,252,196
WL 0
s_barrier
Mvvv 100,220,180
s_add_u32 m0,s76,50176
Ta 240,8,16384
Ta 242,8,20480
Mvvv 104,220,184
v_add_u32 v3,262144,v3
Mvvv 108,220,188
LD 3,28,64,0
Mvvv 112,220,192
EX 172
Mvvv 116,220,196
EX 173
Mvvv 120,224,180
s_nop 0
Ta 244,252,16384
Ta 246,252,20480
Mvvv 124,224,184
LD 3,28,66,2048
Mvvv 128,224,188
s_cmp_le_u32 s89,3
s_cselect_b64 s[24:25],s[68:69],%2
v_add_u32 v0,128,v0
Mvvv 132,224,192
s_add_u32 m0,s77,139264
EX 174
Mvvv 136,224,196
EX 175
Mvvv 228,248,140
s_nop 0
Ta 248,8,16896
Ta 250,8,20992
Mvvv 232,248,144
LD 0,24,48,0
Mvvv 236,248,148
s_add_u32 m0,s77,143360
CV 172,172,173
CV 173,174,175
Mvvv 240,248,152
EX 176
Mvvv 244,248,156
WL 2
EX 177
Maav 0,240,140
s_nop 0
Ta 252,252,16896
Ta 254,252,20992
Maav 4,240,144
LD 0,24,50,0
Maav 8,240,148
s_add_u32 m0,s77,147456
EX 178
Maav 12,240,152
EX 179
Maav 16,240,156
CV 176,176,177
CV 177,178,179
Maav 20,244,140
s_nop 0
Tv 220,8,17408
Tv 222,8,21504
Maav 24,244,144
LD 0,24,52,0
Maav 28,244,148
s_add_u32 m0,s77,151552
Maav 32,244,152
Maav 36,244,156
WL 2
Maav 40,248,140
s_nop 0
Tv 224,252,17408
Tv 226,252,21504
Maav 44,248,144
LD 0,24,54,0
Maav 48,248,148
s_add_u32 m0,s77,155648
Maav 52,248,152
Maav 56,248,156
Maav 60,252,140
s_nop 0
Ta 240,8,17920
Ta 242,8,22016
Maav 64,252,144
LD 0,24,56,0
Maav 68,252,148
Maav 72,252,152
Maav 76,252,156
WL 2
Ta 244,252,17920
Ta 246,252,22016
Mavv 80,220,140
Mavv 84,220,144
Mavv 88,220,148
Mavv 92,220,152
Mavv 96,220,156
Mavv 100,224,140
Ra 248,6,2048
Mavv 104,224,144
Ra 252,7,2048
Mavv 108,224,148
Rv 220,6,3072
Mavv 112,224,152
Mavv 116,224,156
s_waitcnt vmcnt(11) lgkmcnt(3)
s_barrier
Maav 120,240,140
s_add_u32 m0,s76,65536
Rv 224,7,3072
Rv 180,11,20480
Maav 124,240,144
s_cmp_eq_u32 s88,11
s_cselect_b32 s86,0x20000,0
v_subrev_u32 v2,s86,v2
Maav 128,240,148
Rv 184,11,22528
v_add_u32 v2,8192,v2
Maav 132,240,152
LN 2,96,62,0
Maav 136,240,156
s_nop 0
Rv 188,11,24576
Rv 192,11,26624
Maav 140,244,140
Rv 196,11,28672
Maav 144,244,144
Maav 148,244,148
LN 2,96,63,1024
Maav 152,244,152
Maav 156,244,156
WL 4
Nvaa 140,248,160,80
Mvaa 140,252,180
LN 2,96,62,2048
Mvva 140,220,200
Mvva 140,224,220
Nvaa 144,248,164,84
Mvaa 144,252,184
s_nop 0
Ta 240,9,16384
Ta 242,10,16640
Mvva 144,220,204
LN 2,96,63,3072
Mvva 144,224,224
s_and_b32 s86,%12,458752
s_cmp_eq_u32 s86,196608
s_cselect_b32 s86,s91,0x40000000
Nvaa 148,248,168,88
s_add_u32 s86,s86,45056
EX 140
Mvaa 148,252,188
EX 141
Mvva 148,220,208
s_nop 0
Ta 244,9,16896
Ta 246,10,17152
Mvva 148,224,228
buffer_load_dword v255,v17,s[92:95],s86 offen
Nvaa 152,248,172,92
EX 142
Mvaa 152,252,192
EX 143
Mvva 152,220,212
CV 162,140,141
CV 163,142,143
Mvva 152,224,232
EX 144
Nvaa 156,248,176,96
s_nop 0
Ta 248,9,16392
Ta 250,10,16648
Mvaa 156,252,196
EX 145
Mvva 156,220,216
EX 146
Mvva 156,224,236
WL 2
Ta 252,9,16904
Ta 254,10,17160
Mvav 60,240,180
EX 147
Mvav 64,240,184
CV 166,144,145
CV 167,146,147
Mvav 68,240,188
EX 148
Mvav 72,240,192
EX 149
Mvav 76,240,196
EX 150
Mvav 200,244,180
EX 151
Mvav 204,244,184
CV 170,148,149
CV 171,150,151
Mvav 208,244,188
EX 152
Mvav 212,244,192
EX 153
Mvav 216,244,196
s_waitcnt vmcnt(10) lgkmcnt(0)
s_barrier
Mvav 100,248,180
s_add_u32 m0,s76,49152
Rv 220,6,16384
Rv 224,7,16384
Mvav 104,248,184
s_cmp_le_u32 s89,3
s_cselect_b64 s[28:29],s[70:71],%3
Ra 240,6,17408
Mvav 108,248,188
s_nop 0
Ra 244,7,17408
v_add_u32 v3,262144,v3
Mvav 112,248,192
LD 3,28,64,0
Mvav 116,248,196
s_nop 0
EX 154
Mvav 120,252,180
EX 155
Mvav 124,252,184
s_nop 0
CV 174,152,153
CV 175,154,155
Mvav 128,252,188
LD 3,28,66,2048
Mvav 132,252,192
s_add_u32 m0,s76,0
s_cmp_eq_u32 s88,12
s_cselect_b32 s86,0x20000,0
Mvav 136,252,196
WL 1
v_subrev_u32 v1,s86,v1
v_add_u32 v1,8192,v1
Nvva 140,220,160,80
s_nop 0
Rv 180,12,20480
Rv 184,12,22528
Mvva 140,224,180
LN 1,36,58,0
Mvaa 140,240,200
WL 2
Rv 188,12,24576
Rv 192,12,26624
Mvaa 140,244,220
s_nop 0
Rv 196,12,28672
Nvva 144,220,164,84
EX 156
Mvva 144,224,184
s_nop 0
Ta 248,9,17408
Ta 250,10,17664
Mvaa 144,240,204
LN 1,36,59,1024
Mvaa 144,244,224
EX 157
Nvva 148,220,168,88
EX 158
Mvva 148,224,188
EX 159
Mvaa 148,240,208
s_nop 0
Ta 252,9,17920
Ta 254,10,18176
Mvaa 148,244,228
LN 1,36,60,2048
Nvva 152,220,172,92
CV 178,156,157
CV 179,158,159
Mvva 152,224,192
EX 140
Mvaa 152,240,212
EX 141
Mvaa 152,244,232
LN 1,36,61,3072
Nvva 156,220,176,96
s_nop 0
Tv 220,9,17416
Tv 222,10,17672
Mvva 156,224,196
EX 142
Mvaa 156,240,216
EX 143
Mvaa 156,244,236
WL 2
Tv 224,9,17928
Tv 226,10,18184
Mvav 60,248,180
CV 140,140,141
CV 141,142,143
Mvav 64,248,184
EX 144
Mvav 68,248,188
EX 145
Mvav 72,248,192
EX 146
Mvav 76,248,196
EX 147
Mvav 200,252,180
CV 144,144,145
CV 145,146,147
Mvav 204,252,184
EX 148
Mvav 208,252,188
EX 149
Mvav 212,252,192
EX 150
Mvav 216,252,196
WL 0
s_barrier
Mvvv 100,220,180
s_add_u32 m0,s76,50176
Ta 240,8,49152
Ta 242,8,53248
Mvvv 104,220,184
v_add_u32 v3,262144,v3
Mvvv 108,220,188
LD 3,28,64,0
Mvvv 112,220,192
EX 151
Mvvv 116,220,196
CV 148,148,149
CV 149,150,151
Mvvv 120,224,180
s_nop 0
Ta 244,252,49152
Ta 246,252,53248
Mvvv 124,224,184
LD 3,28,66,2048
Mvvv 128,224,188
s_cmp_le_u32 s89,4
s_cselect_b64 s[24:25],s[68:69],%2
v_add_u32 v0,128,v0
Mvvv 132,224,192
s_add_u32 m0,s77,118784
EX 152
Mvvv 136,224,196
EX 153
Mvvv 228,248,160
s_nop 0
Ta 248,8,49664
Ta 250,8,53760
Mvvv 232,248,164
LD 0,24,48,0
Mvvv 236,248,168
s_add_u32 m0,s77,122880
EX 154
Mvvv 240,248,172
EX 155
Mvvv 244,248,176
WL 2
CV 152,152,153
CV 153,154,155
Maav 0,240,160
s_nop 0
Ta 252,252,49664
Ta 254,252,53760
Maav 4,240,164
LD 0,24,50,0
Maav 8,240,168
s_add_u32 m0,s77,126976
EX 156
Maav 12,240,172
EX 157
Maav 16,240,176
EX 158
Maav 20,244,160
s_nop 0
Tv 220,8,50176
Tv 222,8,54272
Maav 24,244,164
LD 0,24,52,0
Maav 28,244,168
s_add_u32 m0,s77,131072
EX 159
Maav 32,244,172
CV 156,156,157
CV 157,158,159
Maav 36,244,176
WL 2
Maav 40,248,160
s_nop 0
Tv 224,252,50176
Tv 226,252,54272
Maav 44,248,164
LD 0,24,54,0
Maav 48,248,168
s_add_u32 m0,s77,135168
Maav 52,248,172
Maav 56,248,176
Maav 60,252,160
s_nop 0
Ta 240,8,50688
Ta 242,8,54784
Maav 64,252,164
LD 0,24,56,0
Maav 68,252,168
Maav 72,252,172
Maav 76,252,176
WL 2
Ta 244,252,50688
Ta 246,252,54784
Mavv 80,220,160
Mavv 84,220,164
Mavv 88,220,168
Mavv 92,220,172
Mavv 96,220,176
Mavv 100,224,160
Ra 248,6,18432
Mavv 104,224,164
Ra 252,7,18432
Mavv 108,224,168
Rv 220,6,19456
Mavv 112,224,172
Mavv 116,224,176
s_waitcnt vmcnt(11) lgkmcnt(3)
s_barrier
Maav 120,240,160
s_add_u32 m0,s76,98304
Rv 224,7,19456
Rv 180,11,40960
Maav 124,240,164
s_cmp_eq_u32 s88,12
s_cselect_b32 s86,0x20000,0
v_subrev_u32 v2,s86,v2
Maav 128,240,168
Rv 184,11,43008
v_add_u32 v2,8192,v2
Maav 132,240,172
LN 2,96,62,0
Maav 136,240,176
s_nop 0
Rv 188,11,45056
Rv 192,11,47104
Maav 140,244,160
Rv 196,11,49152
Maav 144,244,164
Maav 148,244,168
LN 2,96,63,1024
Maav 152,244,172
Maav 156,244,176
WL 4
Nvaa 160,248,160,80
Mvaa 160,252,180
LN 2,96,62,2048
Mvva 160,220,200
Mvva 160,224,220
Nvaa 164,248,164,84
Mvaa 164,252,184
s_nop 0
Ta 240,9,16384
Ta 242,10,16640
Mvva 164,220,204
LN 2,96,63,3072
Mvva 164,224,224
EX 160
Nvaa 168,248,168,88
EX 161
Mvaa 168,252,188
EX 162
Mvva 168,220,208
s_nop 0
Ta 244,9,16896
Ta 246,10,17152
Mvva 168,224,228
EX 163
Nvaa 172,248,172,92
CV 142,160,161
CV 143,162,163
Mvaa 172,252,192
EX 164
Mvva 172,220,212
EX 165
Mvva 172,224,232
EX 166
Nvaa 176,248,176,96
s_nop 0
Ta 248,9,16392
Ta 250,10,16648
Mvaa 176,252,196
EX 167
Mvva 176,220,216
CV 146,164,165
CV 147,166,167
Mvva 176,224,236
WL 2
Ta 252,9,16904
Ta 254,10,17160
Mvav 60,240,180
EX 168
Mvav 64,240,184
EX 169
Mvav 68,240,188
EX 170
Mvav 72,240,192
EX 171
Mvav 76,240,196
CV 150,168,169
CV 151,170,171
Mvav 200,244,180
EX 172
Mvav 204,244,184
EX 173
Mvav 208,244,188
EX 174
Mvav 212,244,192
EX 175
Mvav 216,244,196
s_waitcnt vmcnt(9) lgkmcnt(0)
s_barrier
Mvav 100,248,180
s_add_u32 m0,s76,49152
Rv 220,6,32768
Rv 224,7,32768
Mvav 104,248,184
s_cmp_le_u32 s89,4
s_cselect_b64 s[28:29],s[70:71],%3
Ra 240,6,33792
Mvav 108,248,188
Ra 244,7,33792
v_add_u32 v3,262144,v3
Mvav 112,248,192
LD 3,28,64,0
Mvav 116,248,196
CV 154,172,173
CV 155,174,175
Mvav 120,252,180
EX 176
Mvav 124,252,184
EX 177
Mvav 128,252,188
LD 3,28,66,2048
Mvav 132,252,192
s_add_u32 m0,s76,16384
s_cmp_eq_u32 s88,13
s_cselect_b32 s86,0x20000,0
Mvav 136,252,196
WL 1
v_subrev_u32 v1,s86,v1
v_add_u32 v1,8192,v1
Nvva 160,220,160,80
s_nop 0
Rv 180,12,40960
Rv 184,12,43008
Mvva 160,224,180
LN 1,36,58,0
Mvaa 160,240,200
WL 2
Rv 188,12,45056
Rv 192,12,47104
Mvaa 160,244,220
Rv 196,12,49152
Nvva 164,220,164,84
EX 178
Mvva 164,224,184
s_nop 0
Ta 248,9,17408
Ta 250,10,17664
Mvaa 164,240,204
LN 1,36,59,1024
Mvaa 164,244,224
EX 179
Nvva 168,220,168,88
CV 158,176,177
CV 159,178,179
Mvva 168,224,188
EX 160
Mvaa 168,240,208
s_nop 0
Ta 252,9,17920
Ta 254,10,18176
Mvaa 168,244,228
LN 1,36,60,2048
Nvva 172,220,172,92
EX 161
Mvva 172,224,192
EX 162
Mvaa 172,240,212
EX 163
Mvaa 172,244,232
LN 1,36,61,3072
)ASM"
R"ASM(Nvva 176,220,176,96
s_nop 0
Tv 220,9,17416
Tv 222,10,17672
Mvva 176,224,196
CV 160,160,161
CV 161,162,163
Mvaa 176,240,216
EX 164
Mvaa 176,244,236
WL 2
Tv 224,9,17928
Tv 226,10,18184
Mvav 60,248,180
EX 165
Mvav 64,248,184
EX 166
Mvav 68,248,188
EX 167
Mvav 72,248,192
CV 164,164,165
CV 165,166,167
Mvav 76,248,196
EX 168
Mvav 200,252,180
EX 169
Mvav 204,252,184
EX 170
Mvav 208,252,188
EX 171
Mvav 212,252,192
CV 168,168,169
CV 169,170,171
Mvav 216,252,196
WL 0
s_barrier
Mvvv 100,220,180
s_add_u32 m0,s76,50176
Ta 240,8,32768
Ta 242,8,36864
Mvvv 104,220,184
v_add_u32 v3,262144,v3
Mvvv 108,220,188
LD 3,28,64,0
Mvvv 112,220,192
EX 172
Mvvv 116,220,196
EX 173
Mvvv 120,224,180
s_nop 0
Ta 244,252,32768
Ta 246,252,36864
Mvvv 124,224,184
LD 3,28,66,2048
Mvvv 128,224,188
s_cmp_le_u32 s89,5
s_cselect_b64 s[24:25],s[68:69],%2
v_add_u32 v0,128,v0
Mvvv 132,224,192
s_add_u32 m0,s77,139264
EX 174
Mvvv 136,224,196
EX 175
Mvvv 228,248,140
s_nop 0
Ta 248,8,33280
Ta 250,8,37376
Mvvv 232,248,144
LD 0,24,48,0
Mvvv 236,248,148
s_add_u32 m0,s77,143360
CV 172,172,173
CV 173,174,175
Mvvv 240,248,152
EX 176
Mvvv 244,248,156
WL 2
EX 177
Maav 0,240,140
s_nop 0
Ta 252,252,33280
Ta 254,252,37376
Maav 4,240,144
LD 0,24,50,0
Maav 8,240,148
s_add_u32 m0,s77,147456
EX 178
Maav 12,240,152
EX 179
Maav 16,240,156
CV 176,176,177
CV 177,178,179
Maav 20,244,140
s_nop 0
Tv 220,8,33792
Tv 222,8,37888
Maav 24,244,144
LD 0,24,52,0
Maav 28,244,148
s_add_u32 m0,s77,151552
Maav 32,244,152
Maav 36,244,156
WL 2
Maav 40,248,140
s_nop 0
Tv 224,252,33792
Tv 226,252,37888
Maav 44,248,144
LD 0,24,54,0
Maav 48,248,148
s_add_u32 m0,s77,155648
Maav 52,248,152
Maav 56,248,156
Maav 60,252,140
s_nop 0
Ta 240,8,34304
Ta 242,8,38400
Maav 64,252,144
LD 0,24,56,0
Maav 68,252,148
Maav 72,252,152
Maav 76,252,156
WL 2
Ta 244,252,34304
Ta 246,252,38400
Mavv 80,220,140
Mavv 84,220,144
Mavv 88,220,148
Mavv 92,220,152
Mavv 96,220,156
Mavv 100,224,140
Ra 248,6,34816
Mavv 104,224,144
Ra 252,7,34816
Mavv 108,224,148
Rv 220,6,35840
Mavv 112,224,152
Mavv 116,224,156
s_waitcnt vmcnt(11) lgkmcnt(3)
s_barrier
Maav 120,240,140
s_add_u32 m0,s76,81920
Rv 224,7,35840
Rv 180,11,20480
Maav 124,240,144
s_cmp_eq_u32 s88,13
s_cselect_b32 s86,0x20000,0
v_subrev_u32 v2,s86,v2
Maav 128,240,148
Rv 184,11,22528
v_add_u32 v2,8192,v2
Maav 132,240,152
LN 2,96,62,0
Maav 136,240,156
s_nop 0
Rv 188,11,24576
Rv 192,11,26624
Maav 140,244,140
Rv 196,11,28672
Maav 144,244,144
Maav 148,244,148
LN 2,96,63,1024
Maav 152,244,152
Maav 156,244,156
WL 4
Nvaa 140,248,160,80
Mvaa 140,252,180
LN 2,96,62,2048
Mvva 140,220,200
Mvva 140,224,220
Nvaa 144,248,164,84
Mvaa 144,252,184
s_nop 0
Ta 240,9,16384
Ta 242,10,16640
Mvva 144,220,204
LN 2,96,63,3072
Mvva 144,224,224
s_and_b32 s86,%12,458752
s_cmp_eq_u32 s86,262144
s_cselect_b32 s86,s91,0x40000000
Nvaa 148,248,168,88
s_add_u32 s86,s86,49152
EX 140
Mvaa 148,252,188
EX 141
Mvva 148,220,208
s_nop 0
Ta 244,9,16896
Ta 246,10,17152
Mvva 148,224,228
buffer_load_dword v255,v17,s[92:95],s86 offen
Nvaa 152,248,172,92
EX 142
Mvaa 152,252,192
EX 143
Mvva 152,220,212
CV 162,140,141
CV 163,142,143
Mvva 152,224,232
EX 144
Nvaa 156,248,176,96
s_nop 0
Ta 248,9,16392
Ta 250,10,16648
Mvaa 156,252,196
EX 145
Mvva 156,220,216
EX 146
Mvva 156,224,236
WL 2
Ta 252,9,16904
Ta 254,10,17160
Mvav 60,240,180
EX 147
Mvav 64,240,184
CV 166,144,145
CV 167,146,147
Mvav 68,240,188
EX 148
Mvav 72,240,192
EX 149
Mvav 76,240,196
EX 150
Mvav 200,244,180
EX 151
Mvav 204,244,184
CV 170,148,149
CV 171,150,151
Mvav 208,244,188
EX 152
Mvav 212,244,192
EX 153
Mvav 216,244,196
s_waitcnt vmcnt(10) lgkmcnt(0)
s_barrier
Mvav 100,248,180
s_add_u32 m0,s76,49152
Rv 220,6,0
Rv 224,7,0
Mvav 104,248,184
s_cmp_le_u32 s89,5
s_cselect_b64 s[28:29],s[70:71],%3
Ra 240,6,1024
Mvav 108,248,188
s_nop 0
Ra 244,7,1024
v_add_u32 v3,262144,v3
Mvav 112,248,192
LD 3,28,64,0
Mvav 116,248,196
s_nop 0
EX 154
Mvav 120,252,180
EX 155
Mvav 124,252,184
s_nop 0
CV 174,152,153
CV 175,154,155
Mvav 128,252,188
LD 3,28,66,2048
Mvav 132,252,192
s_add_u32 m0,s76,32768
s_cmp_eq_u32 s88,14
s_cselect_b32 s86,0x20000,0
Mvav 136,252,196
WL 1
v_subrev_u32 v1,s86,v1
v_add_u32 v1,8192,v1
Nvva 140,220,160,80
s_nop 0
Rv 180,12,20480
Rv 184,12,22528
Mvva 140,224,180
LN 1,36,58,0
Mvaa 140,240,200
WL 2
Rv 188,12,24576
Rv 192,12,26624
Mvaa 140,244,220
Rv 196,12,28672
Nvva 144,220,164,84
EX 156
Mvva 144,224,184
s_nop 0
Ta 248,9,17408
Ta 250,10,17664
Mvaa 144,240,204
LN 1,36,59,1024
Mvaa 144,244,224
EX 157
Nvva 148,220,168,88
EX 158
Mvva 148,224,188
EX 159
Mvaa 148,240,208
s_nop 0
Ta 252,9,17920
Ta 254,10,18176
Mvaa 148,244,228
LN 1,36,60,2048
Nvva 152,220,172,92
CV 178,156,157
CV 179,158,159
Mvva 152,224,192
EX 140
Mvaa 152,240,212
EX 141
Mvaa 152,244,232
LN 1,36,61,3072
Nvva 156,220,176,96
s_nop 0
Tv 220,9,17416
Tv 222,10,17672
Mvva 156,224,196
EX 142
Mvaa 156,240,216
EX 143
Mvaa 156,244,236
WL 2
Tv 224,9,17928
Tv 226,10,18184
Mvav 60,248,180
CV 140,140,141
CV 141,142,143
Mvav 64,248,184
EX 144
Mvav 68,248,188
EX 145
Mvav 72,248,192
EX 146
Mvav 76,248,196
EX 147
Mvav 200,252,180
CV 144,144,145
CV 145,146,147
Mvav 204,252,184
EX 148
Mvav 208,252,188
EX 149
Mvav 212,252,192
EX 150
Mvav 216,252,196
WL 0
s_barrier
Mvvv 100,220,180
s_add_u32 m0,s76,50176
Ta 240,8,16384
Ta 242,8,20480
Mvvv 104,220,184
v_add_u32 v3,262144,v3
Mvvv 108,220,188
LD 3,28,64,0
Mvvv 112,220,192
EX 151
Mvvv 116,220,196
CV 148,148,149
CV 149,150,151
Mvvv 120,224,180
s_nop 0
Ta 244,252,16384
Ta 246,252,20480
Mvvv 124,224,184
LD 3,28,66,2048
Mvvv 128,224,188
s_cmp_le_u32 s89,6
s_cselect_b64 s[24:25],s[68:69],%2
v_add_u32 v0,128,v0
Mvvv 132,224,192
s_add_u32 m0,s77,118784
EX 152
Mvvv 136,224,196
EX 153
Mvvv 228,248,160
s_nop 0
Ta 248,8,16896
Ta 250,8,20992
Mvvv 232,248,164
LD 0,24,48,0
Mvvv 236,248,168
s_add_u32 m0,s77,122880
EX 154
Mvvv 240,248,172
EX 155
Mvvv 244,248,176
WL 2
CV 152,152,153
CV 153,154,155
Maav 0,240,160
s_nop 0
Ta 252,252,16896
Ta 254,252,20992
Maav 4,240,164
LD 0,24,50,0
Maav 8,240,168
s_add_u32 m0,s77,126976
EX 156
Maav 12,240,172
EX 157
Maav 16,240,176
EX 158
Maav 20,244,160
s_nop 0
Tv 220,8,17408
Tv 222,8,21504
Maav 24,244,164
LD 0,24,52,0
Maav 28,244,168
s_add_u32 m0,s77,131072
EX 159
Maav 32,244,172
CV 156,156,157
CV 157,158,159
Maav 36,244,176
WL 2
Maav 40,248,160
s_nop 0
Tv 224,252,17408
Tv 226,252,21504
Maav 44,248,164
LD 0,24,54,0
Maav 48,248,168
s_add_u32 m0,s77,135168
Maav 52,248,172
Maav 56,248,176
Maav 60,252,160
s_nop 0
Ta 240,8,17920
Ta 242,8,22016
Maav 64,252,164
LD 0,24,56,0
Maav 68,252,168
Maav 72,252,172
Maav 76,252,176
WL 2
Ta 244,252,17920
Ta 246,252,22016
Mavv 80,220,160
Mavv 84,220,164
Mavv 88,220,168
Mavv 92,220,172
Mavv 96,220,176
Mavv 100,224,160
Ra 248,6,2048
Mavv 104,224,164
Ra 252,7,2048
Mavv 108,224,168
Rv 220,6,3072
Mavv 112,224,172
Mavv 116,224,176
s_waitcnt vmcnt(11) lgkmcnt(3)
s_barrier
Maav 120,240,160
s_add_u32 m0,s76,65536
Rv 224,7,3072
Rv 180,11,40960
Maav 124,240,164
s_cmp_eq_u32 s88,14
s_cselect_b32 s86,0x20000,0
v_subrev_u32 v2,s86,v2
Maav 128,240,168
Rv 184,11,43008
v_add_u32 v2,8192,v2
Maav 132,240,172
LN 2,96,62,0
Maav 136,240,176
s_nop 0
Rv 188,11,45056
Rv 192,11,47104
Maav 140,244,160
Rv 196,11,49152
Maav 144,244,164
Maav 148,244,168
LN 2,96,63,1024
Maav 152,244,172
Maav 156,244,176
WL 4
Nvaa 160,248,160,80
Mvaa 160,252,180
LN 2,96,62,2048
Mvva 160,220,200
Mvva 160,224,220
Nvaa 164,248,164,84
Mvaa 164,252,184
s_nop 0
Ta 240,9,16384
Ta 242,10,16640
Mvva 164,220,204
LN 2,96,63,3072
Mvva 164,224,224
EX 160
Nvaa 168,248,168,88
EX 161
Mvaa 168,252,188
EX 162
Mvva 168,220,208
s_nop 0
Ta 244,9,16896
Ta 246,10,17152
Mvva 168,224,228
EX 163
Nvaa 172,248,172,92
CV 142,160,161
CV 143,162,163
Mvaa 172,252,192
EX 164
Mvva 172,220,212
EX 165
Mvva 172,224,232
EX 166
Nvaa 176,248,176,96
s_nop 0
Ta 248,9,16392
Ta 250,10,16648
Mvaa 176,252,196
EX 167
Mvva 176,220,216
CV 146,164,165
CV 147,166,167
Mvva 176,224,236
WL 2
Ta 252,9,16904
Ta 254,10,17160
Mvav 60,240,180
EX 168
Mvav 64,240,184
EX 169
Mvav 68,240,188
EX 170
Mvav 72,240,192
EX 171
Mvav 76,240,196
CV 150,168,169
CV 151,170,171
Mvav 200,244,180
EX 172
Mvav 204,244,184
EX 173
Mvav 208,244,188
EX 174
Mvav 212,244,192
EX 175
Mvav 216,244,196
s_waitcnt vmcnt(9) lgkmcnt(0)
s_barrier
Mvav 100,248,180
s_add_u32 m0,s76,49152
Rv 220,6,16384
Rv 224,7,16384
Mvav 104,248,184
s_cmp_le_u32 s89,6
s_cselect_b64 s[28:29],s[70:71],%3
Ra 240,6,17408
Mvav 108,248,188
s_nop 0
Ra 244,7,17408
v_add_u32 v3,262144,v3
Mvav 112,248,192
LD 3,28,64,0
Mvav 116,248,196
CV 154,172,173
CV 155,174,175
Mvav 120,252,180
EX 176
Mvav 124,252,184
EX 177
Mvav 128,252,188
LD 3,28,66,2048
Mvav 132,252,192
s_add_u32 m0,s76,0
s_cmp_eq_u32 s88,15
s_cselect_b32 s86,0x20000,0
Mvav 136,252,196
WL 1
v_subrev_u32 v1,s86,v1
v_add_u32 v1,8192,v1
Nvva 160,220,160,80
s_nop 0
Rv 180,12,40960
Rv 184,12,43008
Mvva 160,224,180
LN 1,36,58,0
Mvaa 160,240,200
WL 2
Rv 188,12,45056
Rv 192,12,47104
Mvaa 160,244,220
Rv 196,12,49152
Nvva 164,220,164,84
EX 178
Mvva 164,224,184
s_nop 0
Ta 248,9,17408
Ta 250,10,17664
Mvaa 164,240,204
LN 1,36,59,1024
Mvaa 164,244,224
EX 179
Nvva 168,220,168,88
CV 158,176,177
CV 159,178,179
Mvva 168,224,188
EX 160
Mvaa 168,240,208
s_nop 0
Ta 252,9,17920
Ta 254,10,18176
Mvaa 168,244,228
LN 1,36,60,2048
Nvva 172,220,172,92
EX 161
Mvva 172,224,192
EX 162
Mvaa 172,240,212
EX 163
Mvaa 172,244,232
LN 1,36,61,3072
Nvva 176,220,176,96
s_nop 0
Tv 220,9,17416
Tv 222,10,17672
Mvva 176,224,196
CV 160,160,161
CV 161,162,163
Mvaa 176,240,216
EX 164
Mvaa 176,244,236
WL 2
Tv 224,9,17928
Tv 226,10,18184
Mvav 60,248,180
EX 165
Mvav 64,248,184
EX 166
Mvav 68,248,188
EX 167
Mvav 72,248,192
CV 164,164,165
CV 165,166,167
Mvav 76,248,196
EX 168
Mvav 200,252,180
EX 169
Mvav 204,252,184
EX 170
Mvav 208,252,188
EX 171
Mvav 212,252,192
CV 168,168,169
CV 169,170,171
Mvav 216,252,196
WL 0
s_barrier
Mvvv 100,220,180
s_add_u32 m0,s76,50176
Ta 240,8,49152
Ta 242,8,53248
Mvvv 104,220,184
v_add_u32 v3,262144,v3
Mvvv 108,220,188
LD 3,28,64,0
Mvvv 112,220,192
EX 172
Mvvv 116,220,196
EX 173
Mvvv 120,224,180
s_nop 0
Ta 244,252,49152
Ta 246,252,53248
Mvvv 124,224,184
LD 3,28,66,2048
Mvvv 128,224,188
s_cmp_le_u32 s89,7
s_cselect_b64 s[24:25],s[68:69],%2
v_add_u32 v0,128,v0
Mvvv 132,224,192
s_add_u32 m0,s77,139264
EX 174
Mvvv 136,224,196
EX 175
Mvvv 228,248,140
s_nop 0
Ta 248,8,49664
Ta 250,8,53760
Mvvv 232,248,144
LD 0,24,48,0
Mvvv 236,248,148
s_add_u32 m0,s77,143360
CV 172,172,173
CV 173,174,175
Mvvv 240,248,152
EX 176
Mvvv 244,248,156
WL 2
EX 177
Maav 0,240,140
s_nop 0
Ta 252,252,49664
Ta 254,252,53760
Maav 4,240,144
LD 0,24,50,0
Maav 8,240,148
s_add_u32 m0,s77,147456
EX 178
Maav 12,240,152
EX 179
Maav 16,240,156
CV 176,176,177
CV 177,178,179
Maav 20,244,140
s_nop 0
Tv 220,8,50176
Tv 222,8,54272
Maav 24,244,144
LD 0,24,52,0
Maav 28,244,148
s_add_u32 m0,s77,151552
Maav 32,244,152
Maav 36,244,156
WL 2
Maav 40,248,140
s_nop 0
Tv 224,252,50176
Tv 226,252,54272
Maav 44,248,144
LD 0,24,54,0
Maav 48,248,148
s_add_u32 m0,s77,155648
Maav 52,248,152
Maav 56,248,156
Maav 60,252,140
s_nop 0
Ta 240,8,50688
Ta 242,8,54784
Maav 64,252,144
LD 0,24,56,0
Maav 68,252,148
Maav 72,252,152
Maav 76,252,156
WL 2
Ta 244,252,50688
Ta 246,252,54784
Mavv 80,220,140
Mavv 84,220,144
Mavv 88,220,148
Mavv 92,220,152
Mavv 96,220,156
Mavv 100,224,140
Ra 248,6,18432
Mavv 104,224,144
Ra 252,7,18432
Mavv 108,224,148
Rv 220,6,19456
Mavv 112,224,152
Mavv 116,224,156
s_waitcnt vmcnt(11) lgkmcnt(3)
s_barrier
Maav 120,240,140
s_add_u32 m0,s76,98304
Rv 224,7,19456
Rv 180,11,20480
Maav 124,240,144
s_cmp_eq_u32 s88,15
s_cselect_b32 s86,0x20000,0
v_subrev_u32 v2,s86,v2
Maav 128,240,148
Rv 184,11,22528
v_add_u32 v2,8192,v2
Maav 132,240,152
LN 2,96,62,0
Maav 136,240,156
s_nop 0
Rv 188,11,24576
Rv 192,11,26624
Maav 140,244,140
Rv 196,11,28672
Maav 144,244,144
Maav 148,244,148
LN 2,96,63,1024
Maav 152,244,152
Maav 156,244,156
WL 4
Nvaa 140,248,160,80
Mvaa 140,252,180
LN 2,96,62,2048
Mvva 140,220,200
Mvva 140,224,220
Nvaa 144,248,164,84
Mvaa 144,252,184
s_nop 0
Ta 240,9,16384
Ta 242,10,16640
Mvva 144,220,204
LN 2,96,63,3072
Mvva 144,224,224
s_and_b32 s86,%12,458752
s_cmp_eq_u32 s86,327680
s_cselect_b32 s86,s91,0x40000000
Nvaa 148,248,168,88
s_add_u32 s86,s86,53248
EX 140
Mvaa 148,252,188
EX 141
Mvva 148,220,208
s_nop 0
Ta 244,9,16896
Ta 246,10,17152
Mvva 148,224,228
buffer_load_dword v255,v17,s[92:95],s86 offen
Nvaa 152,248,172,92
EX 142
Mvaa 152,252,192
EX 143
Mvva 152,220,212
CV 162,140,141
CV 163,142,143
Mvva 152,224,232
EX 144
Nvaa 156,248,176,96
s_nop 0
Ta 248,9,16392
Ta 250,10,16648
Mvaa 156,252,196
EX 145
Mvva 156,220,216
EX 146
Mvva 156,224,236
WL 2
Ta 252,9,16904
Ta 254,10,17160
Mvav 60,240,180
EX 147
Mvav 64,240,184
CV 166,144,145
CV 167,146,147
Mvav 68,240,188
EX 148
Mvav 72,240,192
EX 149
Mvav 76,240,196
EX 150
Mvav 200,244,180
EX 151
Mvav 204,244,184
CV 170,148,149
CV 171,150,151
Mvav 208,244,188
EX 152
Mvav 212,244,192
EX 153
Mvav 216,244,196
s_waitcnt vmcnt(10) lgkmcnt(0)
s_barrier
Mvav 100,248,180
s_add_u32 m0,s76,49152
Rv 220,6,32768
Rv 224,7,32768
Mvav 104,248,184
s_cmp_le_u32 s89,7
s_cselect_b64 s[28:29],s[70:71],%3
Ra 240,6,33792
Mvav 108,248,188
s_nop 0
Ra 244,7,33792
v_add_u32 v3,262144,v3
Mvav 112,248,192
LD 3,28,64,0
Mvav 116,248,196
s_nop 0
EX 154
Mvav 120,252,180
EX 155
Mvav 124,252,184
s_nop 0
CV 174,152,153
CV 175,154,155
Mvav 128,252,188
LD 3,28,66,2048
Mvav 132,252,192
s_add_u32 m0,s76,16384
s_cmp_le_u32 s88,15
s_cselect_b32 s86,0x20000,0
Mvav 136,252,196
WL 1
v_add_u32 v1,s86,v1
v_add_u32 v1,139264,v1
Nvva 140,220,160,80
s_nop 0
Rv 180,12,20480
Rv 184,12,22528
Mvva 140,224,180
LN 1,36,58,0
Mvaa 140,240,200
WL 2
Rv 188,12,24576
Rv 192,12,26624
Mvaa 140,244,220
Rv 196,12,28672
Nvva 144,220,164,84
EX 156
Mvva 144,224,184
s_nop 0
Ta 248,9,17408
Ta 250,10,17664
Mvaa 144,240,204
LN 1,36,59,1024
Mvaa 144,244,224
EX 157
Nvva 148,220,168,88
EX 158
Mvva 148,224,188
EX 159
Mvaa 148,240,208
s_nop 0
Ta 252,9,17920
Ta 254,10,18176
Mvaa 148,244,228
LN 1,36,60,2048
Nvva 152,220,172,92
CV 178,156,157
CV 179,158,159
Mvva 152,224,192
EX 140
Mvaa 152,240,212
EX 141
Mvaa 152,244,232
LN 1,36,61,3072
Nvva 156,220,176,96
s_nop 0
Tv 220,9,17416
Tv 222,10,17672
Mvva 156,224,196
EX 142
Mvaa 156,240,216
EX 143
Mvaa 156,244,236
WL 2
Tv 224,9,17928
Tv 226,10,18184
Mvav 60,248,180
CV 140,140,141
CV 141,142,143
Mvav 64,248,184
EX 144
Mvav 68,248,188
EX 145
Mvav 72,248,192
EX 146
Mvav 76,248,196
EX 147
Mvav 200,252,180
CV 144,144,145
CV 145,146,147
Mvav 204,252,184
EX 148
Mvav 208,252,188
EX 149
Mvav 212,252,192
EX 150
Mvav 216,252,196
WL 0
s_barrier
Mvvv 100,220,180
s_add_u32 m0,s76,50176
Ta 240,8,32768
Ta 242,8,36864
Mvvv 104,220,184
v_add_u32 v3,262144,v3
Mvvv 108,220,188
LD 3,28,64,0
Mvvv 112,220,192
EX 151
Mvvv 116,220,196
CV 148,148,149
CV 149,150,151
Mvvv 120,224,180
s_nop 0
Ta 244,252,32768
Ta 246,252,36864
Mvvv 124,224,184
LD 3,28,66,2048
Mvvv 128,224,188
s_cmp_le_u32 s89,8
s_cselect_b64 s[24:25],s[68:69],%2
v_add_u32 v0,128,v0
Mvvv 132,224,192
s_add_u32 m0,s77,118784
EX 152
Mvvv 136,224,196
EX 153
Mvvv 228,248,160
s_nop 0
Ta 248,8,33280
Ta 250,8,37376
Mvvv 232,248,164
LD 0,24,48,0
Mvvv 236,248,168
s_add_u32 m0,s77,122880
EX 154
Mvvv 240,248,172
EX 155
Mvvv 244,248,176
WL 2
CV 152,152,153
CV 153,154,155
Maav 0,240,160
s_nop 0
Ta 252,252,33280
Ta 254,252,37376
Maav 4,240,164
LD 0,24,50,0
Maav 8,240,168
s_add_u32 m0,s77,126976
EX 156
Maav 12,240,172
EX 157
Maav 16,240,176
EX 158
Maav 20,244,160
s_nop 0
Tv 220,8,33792
Tv 222,8,37888
Maav 24,244,164
LD 0,24,52,0
Maav 28,244,168
s_add_u32 m0,s77,131072
EX 159
Maav 32,244,172
CV 156,156,157
CV 157,158,159
Maav 36,244,176
WL 2
Maav 40,248,160
s_nop 0
Tv 224,252,33792
Tv 226,252,37888
Maav 44,248,164
LD 0,24,54,0
Maav 48,248,168
s_add_u32 m0,s77,135168
Maav 52,248,172
Maav 56,248,176
Maav 60,252,160
s_nop 0
Ta 240,8,34304
Ta 242,8,38400
Maav 64,252,164
LD 0,24,56,0
Maav 68,252,168
Maav 72,252,172
Maav 76,252,176
WL 2
Ta 244,252,34304
Ta 246,252,38400
Mavv 80,220,160
Mavv 84,220,164
Mavv 88,220,168
Mavv 92,220,172
Mavv 96,220,176
Mavv 100,224,160
Ra 248,6,34816
Mavv 104,224,164
Ra 252,7,34816
Mavv 108,224,168
Rv 220,6,35840
Mavv 112,224,172
Mavv 116,224,176
s_waitcnt vmcnt(11) lgkmcnt(3)
s_barrier
Maav 120,240,160
s_add_u32 m0,s76,81920
Rv 224,7,35840
Rv 180,11,40960
Maav 124,240,164
s_cmp_le_u32 s88,15
s_cselect_b32 s86,0x20000,0
v_add_u32 v2,s86,v2
Maav 128,240,168
Rv 184,11,43008
v_add_u32 v2,139264,v2
Maav 132,240,172
LN 2,96,62,0
Maav 136,240,176
s_nop 0
Rv 188,11,45056
Rv 192,11,47104
Maav 140,244,160
Rv 196,11,49152
Maav 144,244,164
Maav 148,244,168
LN 2,96,63,1024
Maav 152,244,172
Maav 156,244,176
WL 4
Nvaa 160,248,160,80
Mvaa 160,252,180
LN 2,96,62,2048
Mvva 160,220,200
Mvva 160,224,220
Nvaa 164,248,164,84
Mvaa 164,252,184
s_nop 0
Ta 240,9,16384
Ta 242,10,16640
Mvva 164,220,204
LN 2,96,63,3072
Mvva 164,224,224
EX 160
Nvaa 168,248,168,88
EX 161
Mvaa 168,252,188
EX 162
Mvva 168,220,208
s_nop 0
Ta 244,9,16896
Ta 246,10,17152
Mvva 168,224,228
EX 163
Nvaa 172,248,172,92
CV 142,160,161
CV 143,162,163
Mvaa 172,252,192
EX 164
Mvva 172,220,212
EX 165
Mvva 172,224,232
EX 166
Nvaa 176,248,176,96
s_nop 0
Ta 248,9,16392
Ta 250,10,16648
Mvaa 176,252,196
EX 167
Mvva 176,220,216
CV 146,164,165
CV 147,166,167
Mvva 176,224,236
WL 2
Ta 252,9,16904
Ta 254,10,17160
Mvav 60,240,180
EX 168
Mvav 64,240,184
EX 169
Mvav 68,240,188
EX 170
Mvav 72,240,192
EX 171
Mvav 76,240,196
CV 150,168,169
CV 151,170,171
Mvav 200,244,180
EX 172
Mvav 204,244,184
EX 173
Mvav 208,244,188
EX 174
Mvav 212,244,192
EX 175
Mvav 216,244,196
s_waitcnt vmcnt(9) lgkmcnt(0)
s_barrier
Mvav 100,248,180
s_add_u32 m0,s76,49152
Rv 220,6,0
Rv 224,7,0
Mvav 104,248,184
s_cmp_le_u32 s89,8
s_cselect_b64 s[28:29],s[70:71],%3
Ra 240,6,1024
Mvav 108,248,188
Ra 244,7,1024
v_add_u32 v3,262144,v3
Mvav 112,248,192
LD 3,28,64,0
Mvav 116,248,196
CV 154,172,173
CV 155,174,175
Mvav 120,252,180
EX 176
Mvav 124,252,184
EX 177
Mvav 128,252,188
LD 3,28,66,2048
Mvav 132,252,192
s_add_u32 m0,s76,32768
s_cmp_eq_u32 s88,1
s_cselect_b32 s86,0x20000,0
Mvav 136,252,196
WL 1
v_subrev_u32 v1,s86,v1
v_add_u32 v1,8192,v1
Nvva 160,220,160,80
s_nop 0
Rv 180,12,40960
Rv 184,12,43008
Mvva 160,224,180
LN 1,36,58,0
Mvaa 160,240,200
WL 2
Rv 188,12,45056
Rv 192,12,47104
Mvaa 160,244,220
Rv 196,12,49152
Nvva 164,220,164,84
EX 178
Mvva 164,224,184
s_nop 0
Ta 248,9,17408
Ta 250,10,17664
Mvaa 164,240,204
LN 1,36,59,1024
Mvaa 164,244,224
EX 179
Nvva 168,220,168,88
CV 158,176,177
CV 159,178,179
Mvva 168,224,188
EX 160
Mvaa 168,240,208
s_nop 0
Ta 252,9,17920
Ta 254,10,18176
Mvaa 168,244,228
LN 1,36,60,2048
Nvva 172,220,172,92
EX 161
Mvva 172,224,192
EX 162
Mvaa 172,240,212
EX 163
Mvaa 172,244,232
LN 1,36,61,3072
Nvva 176,220,176,96
s_nop 0
Tv 220,9,17416
Tv 222,10,17672
Mvva 176,224,196
CV 160,160,161
CV 161,162,163
Mvaa 176,240,216
EX 164
Mvaa 176,244,236
WL 2
Tv 224,9,17928
Tv 226,10,18184
Mvav 60,248,180
EX 165
Mvav 64,248,184
EX 166
Mvav 68,248,188
EX 167
Mvav 72,248,192
CV 164,164,165
CV 165,166,167
Mvav 76,248,196
EX 168
Mvav 200,252,180
EX 169
Mvav 204,252,184
EX 170
Mvav 208,252,188
EX 171
Mvav 212,252,192
CV 168,168,169
CV 169,170,171
Mvav 216,252,196
WL 0
s_barrier
Mvvv 100,220,180
s_add_u32 m0,s76,50176
Ta 240,8,16384
Ta 242,8,20480
Mvvv 104,220,184
v_add_u32 v3,262144,v3
Mvvv 108,220,188
LD 3,28,64,0
Mvvv 112,220,192
EX 172
Mvvv 116,220,196
EX 173
Mvvv 120,224,180
s_nop 0
Ta 244,252,16384
Ta 246,252,20480
Mvvv 124,224,184
LD 3,28,66,2048
Mvvv 128,224,188
EX 174
Mvvv 132,224,192
EX 175
Mvvv 136,224,196
CV 172,172,173
CV 173,174,175
Mvvv 228,248,140
s_nop 0
Ta 248,8,16896
Ta 250,8,20992
Mvvv 232,248,144
EX 176
Mvvv 236,248,148
EX 177
Mvvv 240,248,152
EX 178
Mvvv 244,248,156
WL 2
EX 179
Maav 0,240,140
s_nop 0
Ta 252,252,16896
Ta 254,252,20992
Maav 4,240,144
CV 176,176,177
CV 177,178,179
Maav 8,240,148
Maav 12,240,152
Maav 16,240,156
Maav 20,244,140
s_nop 0
Tv 220,8,17408
Tv 222,8,21504
Maav 24,244,144
Maav 28,244,148
Maav 32,244,152
Maav 36,244,156
WL 2
Maav 40,248,140
s_nop 0
Tv 224,252,17408
Tv 226,252,21504
Maav 44,248,144
Maav 48,248,148
Maav 52,248,152
Maav 56,248,156
Maav 60,252,140
s_nop 0
Ta 240,8,17920
Ta 242,8,22016
Maav 64,252,144
Maav 68,252,148
Maav 72,252,152
Maav 76,252,156
WL 2
Ta 244,252,17920
Ta 246,252,22016
Mavv 80,220,140
Mavv 84,220,144
Mavv 88,220,148
Mavv 92,220,152
Mavv 96,220,156
Mavv 100,224,140
Ra 248,6,2048
Mavv 104,224,144
Ra 252,7,2048
Mavv 108,224,148
Rv 220,6,3072
Mavv 112,224,152
Mavv 116,224,156
s_waitcnt vmcnt(6) lgkmcnt(3)
s_barrier
Maav 120,240,140
s_add_u32 m0,s76,65536
Rv 224,7,3072
Rv 180,11,20480
Maav 124,240,144
s_cmp_eq_u32 s88,1
s_cselect_b32 s86,0x20000,0
v_subrev_u32 v2,s86,v2
Maav 128,240,148
Rv 184,11,22528
v_add_u32 v2,8192,v2
Maav 132,240,152
LN 2,96,62,0
Maav 136,240,156
s_nop 0
Rv 188,11,24576
Rv 192,11,26624
Maav 140,244,140
Rv 196,11,28672
Maav 144,244,144
Maav 148,244,148
LN 2,96,63,1024
Maav 152,244,152
Maav 156,244,156
WL 4
Nvaa 140,248,160,80
Mvaa 140,252,180
LN 2,96,62,2048
Mvva 140,220,200
Mvva 140,224,220
Nvaa 144,248,164,84
Mvaa 144,252,184
s_nop 0
Ta 240,9,16384
Ta 242,10,16640
Mvva 144,220,204
LN 2,96,63,3072
Mvva 144,224,224
s_and_b32 s86,%12,458752
s_cmp_eq_u32 s86,393216
s_cselect_b32 s86,s91,0x40000000
Nvaa 148,248,168,88
s_add_u32 s86,s86,57344
EX 140
Mvaa 148,252,188
EX 141
Mvva 148,220,208
s_nop 0
Ta 244,9,16896
Ta 246,10,17152
Mvva 148,224,228
buffer_load_dword v255,v17,s[92:95],s86 offen
Nvaa 152,248,172,92
EX 142
Mvaa 152,252,192
EX 143
Mvva 152,220,212
CV 162,140,141
CV 163,142,143
Mvva 152,224,232
EX 144
Nvaa 156,248,176,96
s_nop 0
Ta 248,9,16392
Ta 250,10,16648
Mvaa 156,252,196
EX 145
Mvva 156,220,216
EX 146
Mvva 156,224,236
WL 2
Ta 252,9,16904
Ta 254,10,17160
Mvav 60,240,180
EX 147
Mvav 64,240,184
CV 166,144,145
CV 167,146,147
Mvav 68,240,188
EX 148
Mvav 72,240,192
EX 149
Mvav 76,240,196
EX 150
Mvav 200,244,180
EX 151
Mvav 204,244,184
CV 170,148,149
CV 171,150,151
Mvav 208,244,188
EX 152
Mvav 212,244,192
EX 153
Mvav 216,244,196
s_waitcnt vmcnt(5) lgkmcnt(0)
s_barrier
Mvav 100,248,180
s_add_u32 m0,s76,0
Tv 220,9,17408
Tv 222,10,17664
Mvav 104,248,184
s_nop 0
Tv 224,9,17920
Tv 226,10,18176
Mvav 108,248,188
s_cmp_eq_u32 s88,2
s_cselect_b32 s86,0x20000,0
v_subrev_u32 v1,s86,v1
Mvav 112,248,192
s_nop 0
v_add_u32 v1,8192,v1
Mvav 116,248,196
LN 1,36,58,0
Mvav 120,252,180
s_nop 0
Ta 240,9,17416
Ta 242,10,17672
Mvav 124,252,184
s_nop 0
EX 154
Mvav 128,252,188
EX 155
Mvav 132,252,192
LN 1,36,59,1024
Mvav 136,252,196
s_nop 0
Rv 180,12,20480
Rv 184,12,22528
Rv 188,12,24576
Rv 192,12,26624
Rv 196,12,28672
WL 4
Mvvv 60,220,180
WL 3
CV 174,152,153
CV 175,154,155
Mvvv 64,220,184
WL 2
EX 156
Mvvv 68,220,188
WL 1
Ta 244,9,17928
Mvvv 72,220,192
WL 1
Ta 246,10,18184
Mvvv 76,220,196
LN 1,36,60,2048
Mvvv 200,224,180
s_nop 0
EX 157
Mvvv 204,224,184
EX 158
Mvvv 208,224,188
EX 159
Mvvv 212,224,192
LN 1,36,61,3072
Mvvv 216,224,196
WL 0
s_barrier
Mvav 100,240,180
s_add_u32 m0,s76,49152
Ta 248,8,49152
Ta 250,8,53248
Mvav 104,240,184
s_cmp_eq_u32 s88,2
s_cselect_b32 s86,0x20000,0
v_subrev_u32 v2,s86,v2
Mvav 108,240,188
v_add_u32 v2,8192,v2
CV 178,156,157
Mvav 112,240,192
LN 2,96,62,0
Mvav 116,240,196
CV 179,158,159
v_mul_f32 v60,s85,v60
Mvav 120,244,180
s_nop 0
Ta 252,252,49152
Ta 254,252,53248
Mvav 124,244,184
v_mul_f32 v61,s85,v61
v_mul_f32 v62,s85,v62
Mvav 128,244,188
LN 2,96,63,1024
Mvav 132,244,192
v_mul_f32 v63,s85,v63
v_mul_f32 v100,s85,v100
Mvav 136,244,196
v_mul_f32 v101,s85,v101
v_mul_f32 v102,s85,v102
Mvvv 228,248,160
s_nop 0
Tv 220,8,49664
Tv 222,8,53760
Mvvv 232,248,164
LN 2,96,62,2048
Mvvv 236,248,168
v_mul_f32 v103,s85,v103
CV 60,60,61
Mvvv 240,248,172
CV 61,62,63
CV 62,100,101
Mvvv 244,248,176
WL 2
CV 63,102,103
v_mul_f32 v64,s85,v64
Maav 0,248,160
s_nop 0
Tv 224,252,49664
Tv 226,252,53760
Maav 4,248,164
LN 2,96,63,3072
Maav 8,248,168
v_mul_f32 v65,s85,v65
v_mul_f32 v66,s85,v66
Maav 12,248,172
v_mul_f32 v67,s85,v67
v_mul_f32 v104,s85,v104
Maav 16,248,176
v_mul_f32 v105,s85,v105
v_mul_f32 v106,s85,v106
Maav 20,252,160
s_nop 0
Ta 240,8,50176
Ta 242,8,54272
Maav 24,252,164
v_mul_f32 v107,s85,v107
CV 64,64,65
Maav 28,252,168
CV 65,66,67
CV 66,104,105
Maav 32,252,172
CV 67,106,107
v_mul_f32 v68,s85,v68
Maav 36,252,176
WL 2
v_mul_f32 v69,s85,v69
v_mul_f32 v70,s85,v70
Mavv 40,220,160
s_nop 0
Ta 244,252,50176
Ta 246,252,54272
Mavv 44,220,164
v_mul_f32 v71,s85,v71
v_mul_f32 v108,s85,v108
Mavv 48,220,168
v_mul_f32 v109,s85,v109
v_mul_f32 v110,s85,v110
Mavv 52,220,172
v_mul_f32 v111,s85,v111
CV 68,68,69
Mavv 56,220,176
CV 69,70,71
CV 70,108,109
Mavv 60,224,160
s_nop 0
Ta 248,8,50688
Ta 250,8,54784
Mavv 64,224,164
CV 71,110,111
v_mul_f32 v72,s85,v72
Mavv 68,224,168
v_mul_f32 v73,s85,v73
v_mul_f32 v74,s85,v74
Mavv 72,224,172
v_mul_f32 v75,s85,v75
v_mul_f32 v112,s85,v112
Mavv 76,224,176
WL 2
Ta 252,252,50688
Ta 254,252,54784
Maav 80,240,160
v_mul_f32 v113,s85,v113
v_mul_f32 v114,s85,v114
Maav 84,240,164
v_mul_f32 v115,s85,v115
CV 72,72,73
Maav 88,240,168
CV 73,74,75
CV 74,112,113
Maav 92,240,172
CV 75,114,115
v_mul_f32 v76,s85,v76
Maav 96,240,176
v_mul_f32 v77,s85,v77
v_mul_f32 v78,s85,v78
Maav 100,244,160
v_mul_f32 v79,s85,v79
v_mul_f32 v116,s85,v116
Maav 104,244,164
v_mul_f32 v117,s85,v117
v_mul_f32 v118,s85,v118
Maav 108,244,168
v_mul_f32 v119,s85,v119
CV 76,76,77
Maav 112,244,172
CV 77,78,79
CV 78,116,117
Maav 116,244,176
WL 0
s_barrier
Maav 120,248,160
s_add_u32 m0,s76,98304
Rv 220,6,16384
Rv 224,7,16384
Maav 124,248,164
s_cmp_eq_u32 s88,3
s_cselect_b32 s86,0x20000,0
v_subrev_u32 v2,s86,v2
Maav 128,248,168
v_add_u32 v2,8192,v2
CV 79,118,119
Maav 132,248,172
LN 2,96,62,0
Maav 136,248,176
v_mul_f32 v200,s85,v200
v_mul_f32 v201,s85,v201
Maav 140,252,160
Ra 240,6,17408
v_mul_f32 v202,s85,v202
Maav 144,252,164
v_mul_f32 v203,s85,v203
v_mul_f32 v120,s85,v120
Maav 148,252,168
LN 2,96,63,1024
Maav 152,252,172
v_mul_f32 v121,s85,v121
v_mul_f32 v122,s85,v122
Maav 156,252,176
WL 1
v_mul_f32 v123,s85,v123
CV 200,200,201
Zvvv 140,220,20
Ra 244,7,17408
CV 201,202,203
Zvvv 144,220,24
LN 2,96,62,2048
Zvvv 148,220,28
CV 202,120,121
CV 203,122,123
Zvvv 152,220,32
v_mul_f32 v204,s85,v204
v_mul_f32 v205,s85,v205
Zvvv 156,220,36
v_mul_f32 v206,s85,v206
v_mul_f32 v207,s85,v207
Mvvv 140,224,40
Ra 248,6,18432
v_mul_f32 v124,s85,v124
Mvvv 144,224,44
LN 2,96,63,3072
Mvvv 148,224,48
v_mul_f32 v125,s85,v125
v_mul_f32 v126,s85,v126
Mvvv 152,224,52
v_mul_f32 v127,s85,v127
CV 204,204,205
Mvvv 156,224,56
WL 1
CV 205,206,207
CV 206,124,125
Mvav 140,240,60
Ra 252,7,18432
CV 207,126,127
Mvav 144,240,64
v_mul_f32 v208,s85,v208
v_mul_f32 v209,s85,v209
Mvav 148,240,68
v_mul_f32 v210,s85,v210
v_mul_f32 v211,s85,v211
Mvav 152,240,72
v_mul_f32 v128,s85,v128
v_mul_f32 v129,s85,v129
v_mul_f32 v130,s85,v130
v_mul_f32 v131,s85,v131
CV 208,208,209
CV 209,210,211
CV 210,128,129
CV 211,130,131
v_mul_f32 v212,s85,v212
v_mul_f32 v213,s85,v213
v_mul_f32 v214,s85,v214
v_mul_f32 v215,s85,v215
v_mul_f32 v132,s85,v132
v_mul_f32 v133,s85,v133
v_mul_f32 v134,s85,v134
v_mul_f32 v135,s85,v135
CV 212,212,213
CV 213,214,215
CV 214,132,133
CV 215,134,135
v_mul_f32 v216,s85,v216
v_mul_f32 v217,s85,v217
v_mul_f32 v218,s85,v218
v_mul_f32 v219,s85,v219
v_mul_f32 v136,s85,v136
v_mul_f32 v137,s85,v137
v_mul_f32 v138,s85,v138
v_mul_f32 v139,s85,v139
CV 216,216,217
CV 217,218,219
CV 218,136,137
CV 219,138,139
Mvav 156,240,76
v_mul_f32 v229,0x4d800000,v229
Mvav 140,244,200
Rv 220,6,19456
Mvav 144,244,204
v_rcp_f32 v228,v228
Mvav 148,244,208
v_mul_f32 v233,0x4d800000,v233
Mvav 152,244,212
v_rcp_f32 v232,v232
Mvav 156,244,216
WL 1
Rv 224,7,19456
v_mul_f32 v237,0x4d800000,v237
Zvav 100,248,20
v_rcp_f32 v236,v236
Zvav 104,248,24
v_mul_f32 v241,0x4d800000,v241
Zvav 108,248,28
v_max3_f32 v80,v140,v141,v142
v_max3_f32 v84,v144,v145,v146
Zvav 112,248,32
v_max3_f32 v88,v148,v149,v150
v_max3_f32 v92,v152,v153,v154
Zvav 116,248,36
v_max3_f32 v96,v156,v157,v158
v_max_f32 v81,v80,v143
Mvav 100,252,40
v_max_f32 v80,v80,v143
v_max_f32 v85,v84,v147
Mvav 104,252,44
v_max_f32 v84,v84,v147
v_max_f32 v89,v88,v151
Mvav 108,252,48
v_max_f32 v88,v88,v151
v_max_f32 v93,v92,v155
Mvav 112,252,52
v_max_f32 v92,v92,v155
v_max_f32 v97,v96,v159
Mvav 116,252,56
WL 0
s_barrier
Mvvv 100,220,60
s_add_u32 m0,s76,16384
Ra 240,6,32768
Ra 244,7,32768
Mvvv 104,220,64
s_cmp_eq_u32 s88,3
s_cselect_b32 s86,0x20000,0
v_subrev_u32 v1,s86,v1
Mvvv 108,220,68
v_add_u32 v1,8192,v1
v_max_f32 v96,v96,v159
Mvvv 112,220,72
LN 1,36,58,0
Mvvv 116,220,76
v_permlane32_swap_b32 v80,v81
Mvvv 100,224,200
Ra 248,6,33792
Mvvv 104,224,204
v_permlane32_swap_b32 v84,v85
Mvvv 108,224,208
LN 1,36,59,1024
Mvvv 112,224,212
v_permlane32_swap_b32 v88,v89
v_permlane32_swap_b32 v92,v93
v_permlane32_swap_b32 v96,v97
v_max_f32 v82,v80,v81
v_max_f32 v80,v80,v81
v_max_f32 v86,v84,v85
v_max_f32 v84,v84,v85
v_max_f32 v90,v88,v89
v_max_f32 v88,v88,v89
v_max_f32 v94,v92,v93
v_max_f32 v92,v92,v93
v_max_f32 v98,v96,v97
v_max_f32 v96,v96,v97
v_permlane16_swap_b32 v80,v82
v_permlane16_swap_b32 v84,v86
v_permlane16_swap_b32 v88,v90
v_permlane16_swap_b32 v92,v94
v_permlane16_swap_b32 v96,v98
v_min_f32_e64 v81,-v80,-v82
v_min_f32_e64 v85,-v84,-v86
v_min_f32_e64 v89,-v88,-v90
v_min_f32_e64 v93,-v92,-v94
v_min_f32_e64 v97,-v96,-v98
v_min_f32_e64 v83,-v80,-v82
v_min_f32_e64 v87,-v84,-v86
v_min_f32_e64 v91,-v88,-v90
v_min_f32_e64 v95,-v92,-v94
v_min_f32_e64 v99,-v96,-v98
v_min_f32_e64 v80,-v80,-v82
v_min_f32_e64 v84,-v84,-v86
v_min_f32_e64 v88,-v88,-v90
v_min_f32_e64 v92,-v92,-v94
v_min_f32_e64 v96,-v96,-v98
v_mov_b32 v82,v80
v_mov_b32 v86,v84
v_mov_b32 v90,v88
v_mov_b32 v94,v92
v_mov_b32 v98,v96
Mvvv 116,224,216
WL 1
v_add_f32 v140,v140,v80
v_add_f32 v141,v141,v80
Nvav 160,240,20,80
Ra 252,7,33792
v_add_f32 v142,v142,v80
Nvav 164,240,24,84
LN 1,36,60,2048
Nvav 168,240,28,88
v_add_f32 v143,v143,v80
Nvav 172,240,32,92
EX 140
Nvav 176,240,36,96
EX 141
Mvav 160,244,40
Rv 220,6,34816
Mvav 164,244,44
LN 1,36,61,3072
Mvav 168,244,48
s_and_b32 s86,%12,458752
s_cmp_eq_u32 s86,458752
s_cselect_b32 s86,s91,0x40000000
Mvav 172,244,52
s_add_u32 s86,s86,61440
EX 142
Mvav 176,244,56
WL 1
EX 143
Mvav 160,248,60
Rv 224,7,34816
CV 140,140,141
Mvav 164,248,64
buffer_load_dword v255,v17,s[92:95],s86 offen
Mvav 168,248,68
CV 141,142,143
v_add_f32 v144,v144,v84
Mvav 172,248,72
v_add_f32 v145,v145,v84
v_add_f32 v146,v146,v84
Mvav 176,248,76
v_add_f32 v147,v147,v84
Mvav 160,252,200
Ra 240,6,35840
Mvav 164,252,204
EX 144
Mvav 168,252,208
EX 145
Mvav 172,252,212
EX 146
EX 147
CV 144,144,145
CV 145,146,147
v_add_f32 v148,v148,v88
v_add_f32 v149,v149,v88
v_add_f32 v150,v150,v88
v_add_f32 v151,v151,v88
EX 148
EX 149
EX 150
EX 151
CV 148,148,149
CV 149,150,151
v_add_f32 v152,v152,v92
v_add_f32 v153,v153,v92
v_add_f32 v154,v154,v92
v_add_f32 v155,v155,v92
EX 152
EX 153
EX 154
EX 155
CV 152,152,153
CV 153,154,155
v_add_f32 v156,v156,v96
v_add_f32 v157,v157,v96
v_add_f32 v158,v158,v96
v_add_f32 v159,v159,v96
EX 156
EX 157
EX 158
EX 159
CV 156,156,157
CV 157,158,159
v_add_f32 v100,v100,v80
v_add_f32 v101,v101,v80
v_add_f32 v102,v102,v80
v_add_f32 v103,v103,v80
EX 100
EX 101
EX 102
EX 103
CV 142,100,101
CV 143,102,103
Mvav 176,252,216
v_add_f32 v104,v104,v84
v_add_f32 v105,v105,v84
v_add_f32 v106,v106,v84
v_add_f32 v107,v107,v84
EX 104
EX 105
EX 106
EX 107
CV 146,104,105
CV 147,106,107
WL 1
Nvvv 100,220,20,80
v_add_f32 v108,v108,v88
v_add_f32 v109,v109,v88
v_add_f32 v110,v110,v88
v_add_f32 v111,v111,v88
EX 108
EX 109
EX 110
EX 111
CV 150,108,109
CV 151,110,111
Nvvv 104,220,24,84
v_add_f32 v112,v112,v92
v_add_f32 v113,v113,v92
v_add_f32 v114,v114,v92
v_add_f32 v115,v115,v92
EX 112
EX 113
EX 114
EX 115
CV 154,112,113
CV 155,114,115
Nvvv 108,220,28,88
v_add_f32 v116,v116,v96
v_add_f32 v117,v117,v96
v_add_f32 v118,v118,v96
v_add_f32 v119,v119,v96
EX 116
EX 117
EX 118
EX 119
CV 158,116,117
CV 159,118,119
Nvvv 112,220,32,92
Ra 244,7,35840
Nvvv 116,220,36,96
v_rcp_f32 v240,v240
Mvvv 100,224,40
v_mul_f32 v245,0x4d800000,v245
Mvvv 104,224,44
v_rcp_f32 v244,v244
Mvvv 108,224,48
v_sub_f32 v229,v229,v229
v_sub_f32 v233,v233,v233
Mvvv 112,224,52
v_sub_f32 v237,v237,v237
v_sub_f32 v241,v241,v241
Mvvv 116,224,56
WL 1
v_sub_f32 v245,v245,v245
v_add_f32 v0,v228,v229
Mvav 100,240,60
s_nop 0
Ta 248,8,32768
Ta 250,8,36864
Mvav 104,240,64
v_add_f32 v3,v232,v233
v_add_f32 v9,v236,v237
Mvav 108,240,68
)ASM"
R"ASM(v_add_f32 v10,v240,v241
v_add_f32 v11,v244,v245
Mvav 112,240,72
AR 120,0
AR 12,1
Mvav 116,240,76
WL 2
v_mul_f32 v120,v0,v120
v_mul_f32 v12,v0,v12
Mvav 100,244,200
s_nop 0
Ta 252,252,32768
Ta 254,252,36864
Mvav 104,244,204
CV 120,120,12
AR 121,2
Mvav 108,244,208
AR 253,3
v_mul_f32 v121,v0,v121
Mvav 112,244,212
v_mul_f32 v253,v0,v253
CV 121,121,253
Mvav 116,244,216
AR 122,20
AR 12,21
Zvvv 228,248,140
s_nop 0
Tv 220,8,33280
Tv 222,8,37376
Zvvv 232,248,144
v_mul_f32 v122,v0,v122
v_mul_f32 v12,v0,v12
Zvvv 236,248,148
CV 122,122,12
AR 123,22
Zvvv 240,248,152
AR 253,23
v_mul_f32 v123,v0,v123
Zvvv 244,248,156
WL 2
v_mul_f32 v253,v0,v253
CV 123,123,253
Zaav 160,248,140
s_nop 0
Tv 224,252,33280
Tv 226,252,37376
Zaav 164,248,144
AR 124,4
AR 12,5
Zaav 168,248,148
v_mul_f32 v124,v3,v124
v_mul_f32 v12,v3,v12
Zaav 172,248,152
CV 124,124,12
AR 125,6
Zaav 176,248,156
AR 253,7
v_mul_f32 v125,v3,v125
Zaav 180,252,140
s_nop 0
Ta 240,8,33792
Ta 242,8,37888
Zaav 184,252,144
v_mul_f32 v253,v3,v253
CV 125,125,253
Zaav 188,252,148
AR 126,24
AR 12,25
Zaav 192,252,152
v_mul_f32 v126,v3,v126
v_mul_f32 v12,v3,v12
Zaav 196,252,156
WL 2
CV 126,126,12
AR 127,26
Zavv 200,220,140
s_nop 0
Ta 244,252,33792
Ta 246,252,37888
Zavv 204,220,144
AR 253,27
v_mul_f32 v127,v3,v127
Zavv 208,220,148
v_mul_f32 v253,v3,v253
CV 127,127,253
Zavv 212,220,152
AR 128,8
AR 12,9
Zavv 216,220,156
v_mul_f32 v128,v9,v128
v_mul_f32 v12,v9,v12
Zavv 220,224,140
s_nop 0
Ta 248,8,34304
Ta 250,8,38400
Zavv 224,224,144
CV 128,128,12
AR 129,10
Zavv 228,224,148
AR 253,11
v_mul_f32 v129,v9,v129
Zavv 232,224,152
v_mul_f32 v253,v9,v253
CV 129,129,253
Zavv 236,224,156
WL 2
Ta 252,252,34304
Ta 254,252,38400
Zaav 0,240,140
AR 130,28
AR 12,29
v_mul_f32 v130,v9,v130
v_mul_f32 v12,v9,v12
CV 130,130,12
AR 131,30
AR 253,31
v_mul_f32 v131,v9,v131
v_mul_f32 v253,v9,v253
CV 131,131,253
Zaav 4,240,144
AR 132,12
AR 12,13
v_mul_f32 v132,v10,v132
v_mul_f32 v12,v10,v12
CV 132,132,12
AR 133,14
AR 253,15
v_mul_f32 v133,v10,v133
v_mul_f32 v253,v10,v253
CV 133,133,253
AR 134,32
AR 12,33
v_mul_f32 v134,v10,v134
v_mul_f32 v12,v10,v12
CV 134,134,12
AR 135,34
AR 253,35
v_mul_f32 v135,v10,v135
v_mul_f32 v253,v10,v253
CV 135,135,253
Zaav 8,240,148
AR 136,16
AR 12,17
v_mul_f32 v136,v11,v136
v_mul_f32 v12,v11,v12
CV 136,136,12
AR 137,18
AR 253,19
v_mul_f32 v137,v11,v137
v_mul_f32 v253,v11,v253
CV 137,137,253
AR 138,36
AR 12,37
v_mul_f32 v138,v11,v138
v_mul_f32 v12,v11,v12
CV 138,138,12
AR 139,38
AR 253,39
v_mul_f32 v139,v11,v139
v_mul_f32 v253,v11,v253
CV 139,139,253
Zaav 12,240,152
AR 180,40
AR 12,41
Zaav 16,240,156
v_mul_f32 v180,v0,v180
v_mul_f32 v12,v0,v12
Zaav 20,244,140
CV 180,180,12
AR 181,42
Zaav 24,244,144
AR 253,43
v_mul_f32 v181,v0,v181
Zaav 28,244,148
v_mul_f32 v253,v0,v253
CV 181,181,253
Zaav 32,244,152
AR 182,60
AR 12,61
v_mul_f32 v182,v0,v182
v_mul_f32 v12,v0,v12
CV 182,182,12
AR 183,62
AR 253,63
v_mul_f32 v183,v0,v183
v_mul_f32 v253,v0,v253
CV 183,183,253
Zaav 36,244,156
s_waitcnt vmcnt(13) lgkmcnt(0)
s_barrier
AR 184,44
AR 12,45
v_mul_f32 v184,v3,v184
v_mul_f32 v12,v3,v12
CV 184,184,12
AR 185,46
AR 253,47
v_mul_f32 v185,v3,v185
v_mul_f32 v253,v3,v253
CV 185,185,253
AR 186,64
AR 12,65
v_mul_f32 v186,v3,v186
v_mul_f32 v12,v3,v12
CV 186,186,12
AR 187,66
AR 253,67
v_mul_f32 v187,v3,v187
v_mul_f32 v253,v3,v253
CV 187,187,253
Zaav 40,248,140
AR 188,48
AR 12,49
v_mul_f32 v188,v9,v188
v_mul_f32 v12,v9,v12
CV 188,188,12
AR 189,50
AR 253,51
v_mul_f32 v189,v9,v189
v_mul_f32 v253,v9,v253
CV 189,189,253
AR 190,68
AR 12,69
v_mul_f32 v190,v9,v190
v_mul_f32 v12,v9,v12
CV 190,190,12
AR 191,70
AR 253,71
v_mul_f32 v191,v9,v191
v_mul_f32 v253,v9,v253
CV 191,191,253
Zaav 44,248,144
AR 192,52
AR 12,53
v_mul_f32 v192,v10,v192
v_mul_f32 v12,v10,v12
CV 192,192,12
AR 193,54
AR 253,55
v_mul_f32 v193,v10,v193
v_mul_f32 v253,v10,v253
CV 193,193,253
AR 194,72
AR 12,73
v_mul_f32 v194,v10,v194
v_mul_f32 v12,v10,v12
CV 194,194,12
AR 195,74
AR 253,75
v_mul_f32 v195,v10,v195
v_mul_f32 v253,v10,v253
CV 195,195,253
Zaav 48,248,148
AR 196,56
AR 12,57
v_mul_f32 v196,v11,v196
v_mul_f32 v12,v11,v12
CV 196,196,12
AR 197,58
AR 253,59
v_mul_f32 v197,v11,v197
v_mul_f32 v253,v11,v253
CV 197,197,253
AR 198,76
AR 12,77
v_mul_f32 v198,v11,v198
v_mul_f32 v12,v11,v12
CV 198,198,12
AR 199,78
AR 253,79
v_mul_f32 v199,v11,v199
v_mul_f32 v253,v11,v253
CV 199,199,253
Zaav 52,248,152
s_add_u32 m0,s76,32768
Rv 220,6,0
Rv 224,7,0
Zaav 56,248,156
Ra 240,6,1024
AR 254,80
Zaav 60,252,140
s_cmp_eq_u32 s88,4
s_cselect_b32 s86,0x20000,0
Ra 244,7,1024
Zaav 64,252,144
v_subrev_u32 v1,s86,v1
v_add_u32 v1,8192,v1
Zaav 68,252,148
LN 1,36,58,0
Zaav 72,252,152
EX 160
Zaav 76,252,156
WL 3
EX 161
Nvvv 140,220,20,80
WL 2
EX 162
Mvvv 140,224,40
WL 1
LN 1,36,59,1024
Mvav 140,240,60
WL 0
EX 163
Mvav 140,244,200
CV 160,160,161
CV 161,162,163
Nvvv 144,220,24,84
EX 164
Mvvv 144,224,44
LN 1,36,60,2048
Mvav 144,240,64
EX 165
Mvav 144,244,204
EX 166
Nvvv 148,220,28,88
EX 167
Mvvv 148,224,48
LN 1,36,61,3072
Mvav 148,240,68
s_add_u32 m0,s76,81920
Ta 248,8,16384
Ta 250,8,20480
Mvav 148,244,208
s_cmp_eq_u32 s88,4
s_cselect_b32 s86,0x20000,0
v_subrev_u32 v2,s86,v2
Nvvv 152,220,32,92
v_add_u32 v2,8192,v2
CV 164,164,165
Mvvv 152,224,52
LN 2,96,62,0
Mvav 152,240,72
CV 165,166,167
AR 253,81
Mvav 152,244,212
s_nop 0
Ta 252,252,16384
Ta 254,252,20480
Nvvv 156,220,36,96
EX 168
Mvvv 156,224,56
EX 169
EX 170
EX 171
CV 168,168,169
CV 169,170,171
EX 172
EX 173
EX 174
EX 175
CV 172,172,173
CV 173,174,175
EX 176
EX 177
EX 178
EX 179
CV 176,176,177
CV 177,178,179
EX 100
EX 101
EX 102
EX 103
CV 162,100,101
CV 163,102,103
Mvav 156,240,76
EX 104
EX 105
EX 106
EX 107
CV 166,104,105
CV 167,106,107
Mvav 156,244,216
EX 108
EX 109
EX 110
EX 111
CV 170,108,109
CV 171,110,111
Mvvv 228,248,160
EX 112
EX 113
EX 114
EX 115
CV 174,112,113
CV 175,114,115
Mvvv 232,248,164
EX 116
EX 117
EX 118
EX 119
CV 178,116,117
CV 179,118,119
Mvvv 236,248,168
s_nop 0
Tv 220,8,16896
Tv 222,8,20992
Mvvv 240,248,172
LN 2,96,63,1024
Mvvv 244,248,176
WL 2
EX 140
Maav 160,248,160
s_nop 0
Tv 224,252,16896
Tv 226,252,20992
Maav 164,248,164
EX 141
Maav 168,248,168
LN 2,96,62,2048
Maav 172,248,172
EX 142
Maav 176,248,176
EX 143
Maav 180,252,160
Ra 240,6,2048
CV 140,140,141
Maav 184,252,164
Ra 244,7,2048
CV 141,142,143
Maav 188,252,168
Ra 248,6,3072
v_mul_f32 v254,v0,v254
Maav 192,252,172
LN 2,96,63,3072
Maav 196,252,176
WL 3
Ra 252,7,3072
v_mul_f32 v253,v0,v253
Mavv 200,220,160
EX 144
Mavv 204,220,164
EX 145
Mavv 208,220,168
EX 146
Mavv 212,220,172
EX 147
Mavv 216,220,176
CV 144,144,145
CV 145,146,147
Mavv 220,224,160
EX 148
Mavv 224,224,164
EX 149
Mavv 228,224,168
EX 150
Mavv 232,224,172
EX 151
Mavv 236,224,176
WL 0
CV 148,148,149
CV 149,150,151
Nvav 100,240,20,80
EX 152
Mvav 100,244,40
EX 153
Mvav 100,248,60
EX 154
Mvav 100,252,200
EX 155
Nvav 104,240,24,84
CV 152,152,153
CV 153,154,155
Mvav 104,244,44
s_nop 0
Tv 220,8,17408
Tv 222,8,21504
Mvav 104,248,64
EX 156
Mvav 104,252,204
EX 157
Nvav 108,240,28,88
EX 158
Mvav 108,244,48
EX 159
Mvav 108,248,68
s_nop 0
Tv 224,252,17408
Tv 226,252,21504
Mvav 108,252,208
CV 156,156,157
CV 157,158,159
Nvav 112,240,32,92
EX 100
Mvav 112,244,52
EX 101
Mvav 112,248,72
EX 102
Mvav 112,252,212
EX 103
Nvav 116,240,36,96
s_nop 0
Ta 240,8,17920
Ta 242,8,22016
Mvav 116,244,56
CV 142,100,101
CV 143,102,103
Mvav 116,248,76
EX 104
Mvav 116,252,216
WL 2
Ta 244,252,17920
Ta 246,252,22016
Mavv 0,220,160
EX 105
Mavv 4,220,164
EX 106
Mavv 8,220,168
EX 107
Mavv 12,220,172
CV 146,104,105
CV 147,106,107
Mavv 16,220,176
EX 108
Mavv 20,224,160
EX 109
Mavv 24,224,164
EX 110
Mavv 28,224,168
EX 111
Mavv 32,224,172
CV 150,108,109
CV 151,110,111
Mavv 36,224,176
s_waitcnt vmcnt(9) lgkmcnt(0)
s_barrier
Maav 40,240,160
s_add_u32 m0,s76,0
Ra 248,6,16384
Ra 252,7,16384
Maav 44,240,164
s_cmp_eq_u32 s88,5
s_cselect_b32 s86,0x20000,0
Rv 220,6,17408
Maav 48,240,168
Rv 224,7,17408
v_subrev_u32 v1,s86,v1
Maav 52,240,172
v_add_u32 v1,8192,v1
CV 254,254,253
Maav 56,240,176
LN 1,36,58,0
Maav 60,244,160
EX 112
Maav 64,244,164
EX 113
Maav 68,244,168
EX 114
Maav 72,244,172
LN 1,36,59,1024
Maav 76,244,176
WL 1
EX 115
Nvav 160,248,20,80
CV 154,112,113
CV 155,114,115
Mvav 160,252,40
EX 116
Mvvv 160,220,60
WL 0
LN 1,36,60,2048
Mvvv 160,224,200
EX 117
Nvav 164,248,24,84
EX 118
Mvav 164,252,44
EX 119
Mvvv 164,220,64
LN 1,36,61,3072
Mvvv 164,224,204
s_add_u32 m0,s76,65536
s_cmp_eq_u32 s88,5
s_cselect_b32 s86,0x20000,0
Nvav 168,248,28,88
v_subrev_u32 v2,s86,v2
v_add_u32 v2,8192,v2
Mvav 168,252,48
CV 158,116,117
CV 159,118,119
Mvvv 168,220,68
s_nop 0
Ta 240,8,0
Ta 242,8,4096
Mvvv 168,224,208
LN 2,96,62,0
Nvav 172,248,32,92
EX 160
Mvav 172,252,52
EX 161
Mvvv 172,220,72
EX 162
Mvvv 172,224,212
s_nop 0
Ta 244,252,0
Ta 246,252,4096
Nvav 176,248,36,96
LN 2,96,63,1024
Mvav 176,252,56
EX 163
Mvvv 176,220,76
CV 160,160,161
CV 161,162,163
Mvvv 176,224,216
EX 164
Mvvv 228,248,140
s_nop 0
Ta 248,8,512
Ta 250,8,4608
Mvvv 232,248,144
LN 2,96,62,2048
Mvvv 236,248,148
EX 165
Mvvv 240,248,152
EX 166
Mvvv 244,248,156
WL 2
EX 167
Maav 160,240,140
s_nop 0
Ta 252,252,512
Ta 254,252,4608
Maav 164,240,144
LN 2,96,63,3072
Maav 168,240,148
s_and_b32 s86,%12,458752
s_cmp_eq_u32 s86,0
s_cselect_b32 s86,s91,0x40000000
Maav 172,240,152
s_add_u32 s86,s86,65536
CV 164,164,165
CV 165,166,167
Maav 176,240,156
EX 168
Maav 180,244,140
Rv 220,6,18432
AW 80,254
Maav 184,244,144
Rv 224,7,18432
AR 12,82
Maav 188,244,148
Ra 240,6,19456
AR 253,83
Maav 192,244,152
buffer_load_dword v255,v17,s[92:95],s86 offen
Maav 196,244,156
WL 3
Ra 244,7,19456
v_mul_f32 v12,v0,v12
Maav 200,248,140
EX 169
Maav 204,248,144
EX 170
Maav 208,248,148
EX 171
Maav 212,248,152
CV 168,168,169
CV 169,170,171
Maav 216,248,156
EX 172
Maav 220,252,140
EX 173
Maav 224,252,144
EX 174
Maav 228,252,148
EX 175
Maav 232,252,152
CV 172,172,173
CV 173,174,175
Maav 236,252,156
WL 0
EX 176
Nvvv 100,220,20,80
EX 177
Mvvv 100,224,40
EX 178
Mvav 100,240,60
EX 179
Mvav 100,244,200
CV 176,176,177
CV 177,178,179
Nvvv 104,220,24,84
ds_write_b128 v15,v[120:123] offset:53248
Mvvv 104,224,44
v_mul_f32 v253,v0,v253
CV 12,12,253
Mvav 104,240,64
AW 81,12
AR 254,100
Mvav 104,244,204
EX 100
Nvvv 108,220,28,88
EX 101
Mvvv 108,224,48
Ta 248,8,1024
AR 253,101
Mvav 108,240,68
s_nop 0
Ta 250,8,5120
Ta 252,252,1024
Mvav 108,244,208
Ta 254,252,5120
v_mul_f32 v254,v0,v254
Nvvv 112,220,32,92
EX 102
Mvvv 112,224,52
EX 103
Mvav 112,240,72
CV 162,100,101
CV 163,102,103
Mvav 112,244,212
EX 104
Nvvv 116,220,36,96
s_nop 0
Tv 220,8,1536
Tv 222,8,5632
Mvvv 116,224,56
EX 105
Mvav 116,240,76
EX 106
Mvav 116,244,216
WL 2
Tv 224,252,1536
Tv 226,252,5632
Maav 0,248,140
EX 107
Maav 4,248,144
CV 166,104,105
CV 167,106,107
Maav 8,248,148
EX 108
Maav 12,248,152
EX 109
Maav 16,248,156
EX 110
Maav 20,252,140
EX 111
Maav 24,252,144
CV 170,108,109
CV 171,110,111
Maav 28,252,148
EX 112
Maav 32,252,152
EX 113
Maav 36,252,156
s_waitcnt vmcnt(13) lgkmcnt(0)
s_barrier
Mavv 40,220,140
s_add_u32 m0,s76,16384
Ra 240,6,32768
Ra 244,7,32768
Mavv 44,220,144
s_cmp_eq_u32 s88,6
s_cselect_b32 s86,0x20000,0
Ra 248,6,33792
Mavv 48,220,148
Ra 252,7,33792
v_subrev_u32 v1,s86,v1
Mavv 52,220,152
v_add_u32 v1,8192,v1
v_mul_f32 v253,v0,v253
Mavv 56,220,156
LN 1,36,58,0
Mavv 60,224,140
EX 114
Mavv 64,224,144
EX 115
Mavv 68,224,148
CV 174,112,113
CV 175,114,115
Mavv 72,224,152
LN 1,36,59,1024
Mavv 76,224,156
WL 1
EX 116
Nvav 140,240,20,80
EX 117
Mvav 140,244,40
EX 118
Mvav 140,248,60
WL 0
LN 1,36,60,2048
Mvav 140,252,200
EX 119
Nvav 144,240,24,84
CV 178,116,117
CV 179,118,119
Mvav 144,244,44
ds_write_b128 v15,v[124:127] offset:54272
Mvav 144,248,64
LN 1,36,61,3072
Mvav 144,252,204
s_add_u32 m0,s76,49152
s_cmp_eq_u32 s88,6
s_cselect_b32 s86,0x20000,0
Nvav 148,240,28,88
v_subrev_u32 v2,s86,v2
v_add_u32 v2,8192,v2
Mvav 148,244,48
EX 140
Mvav 148,248,68
Tv 220,8,49152
CV 254,254,253
Mvav 148,252,208
Tv 222,8,53248
AW 82,254
Nvav 152,240,32,92
LN 2,96,62,0
Mvav 152,244,52
EX 141
Mvav 152,248,72
EX 142
Mvav 152,252,212
s_nop 0
Tv 224,252,49152
Tv 226,252,53248
Nvav 156,240,36,96
LN 2,96,63,1024
Mvav 156,244,56
EX 143
Mvav 156,248,76
CV 140,140,141
CV 141,142,143
Mvav 156,252,216
EX 144
Mvvv 228,248,160
s_nop 0
Ta 240,8,49664
Ta 242,8,53760
Mvvv 232,248,164
LN 2,96,62,2048
Mvvv 236,248,168
EX 145
Mvvv 240,248,172
EX 146
Mvvv 244,248,176
WL 2
EX 147
Mavv 160,220,160
s_nop 0
Ta 244,252,49664
Ta 246,252,53760
Mavv 164,220,164
LN 2,96,63,3072
Mavv 168,220,168
CV 144,144,145
CV 145,146,147
Mavv 172,220,172
EX 148
Mavv 176,220,176
EX 149
Mavv 180,224,160
Ra 248,6,34816
AR 12,102
Mavv 184,224,164
Ra 252,7,34816
AR 253,103
Mavv 188,224,168
Rv 220,6,35840
v_mul_f32 v12,v0,v12
Mavv 192,224,172
EX 150
Mavv 196,224,176
WL 3
Rv 224,7,35840
v_mul_f32 v253,v0,v253
Maav 200,240,160
EX 151
Maav 204,240,164
CV 148,148,149
CV 149,150,151
Maav 208,240,168
EX 152
Maav 212,240,172
EX 153
Maav 216,240,176
EX 154
Maav 220,244,160
EX 155
Maav 224,244,164
CV 152,152,153
CV 153,154,155
Maav 228,244,168
EX 156
Maav 232,244,172
EX 157
Maav 236,244,176
WL 0
EX 158
Nvav 100,248,20,80
EX 159
Mvav 100,252,40
CV 156,156,157
CV 157,158,159
Mvvv 100,220,60
ds_write_b128 v15,v[128:131] offset:55296
Mvvv 100,224,200
CV 12,12,253
AW 83,12
Nvav 104,248,24,84
AR 254,84
AR 253,85
Mvav 104,252,44
v_mul_f32 v254,v3,v254
v_mul_f32 v253,v3,v253
Mvvv 104,220,64
CV 254,254,253
AW 84,254
Mvvv 104,224,204
Ta 240,8,50176
AR 12,86
Nvav 108,248,28,88
Ta 242,8,54272
AR 253,87
Mvav 108,252,48
EX 100
Mvvv 108,220,68
s_nop 0
Ta 244,252,50176
Ta 246,252,54272
Mvvv 108,224,208
EX 101
Nvav 112,248,32,92
EX 102
Mvav 112,252,52
EX 103
Mvvv 112,220,72
CV 142,100,101
CV 143,102,103
Mvvv 112,224,212
EX 104
Nvav 116,248,36,96
s_nop 0
Ta 248,8,50688
Ta 250,8,54784
Mvav 116,252,56
EX 105
Mvvv 116,220,76
EX 106
Mvvv 116,224,216
WL 2
Ta 252,252,50688
Ta 254,252,54784
Maav 0,240,160
EX 107
Maav 4,240,164
CV 146,104,105
CV 147,106,107
Maav 8,240,168
EX 108
Maav 12,240,172
EX 109
Maav 16,240,176
EX 110
Maav 20,244,160
EX 111
Maav 24,244,164
CV 150,108,109
CV 151,110,111
Maav 28,244,168
EX 112
Maav 32,244,172
EX 113
Maav 36,244,176
s_waitcnt vmcnt(13) lgkmcnt(0)
s_barrier
Maav 40,248,160
s_add_u32 m0,s76,32768
Rv 220,6,0
Rv 224,7,0
Maav 44,248,164
s_cmp_eq_u32 s88,7
s_cselect_b32 s86,0x20000,0
Ra 240,6,1024
Maav 48,248,168
Ra 244,7,1024
v_subrev_u32 v1,s86,v1
Maav 52,248,172
v_add_u32 v1,8192,v1
v_mul_f32 v12,v3,v12
Maav 56,248,176
LN 1,36,58,0
Maav 60,252,160
EX 114
Maav 64,252,164
EX 115
Maav 68,252,168
CV 154,112,113
CV 155,114,115
Maav 72,252,172
LN 1,36,59,1024
Maav 76,252,176
WL 1
EX 116
Nvvv 160,220,20,80
EX 117
Mvvv 160,224,40
EX 118
Mvav 160,240,60
WL 0
LN 1,36,60,2048
Mvav 160,244,200
EX 119
Nvvv 164,220,24,84
CV 158,116,117
CV 159,118,119
Mvvv 164,224,44
ds_write_b128 v15,v[132:135] offset:56320
Mvav 164,240,64
LN 1,36,61,3072
Mvav 164,244,204
s_add_u32 m0,s76,98304
s_cmp_eq_u32 s88,7
s_cselect_b32 s86,0x20000,0
Nvvv 168,220,28,88
v_subrev_u32 v2,s86,v2
v_add_u32 v2,8192,v2
Mvvv 168,224,48
EX 160
Mvav 168,240,68
Ta 248,8,32768
v_mul_f32 v253,v3,v253
Mvav 168,244,208
Ta 250,8,36864
CV 12,12,253
Nvvv 172,220,32,92
LN 2,96,62,0
Mvvv 172,224,52
EX 161
Mvav 172,240,72
EX 162
Mvav 172,244,212
s_nop 0
Ta 252,252,32768
Ta 254,252,36864
Nvvv 176,220,36,96
LN 2,96,63,1024
Mvvv 176,224,56
EX 163
Mvav 176,240,76
CV 160,160,161
CV 161,162,163
Mvav 176,244,216
EX 164
Mvvv 228,248,140
s_nop 0
Tv 220,8,33280
Tv 222,8,37376
Mvvv 232,248,144
LN 2,96,62,2048
Mvvv 236,248,148
EX 165
Mvvv 240,248,152
EX 166
Mvvv 244,248,156
WL 2
EX 167
Maav 160,248,140
s_nop 0
Tv 224,252,33280
Tv 226,252,37376
Maav 164,248,144
LN 2,96,63,3072
Maav 168,248,148
s_mul_i32 s86,%8,73728
s_add_u32 s68,s86,0
s_add_u32 s69,s86,8192
Maav 172,248,152
s_add_u32 s70,s86,16384
s_add_u32 s71,s86,24576
s_add_u32 s72,s86,36864
Maav 176,248,156
s_add_u32 s73,s86,45056
s_add_u32 s74,s86,53248
Maav 180,252,140
s_add_u32 s75,s86,61440
s_mov_b64 s[28:29],%6
Ra 240,6,2048
Maav 184,252,144
s_sub_u32 s28,s28,1152
s_subb_u32 s29,s29,0
Ra 244,7,2048
Maav 188,252,148
Ra 248,6,3072
CV 164,164,165
Maav 192,252,152
CV 165,166,167
AW 85,12
Maav 196,252,156
WL 3
Ra 252,7,3072
AR 254,104
Mavv 200,220,140
EX 168
Mavv 204,220,144
EX 169
Mavv 208,220,148
EX 170
Mavv 212,220,152
EX 171
Mavv 216,220,156
CV 168,168,169
CV 169,170,171
Mavv 220,224,140
EX 172
Mavv 224,224,144
EX 173
Mavv 228,224,148
EX 174
Mavv 232,224,152
EX 175
Mavv 236,224,156
WL 0
CV 172,172,173
CV 173,174,175
Nvav 100,240,20,80
EX 176
Mvav 100,244,40
EX 177
Mvav 100,248,60
EX 178
Mvav 100,252,200
EX 179
Nvav 104,240,24,84
CV 176,176,177
CV 177,178,179
Mvav 104,244,44
s_nop 0
Tv 220,8,33792
Tv 222,8,37888
Mvav 104,248,64
ds_write_b128 v15,v[136:139] offset:57344
Mvav 104,252,204
EX 100
Nvav 108,240,28,88
EX 101
Mvav 108,244,48
EX 102
Mvav 108,248,68
EX 103
Mvav 108,252,208
Tv 224,252,33792
CV 162,100,101
Nvav 112,240,32,92
Tv 226,252,37888
CV 163,102,103
Mvav 112,244,52
EX 104
Mvav 112,248,72
EX 105
Mvav 112,252,212
EX 106
Nvav 116,240,36,96
s_nop 0
Ta 240,8,34304
Ta 242,8,38400
Mvav 116,244,56
EX 107
Mvav 116,248,76
CV 166,104,105
CV 167,106,107
Mvav 116,252,216
WL 3
Ta 244,252,34304
Ta 246,252,38400
Mavv 0,220,140
EX 108
Mavv 4,220,144
EX 109
Mavv 8,220,148
EX 110
Mavv 12,220,152
EX 111
Mavv 16,220,156
WL 2
CV 170,108,109
CV 171,110,111
Mavv 20,224,140
EX 112
Mavv 24,224,144
EX 113
Mavv 28,224,148
EX 114
Mavv 32,224,152
EX 115
Mavv 36,224,156
s_waitcnt vmcnt(12) lgkmcnt(0)
s_barrier
Maav 40,240,140
s_add_u32 m0,s76,0
Ra 248,6,16384
Ra 252,7,16384
Maav 44,240,144
s_cmp_eq_u32 s88,8
s_cselect_b32 s86,0x20000,0
Rv 220,6,17408
Maav 48,240,148
Rv 224,7,17408
v_subrev_u32 v1,s86,v1
Maav 52,240,152
v_add_u32 v1,8192,v1
CV 174,112,113
Maav 56,240,156
LN 1,36,58,0
Maav 60,244,140
CV 175,114,115
AR 253,105
Maav 64,244,144
EX 116
Maav 68,244,148
EX 117
Maav 72,244,152
LN 1,36,59,1024
Maav 76,244,156
WL 1
EX 118
Nvav 140,248,20,80
EX 119
Mvav 140,252,40
CV 178,116,117
CV 179,118,119
Mvvv 140,220,60
WL 0
LN 1,36,60,2048
Mvvv 140,224,200
ds_write_b128 v15,v[180:183] offset:58368
Nvav 144,248,24,84
v_mul_f32 v254,v3,v254
v_mul_f32 v253,v3,v253
Mvav 144,252,44
CV 254,254,253
AW 86,254
Mvvv 144,220,64
LN 1,36,61,3072
Mvvv 144,224,204
s_add_u32 m0,s76,81920
s_cmp_eq_u32 s88,8
s_cselect_b32 s86,0x20000,0
Nvav 148,248,28,88
v_subrev_u32 v2,s86,v2
v_add_u32 v2,8192,v2
Mvav 148,252,48
EX 140
Mvvv 148,220,68
s_nop 0
Ta 240,8,16384
Ta 242,8,20480
Mvvv 148,224,208
LN 2,96,62,0
Nvav 152,248,32,92
EX 141
Mvav 152,252,52
EX 142
Mvvv 152,220,72
EX 143
Mvvv 152,224,212
s_nop 0
Ta 244,252,16384
Ta 246,252,20480
Nvav 156,248,36,96
LN 2,96,63,1024
Mvav 156,252,56
CV 140,140,141
CV 141,142,143
Mvvv 156,220,76
EX 144
Mvvv 156,224,216
EX 145
Mvvv 228,248,160
s_nop 0
Ta 248,8,16896
Ta 250,8,20992
Mvvv 232,248,164
LN 2,96,62,2048
Mvvv 236,248,168
EX 146
Mvvv 240,248,172
EX 147
Mvvv 244,248,176
WL 2
CV 144,144,145
CV 145,146,147
Maav 160,240,160
s_nop 0
Ta 252,252,16896
Ta 254,252,20992
Maav 164,240,164
LN 2,96,63,3072
Maav 168,240,168
s_and_b32 s86,%12,458752
s_cmp_eq_u32 s86,65536
s_cselect_b32 s86,s91,0x40000000
Maav 172,240,172
s_add_u32 s86,s86,69632
EX 148
Maav 176,240,176
EX 149
Maav 180,244,160
Rv 220,6,18432
AR 12,106
Maav 184,244,164
Rv 224,7,18432
AR 253,107
Maav 188,244,168
Ra 240,6,19456
v_mul_f32 v12,v3,v12
Maav 192,244,172
buffer_load_dword v255,v17,s[92:95],s86 offen
Maav 196,244,176
WL 3
Ra 244,7,19456
v_mul_f32 v253,v3,v253
Maav 200,248,160
EX 150
Maav 204,248,164
EX 151
Maav 208,248,168
CV 148,148,149
CV 149,150,151
Maav 212,248,172
EX 152
Maav 216,248,176
EX 153
Maav 220,252,160
EX 154
Maav 224,252,164
EX 155
Maav 228,252,168
CV 152,152,153
CV 153,154,155
Maav 232,252,172
EX 156
Maav 236,252,176
WL 0
EX 157
Nvvv 100,220,20,80
EX 158
Mvvv 100,224,40
EX 159
Mvav 100,240,60
CV 156,156,157
CV 157,158,159
Mvav 100,244,200
ds_write_b128 v15,v[184:187] offset:59392
Nvvv 104,220,24,84
CV 12,12,253
AW 87,12
Mvvv 104,224,44
AR 254,88
AR 253,89
Mvav 104,240,64
v_mul_f32 v254,v9,v254
v_mul_f32 v253,v9,v253
Mvav 104,244,204
EX 100
Nvvv 108,220,28,88
Ta 248,8,17408
CV 254,254,253
Mvvv 108,224,48
Ta 250,8,21504
AW 88,254
Mvav 108,240,68
s_nop 0
Ta 252,252,17408
Ta 254,252,21504
Mvav 108,244,208
EX 101
Nvvv 112,220,32,92
EX 102
Mvvv 112,224,52
EX 103
Mvav 112,240,72
CV 142,100,101
CV 143,102,103
Mvav 112,244,212
EX 104
Nvvv 116,220,36,96
s_nop 0
Tv 220,8,17920
Tv 222,8,22016
Mvvv 116,224,56
EX 105
Mvav 116,240,76
EX 106
Mvav 116,244,216
WL 2
Tv 224,252,17920
Tv 226,252,22016
Maav 0,248,160
EX 107
Maav 4,248,164
CV 146,104,105
CV 147,106,107
Maav 8,248,168
EX 108
Maav 12,248,172
EX 109
Maav 16,248,176
EX 110
Maav 20,252,160
EX 111
Maav 24,252,164
CV 150,108,109
CV 151,110,111
Maav 28,252,168
EX 112
Maav 32,252,172
EX 113
Maav 36,252,176
s_waitcnt vmcnt(13) lgkmcnt(0)
s_barrier
Mavv 40,220,160
s_add_u32 m0,s76,16384
Ra 240,6,32768
Ra 244,7,32768
Mavv 44,220,164
s_cmp_eq_u32 s88,9
s_cselect_b32 s86,0x20000,0
Ra 248,6,33792
Mavv 48,220,168
s_nop 0
Ra 252,7,33792
v_subrev_u32 v1,s86,v1
Mavv 52,220,172
s_nop 0
v_add_u32 v1,8192,v1
AR 12,90
Mavv 56,220,176
LN 1,36,58,0
Mavv 60,224,160
s_nop 0
EX 114
Mavv 64,224,164
EX 115
Mavv 68,224,168
s_nop 0
CV 154,112,113
CV 155,114,115
Mavv 72,224,172
LN 1,36,59,1024
Mavv 76,224,176
WL 1
EX 116
Nvav 160,240,20,80
EX 117
Mvav 160,244,40
EX 118
Mvav 160,248,60
WL 0
LN 1,36,60,2048
Mvav 160,252,200
EX 119
Nvav 164,240,24,84
CV 158,116,117
CV 159,118,119
Mvav 164,244,44
ds_write_b128 v15,v[188:191] offset:60416
Mvav 164,248,64
LN 1,36,61,3072
Mvav 164,252,204
s_add_u32 m0,s76,65536
s_cmp_eq_u32 s88,9
s_cselect_b32 s86,0x20000,0
Nvav 168,240,28,88
v_subrev_u32 v2,s86,v2
v_add_u32 v2,8192,v2
Mvav 168,244,48
EX 160
Mvav 168,248,68
Tv 220,8,0
AR 253,91
Mvav 168,252,208
Tv 222,8,4096
v_mul_f32 v12,v9,v12
Nvav 172,240,32,92
LN 2,96,62,0
Mvav 172,244,52
ds_write_b128 v15,v[192:195] offset:61440
Mvav 172,248,72
EX 161
Mvav 172,252,212
EX 162
Nvav 176,240,36,96
LN 2,96,63,1024
Mvav 176,244,56
EX 163
Mvav 176,248,76
Tv 224,252,0
CV 160,160,161
Mvav 176,252,216
Tv 226,252,4096
CV 161,162,163
Mvvv 228,248,140
s_nop 0
Ta 240,8,512
Ta 242,8,4608
Mvvv 232,248,144
LN 2,96,62,2048
Mvvv 236,248,148
ds_write_b128 v15,v[196:199] offset:62464
Mvvv 240,248,152
EX 164
Mvvv 244,248,156
WL 5
EX 165
Mavv 160,220,140
LN 2,96,63,3072
Mavv 164,220,144
EX 166
Mavv 168,220,148
Ta 244,252,512
v_mul_f32 v253,v9,v253
Mavv 172,220,152
Ta 246,252,4608
CV 12,12,253
Mavv 176,220,156
WL 3
EX 167
Mavv 180,224,140
Ra 248,6,34816
CV 164,164,165
Mavv 184,224,144
Ra 252,7,34816
CV 165,166,167
Mavv 188,224,148
Rv 220,6,35840
AW 89,12
Mavv 192,224,152
EX 168
Mavv 196,224,156
Rv 224,7,35840
AR 254,108
Maav 200,240,140
EX 169
Maav 204,240,144
EX 170
Maav 208,240,148
EX 171
Maav 212,240,152
CV 168,168,169
CV 169,170,171
Maav 216,240,156
WL 2
EX 172
Maav 220,244,140
EX 173
Maav 224,244,144
EX 174
Maav 228,244,148
EX 175
Maav 232,244,152
CV 172,172,173
CV 173,174,175
Maav 236,244,156
EX 176
Nvav 100,248,20,80
EX 177
Mvav 100,252,40
WL 0
EX 178
Mvvv 100,220,60
EX 179
Mvvv 100,224,200
CV 176,176,177
CV 177,178,179
Nvav 104,248,24,84
AR 253,109
v_mul_f32 v254,v9,v254
Mvav 104,252,44
s_nop 0
Ta 240,8,1024
Ta 242,8,5120
Mvvv 104,220,64
v_mul_f32 v253,v9,v253
CV 254,254,253
Mvvv 104,224,204
EX 100
Nvav 108,248,28,88
EX 101
Mvav 108,252,48
EX 102
Mvvv 108,220,68
s_nop 0
Ta 244,252,1024
Ta 246,252,5120
Mvvv 108,224,208
EX 103
Nvav 112,248,32,92
CV 162,100,101
CV 163,102,103
Mvav 112,252,52
EX 104
Mvvv 112,220,72
EX 105
Mvvv 112,224,212
EX 106
Nvav 116,248,36,96
s_nop 0
Ta 248,8,1536
Ta 250,8,5632
Mvav 116,252,56
EX 107
Mvvv 116,220,76
CV 166,104,105
CV 167,106,107
Mvvv 116,224,216
WL 2
Ta 252,252,1536
Ta 254,252,5632
Maav 0,240,140
EX 108
Maav 4,240,144
EX 109
Maav 8,240,148
EX 110
Maav 12,240,152
EX 111
Maav 16,240,156
CV 170,108,109
CV 171,110,111
Maav 20,244,140
EX 112
Maav 24,244,144
EX 113
Maav 28,244,148
EX 114
Maav 32,244,152
EX 115
Maav 36,244,156
s_waitcnt vmcnt(13) lgkmcnt(0)
s_barrier
Maav 40,248,140
s_add_u32 m0,s76,32768
Rv 220,6,0
Rv 224,7,0
Maav 44,248,144
s_cmp_eq_u32 s88,10
s_cselect_b32 s86,0x20000,0
Ra 240,6,1024
Maav 48,248,148
Ra 244,7,1024
v_subrev_u32 v1,s86,v1
Maav 52,248,152
v_add_u32 v1,8192,v1
CV 174,112,113
Maav 56,248,156
LN 1,36,58,0
Maav 60,252,140
CV 175,114,115
AW 90,254
Maav 64,252,144
EX 116
Maav 68,252,148
EX 117
Maav 72,252,152
LN 1,36,59,1024
Maav 76,252,156
WL 1
EX 118
Nvvv 140,220,20,80
EX 119
Mvvv 140,224,40
CV 178,116,117
CV 179,118,119
Mvav 140,240,60
WL 0
LN 1,36,60,2048
Mvav 140,244,200
AR 12,110
AR 253,111
Nvvv 144,220,24,84
v_mul_f32 v12,v9,v12
v_mul_f32 v253,v9,v253
Mvvv 144,224,44
CV 12,12,253
AW 91,12
Mvav 144,240,64
LN 1,36,61,3072
Mvav 144,244,204
s_add_u32 m0,s76,49152
s_cmp_eq_u32 s88,10
s_cselect_b32 s86,0x20000,0
Nvvv 148,220,28,88
v_subrev_u32 v2,s86,v2
v_add_u32 v2,8192,v2
Mvvv 148,224,48
EX 140
Mvav 148,240,68
s_nop 0
Ta 248,8,49152
Ta 250,8,53248
Mvav 148,244,208
LN 2,96,62,0
Nvvv 152,220,32,92
EX 141
Mvvv 152,224,52
EX 142
Mvav 152,240,72
EX 143
Mvav 152,244,212
s_nop 0
Ta 252,252,49152
Ta 254,252,53248
Nvvv 156,220,36,96
LN 2,96,63,1024
Mvvv 156,224,56
CV 140,140,141
CV 141,142,143
Mvav 156,240,76
EX 144
Mvav 156,244,216
EX 145
Mvvv 228,248,160
s_nop 0
Tv 220,8,49664
Tv 222,8,53760
Mvvv 232,248,164
LN 2,96,62,2048
Mvvv 236,248,168
EX 146
Mvvv 240,248,172
EX 147
Mvvv 244,248,176
WL 2
CV 144,144,145
CV 145,146,147
Maav 160,248,160
s_nop 0
Tv 224,252,49664
Tv 226,252,53760
Maav 164,248,164
LN 2,96,63,3072
Maav 168,248,168
s_and_b32 s86,%12,458752
s_cmp_eq_u32 s86,131072
s_cselect_b32 s86,s91,0x40000000
Maav 172,248,172
s_add_u32 s86,s86,73728
EX 148
Maav 176,248,176
EX 149
Maav 180,252,160
Ra 240,6,2048
AR 254,92
Maav 184,252,164
Ra 244,7,2048
AR 253,93
Maav 188,252,168
Ra 248,6,3072
v_mul_f32 v254,v10,v254
Maav 192,252,172
buffer_load_dword v255,v17,s[92:95],s86 offen
Maav 196,252,176
WL 3
Ra 252,7,3072
v_mul_f32 v253,v10,v253
Mavv 200,220,160
EX 150
Mavv 204,220,164
EX 151
Mavv 208,220,168
CV 148,148,149
CV 149,150,151
Mavv 212,220,172
EX 152
Mavv 216,220,176
EX 153
Mavv 220,224,160
EX 154
Mavv 224,224,164
EX 155
Mavv 228,224,168
CV 152,152,153
CV 153,154,155
Mavv 232,224,172
EX 156
Mavv 236,224,176
WL 0
EX 157
Nvav 100,240,20,80
EX 158
Mvav 100,244,40
EX 159
Mvav 100,248,60
CV 156,156,157
CV 157,158,159
Mvav 100,252,200
CV 254,254,253
AW 92,254
Nvav 104,240,24,84
AR 12,94
AR 253,95
Mvav 104,244,44
s_nop 0
Tv 220,8,50176
Tv 222,8,54272
Mvav 104,248,64
v_mul_f32 v12,v10,v12
v_mul_f32 v253,v10,v253
Mvav 104,252,204
EX 100
Nvav 108,240,28,88
EX 101
Mvav 108,244,48
EX 102
Mvav 108,248,68
s_nop 0
Tv 224,252,50176
Tv 226,252,54272
Mvav 108,252,208
EX 103
Nvav 112,240,32,92
CV 142,100,101
CV 143,102,103
Mvav 112,244,52
EX 104
Mvav 112,248,72
EX 105
Mvav 112,252,212
EX 106
Nvav 116,240,36,96
s_nop 0
Ta 240,8,50688
Ta 242,8,54784
Mvav 116,244,56
EX 107
Mvav 116,248,76
CV 146,104,105
CV 147,106,107
Mvav 116,252,216
WL 2
Ta 244,252,50688
Ta 246,252,54784
Mavv 0,220,160
EX 108
Mavv 4,220,164
EX 109
Mavv 8,220,168
EX 110
Mavv 12,220,172
EX 111
Mavv 16,220,176
CV 150,108,109
CV 151,110,111
Mavv 20,224,160
EX 112
Mavv 24,224,164
EX 113
Mavv 28,224,168
EX 114
Mavv 32,224,172
EX 115
Mavv 36,224,176
s_waitcnt vmcnt(13) lgkmcnt(0)
s_barrier
Maav 40,240,160
s_add_u32 m0,s76,0
Ra 248,6,16384
Ra 252,7,16384
Maav 44,240,164
s_cmp_eq_u32 s88,11
s_cselect_b32 s86,0x20000,0
Rv 220,6,17408
Maav 48,240,168
s_nop 0
Rv 224,7,17408
v_subrev_u32 v1,s86,v1
Maav 52,240,172
v_add_u32 v1,8192,v1
CV 154,112,113
Maav 56,240,176
LN 1,36,58,0
Maav 60,244,160
CV 155,114,115
CV 12,12,253
Maav 64,244,164
EX 116
Maav 68,244,168
EX 117
Maav 72,244,172
LN 1,36,59,1024
Maav 76,244,176
WL 1
EX 118
Nvav 160,248,20,80
EX 119
Mvav 160,252,40
CV 158,116,117
CV 159,118,119
Mvvv 160,220,60
WL 0
LN 1,36,60,2048
Mvvv 160,224,200
AW 93,12
AR 254,112
Nvav 164,248,24,84
AR 253,113
v_mul_f32 v254,v10,v254
Mvav 164,252,44
v_mul_f32 v253,v10,v253
CV 254,254,253
Mvvv 164,220,64
LN 1,36,61,3072
Mvvv 164,224,204
s_add_u32 m0,s76,98304
s_cmp_eq_u32 s88,11
s_cselect_b32 s86,0x20000,0
Nvav 168,248,28,88
v_subrev_u32 v2,s86,v2
v_add_u32 v2,8192,v2
Mvav 168,252,48
EX 160
Mvvv 168,220,68
s_nop 0
Ta 240,8,32768
Ta 242,8,36864
Mvvv 168,224,208
LN 2,96,62,0
Nvav 172,248,32,92
EX 161
Mvav 172,252,52
EX 162
Mvvv 172,220,72
EX 163
Mvvv 172,224,212
s_nop 0
Ta 244,252,32768
Ta 246,252,36864
Nvav 176,248,36,96
LN 2,96,63,1024
Mvav 176,252,56
CV 160,160,161
CV 161,162,163
Mvvv 176,220,76
EX 164
Mvvv 176,224,216
EX 165
Mvvv 228,248,140
s_nop 0
Ta 248,8,33280
Ta 250,8,37376
Mvvv 232,248,144
LN 2,96,62,2048
Mvvv 236,248,148
EX 166
Mvvv 240,248,152
EX 167
Mvvv 244,248,156
WL 2
CV 164,164,165
CV 165,166,167
Maav 160,240,140
s_nop 0
Ta 252,252,33280
Ta 254,252,37376
Maav 164,240,144
LN 2,96,63,3072
Maav 168,240,148
EX 168
Maav 172,240,152
EX 169
Maav 176,240,156
EX 170
Maav 180,244,140
Rv 220,6,18432
AW 94,254
Maav 184,244,144
Rv 224,7,18432
AR 12,114
Maav 188,244,148
Ra 240,6,19456
AR 253,115
Maav 192,244,152
EX 171
Maav 196,244,156
WL 3
Ra 244,7,19456
CV 168,168,169
Maav 200,248,140
CV 169,170,171
v_mul_f32 v12,v10,v12
Maav 204,248,144
EX 172
Maav 208,248,148
EX 173
Maav 212,248,152
EX 174
Maav 216,248,156
EX 175
Maav 220,252,140
CV 172,172,173
CV 173,174,175
Maav 224,252,144
EX 176
Maav 228,252,148
EX 177
Maav 232,252,152
EX 178
Maav 236,252,156
WL 0
EX 179
Nvvv 100,220,20,80
CV 176,176,177
CV 177,178,179
Mvvv 100,224,40
v_mul_f32 v253,v10,v253
CV 12,12,253
Mvav 100,240,60
AW 95,12
AR 254,96
Mvav 100,244,200
AR 253,97
v_mul_f32 v254,v11,v254
Nvvv 104,220,24,84
v_mul_f32 v253,v11,v253
CV 254,254,253
Mvvv 104,224,44
s_nop 0
Ta 248,8,33792
Ta 250,8,37888
Mvav 104,240,64
AW 96,254
AR 12,98
Mvav 104,244,204
EX 100
)ASM"
R"ASM(Nvvv 108,220,28,88
EX 101
Mvvv 108,224,48
EX 102
Mvav 108,240,68
s_nop 0
Ta 252,252,33792
Ta 254,252,37888
Mvav 108,244,208
EX 103
Nvvv 112,220,32,92
CV 162,100,101
CV 163,102,103
Mvvv 112,224,52
EX 104
Mvav 112,240,72
EX 105
Mvav 112,244,212
EX 106
Nvvv 116,220,36,96
s_nop 0
Tv 220,8,34304
Tv 222,8,38400
Mvvv 116,224,56
EX 107
Mvav 116,240,76
CV 166,104,105
CV 167,106,107
Mvav 116,244,216
WL 2
Tv 224,252,34304
Tv 226,252,38400
Maav 0,248,140
EX 108
Maav 4,248,144
EX 109
Maav 8,248,148
EX 110
Maav 12,248,152
EX 111
Maav 16,248,156
CV 170,108,109
CV 171,110,111
Maav 20,252,140
EX 112
Maav 24,252,144
EX 113
Maav 28,252,148
EX 114
Maav 32,252,152
EX 115
Maav 36,252,156
s_waitcnt vmcnt(13) lgkmcnt(0)
s_barrier
Mavv 40,220,140
s_add_u32 m0,s76,16384
Ra 240,6,32768
Ra 244,7,32768
Mavv 44,220,144
s_cmp_eq_u32 s88,12
s_cselect_b32 s86,0x20000,0
Ra 248,6,33792
Mavv 48,220,148
Ra 252,7,33792
v_subrev_u32 v1,s86,v1
Mavv 52,220,152
v_add_u32 v1,8192,v1
CV 174,112,113
Mavv 56,220,156
LN 1,36,58,0
Mavv 60,224,140
CV 175,114,115
AR 253,99
Mavv 64,224,144
EX 116
Mavv 68,224,148
EX 117
Mavv 72,224,152
LN 1,36,59,1024
Mavv 76,224,156
WL 1
EX 118
Nvav 140,240,20,80
EX 119
Mvav 140,244,40
CV 178,116,117
CV 179,118,119
Mvav 140,248,60
WL 0
LN 1,36,60,2048
Mvav 140,252,200
v_mul_f32 v12,v11,v12
v_mul_f32 v253,v11,v253
Nvav 144,240,24,84
CV 12,12,253
AW 97,12
Mvav 144,244,44
AR 254,116
AR 253,117
Mvav 144,248,64
LN 1,36,61,3072
Mvav 144,252,204
s_add_u32 m0,s76,81920
s_cmp_eq_u32 s88,12
s_cselect_b32 s86,0x20000,0
Nvav 148,240,28,88
v_subrev_u32 v2,s86,v2
v_add_u32 v2,8192,v2
Mvav 148,244,48
EX 140
Mvav 148,248,68
s_nop 0
Tv 220,8,16384
Tv 222,8,20480
Mvav 148,252,208
LN 2,96,62,0
Nvav 152,240,32,92
EX 141
Mvav 152,244,52
EX 142
Mvav 152,248,72
EX 143
Mvav 152,252,212
s_nop 0
Tv 224,252,16384
Tv 226,252,20480
Nvav 156,240,36,96
LN 2,96,63,1024
Mvav 156,244,56
CV 140,140,141
CV 141,142,143
Mvav 156,248,76
EX 144
Mvav 156,252,216
EX 145
Mvvv 228,248,160
s_nop 0
Ta 240,8,16896
Ta 242,8,20992
Mvvv 232,248,164
LN 2,96,62,2048
Mvvv 236,248,168
EX 146
Mvvv 240,248,172
EX 147
Mvvv 244,248,176
WL 2
CV 144,144,145
CV 145,146,147
Mavv 160,220,160
s_nop 0
Ta 244,252,16896
Ta 246,252,20992
Mavv 164,220,164
LN 2,96,63,3072
Mavv 168,220,168
EX 148
Mavv 172,220,172
EX 149
Mavv 176,220,176
EX 150
Mavv 180,224,160
Ra 248,6,34816
v_mul_f32 v254,v11,v254
Mavv 184,224,164
Ra 252,7,34816
v_mul_f32 v253,v11,v253
Mavv 188,224,168
Rv 220,6,35840
CV 254,254,253
Mavv 192,224,172
EX 151
Mavv 196,224,176
WL 3
Rv 224,7,35840
CV 148,148,149
Maav 200,240,160
CV 149,150,151
AW 98,254
Maav 204,240,164
EX 152
Maav 208,240,168
EX 153
Maav 212,240,172
EX 154
Maav 216,240,176
EX 155
Maav 220,244,160
CV 152,152,153
CV 153,154,155
Maav 224,244,164
EX 156
Maav 228,244,168
EX 157
Maav 232,244,172
EX 158
Maav 236,244,176
WL 0
EX 159
Nvav 100,248,20,80
CV 156,156,157
CV 157,158,159
Mvav 100,252,40
AR 12,118
AR 253,119
Mvvv 100,220,60
v_mul_f32 v12,v11,v12
v_mul_f32 v253,v11,v253
Mvvv 100,224,200
CV 12,12,253
AW 99,12
Nvav 104,248,24,84
AR 254,120
AR 253,121
Mvav 104,252,44
s_nop 0
Ta 240,8,17408
Ta 242,8,21504
Mvvv 104,220,64
v_mul_f32 v254,v0,v254
v_mul_f32 v253,v0,v253
Mvvv 104,224,204
EX 100
Nvav 108,248,28,88
EX 101
Mvav 108,252,48
EX 102
Mvvv 108,220,68
s_nop 0
Ta 244,252,17408
Ta 246,252,21504
Mvvv 108,224,208
EX 103
Nvav 112,248,32,92
CV 142,100,101
CV 143,102,103
Mvav 112,252,52
EX 104
Mvvv 112,220,72
EX 105
Mvvv 112,224,212
EX 106
Nvav 116,248,36,96
s_nop 0
Ta 248,8,17920
Ta 250,8,22016
Mvav 116,252,56
EX 107
Mvvv 116,220,76
CV 146,104,105
CV 147,106,107
Mvvv 116,224,216
WL 2
Ta 252,252,17920
Ta 254,252,22016
Maav 0,240,160
EX 108
Maav 4,240,164
EX 109
Maav 8,240,168
EX 110
Maav 12,240,172
EX 111
Maav 16,240,176
CV 150,108,109
CV 151,110,111
Maav 20,244,160
EX 112
Maav 24,244,164
EX 113
Maav 28,244,168
EX 114
Maav 32,244,172
EX 115
Maav 36,244,176
s_waitcnt vmcnt(12) lgkmcnt(0)
s_barrier
Maav 40,248,160
s_add_u32 m0,s76,32768
Rv 220,6,0
Rv 224,7,0
Maav 44,248,164
s_cmp_eq_u32 s88,13
s_cselect_b32 s86,0x20000,0
Ra 240,6,1024
Maav 48,248,168
Ra 244,7,1024
v_subrev_u32 v1,s86,v1
Maav 52,248,172
v_add_u32 v1,8192,v1
CV 154,112,113
Maav 56,248,176
LN 1,36,58,0
Maav 60,252,160
CV 155,114,115
CV 254,254,253
Maav 64,252,164
EX 116
Maav 68,252,168
EX 117
Maav 72,252,172
LN 1,36,59,1024
Maav 76,252,176
WL 1
EX 118
Nvvv 160,220,20,80
EX 119
Mvvv 160,224,40
CV 158,116,117
CV 159,118,119
Mvav 160,240,60
WL 0
LN 1,36,60,2048
Mvav 160,244,200
AW 120,254
AR 12,122
Nvvv 164,220,24,84
AR 253,123
v_mul_f32 v12,v0,v12
Mvvv 164,224,44
v_mul_f32 v253,v0,v253
CV 12,12,253
Mvav 164,240,64
LN 1,36,61,3072
Mvav 164,244,204
s_add_u32 m0,s76,65536
s_cmp_eq_u32 s88,13
s_cselect_b32 s86,0x20000,0
Nvvv 168,220,28,88
v_subrev_u32 v2,s86,v2
v_add_u32 v2,8192,v2
Mvvv 168,224,48
EX 160
Mvav 168,240,68
s_nop 0
Ta 248,8,0
Ta 250,8,4096
Mvav 168,244,208
LN 2,96,62,0
Nvvv 172,220,32,92
EX 161
Mvvv 172,224,52
EX 162
Mvav 172,240,72
EX 163
Mvav 172,244,212
s_nop 0
Ta 252,252,0
Ta 254,252,4096
Nvvv 176,220,36,96
LN 2,96,63,1024
Mvvv 176,224,56
CV 160,160,161
CV 161,162,163
Mvav 176,240,76
EX 164
Mvav 176,244,216
EX 165
Mvvv 228,248,140
s_nop 0
Tv 220,8,512
Tv 222,8,4608
Mvvv 232,248,144
LN 2,96,62,2048
Mvvv 236,248,148
EX 166
Mvvv 240,248,152
EX 167
Mvvv 244,248,156
WL 2
CV 164,164,165
CV 165,166,167
Maav 160,248,140
s_nop 0
Tv 224,252,512
Tv 226,252,4608
Maav 164,248,144
LN 2,96,63,3072
Maav 168,248,148
s_and_b32 s86,%12,458752
s_cmp_eq_u32 s86,196608
s_cselect_b32 s86,s91,0x40000000
Maav 172,248,152
s_add_u32 s86,s86,77824
EX 168
Maav 176,248,156
EX 169
Maav 180,252,140
Ra 240,6,2048
AW 121,12
Maav 184,252,144
Ra 244,7,2048
AR 254,140
Maav 188,252,148
Ra 248,6,3072
AR 253,141
Maav 192,252,152
buffer_load_dword v255,v17,s[92:95],s86 offen
Maav 196,252,156
WL 3
Ra 252,7,3072
v_mul_f32 v254,v0,v254
Mavv 200,220,140
EX 170
Mavv 204,220,144
EX 171
Mavv 208,220,148
CV 168,168,169
CV 169,170,171
Mavv 212,220,152
EX 172
Mavv 216,220,156
EX 173
Mavv 220,224,140
EX 174
Mavv 224,224,144
EX 175
Mavv 228,224,148
CV 172,172,173
CV 173,174,175
Mavv 232,224,152
EX 176
Mavv 236,224,156
WL 0
EX 177
Nvav 100,240,20,80
EX 178
Mvav 100,244,40
EX 179
Mvav 100,248,60
CV 176,176,177
CV 177,178,179
Mvav 100,252,200
v_mul_f32 v253,v0,v253
CV 254,254,253
Nvav 104,240,24,84
AW 122,254
AR 12,142
Mvav 104,244,44
s_nop 0
Tv 220,8,1024
Tv 222,8,5120
Mvav 104,248,64
AR 253,143
v_mul_f32 v12,v0,v12
Mvav 104,252,204
EX 100
Nvav 108,240,28,88
EX 101
Mvav 108,244,48
EX 102
Mvav 108,248,68
s_nop 0
Tv 224,252,1024
Tv 226,252,5120
Mvav 108,252,208
EX 103
Nvav 112,240,32,92
CV 162,100,101
CV 163,102,103
Mvav 112,244,52
EX 104
Mvav 112,248,72
EX 105
Mvav 112,252,212
EX 106
Nvav 116,240,36,96
s_nop 0
Ta 240,8,1536
Ta 242,8,5632
Mvav 116,244,56
EX 107
Mvav 116,248,76
CV 166,104,105
CV 167,106,107
Mvav 116,252,216
WL 2
Ta 244,252,1536
Ta 246,252,5632
Mavv 0,220,140
EX 108
Mavv 4,220,144
EX 109
Mavv 8,220,148
EX 110
Mavv 12,220,152
EX 111
Mavv 16,220,156
CV 170,108,109
CV 171,110,111
Mavv 20,224,140
EX 112
Mavv 24,224,144
EX 113
Mavv 28,224,148
EX 114
Mavv 32,224,152
EX 115
Mavv 36,224,156
s_waitcnt vmcnt(13) lgkmcnt(0)
s_barrier
Maav 40,240,140
s_add_u32 m0,s76,0
Ra 248,6,16384
Ra 252,7,16384
Maav 44,240,144
s_cmp_eq_u32 s88,14
s_cselect_b32 s86,0x20000,0
Rv 220,6,17408
Maav 48,240,148
Rv 224,7,17408
v_subrev_u32 v1,s86,v1
Maav 52,240,152
v_add_u32 v1,8192,v1
CV 174,112,113
Maav 56,240,156
LN 1,36,58,0
Maav 60,244,140
CV 175,114,115
v_mul_f32 v253,v0,v253
Maav 64,244,144
EX 116
Maav 68,244,148
EX 117
Maav 72,244,152
LN 1,36,59,1024
Maav 76,244,156
WL 1
EX 118
Nvav 140,248,20,80
EX 119
Mvav 140,252,40
CV 178,116,117
CV 179,118,119
Mvvv 140,220,60
WL 0
LN 1,36,60,2048
Mvvv 140,224,200
CV 12,12,253
AW 123,12
Nvav 144,248,24,84
AR 254,124
AR 253,125
Mvav 144,252,44
v_mul_f32 v254,v3,v254
v_mul_f32 v253,v3,v253
Mvvv 144,220,64
LN 1,36,61,3072
Mvvv 144,224,204
CV 254,254,253
AW 124,254
Nvav 148,248,28,88
AR 12,126
AR 253,127
Mvav 148,252,48
v_mul_f32 v12,v3,v12
v_mul_f32 v253,v3,v253
Mvvv 148,220,68
s_nop 0
Ta 240,8,49152
Ta 242,8,53248
Mvvv 148,224,208
CV 12,12,253
AW 125,12
Nvav 152,248,32,92
AR 254,144
AR 253,145
Mvav 152,252,52
v_mul_f32 v254,v3,v254
v_mul_f32 v253,v3,v253
Mvvv 152,220,72
CV 254,254,253
AW 126,254
Mvvv 152,224,212
s_nop 0
Ta 244,252,49152
Ta 246,252,53248
Nvav 156,248,36,96
AR 12,146
AR 253,147
Mvav 156,252,56
v_mul_f32 v12,v3,v12
v_mul_f32 v253,v3,v253
Mvvv 156,220,76
CV 12,12,253
AW 127,12
Mvvv 156,224,216
AR 254,128
AR 253,129
Mvvv 228,248,160
s_nop 0
Ta 248,8,49664
Ta 250,8,53760
Mvvv 232,248,164
v_mul_f32 v254,v9,v254
v_mul_f32 v253,v9,v253
Mvvv 236,248,168
CV 254,254,253
AW 128,254
Mvvv 240,248,172
AR 12,130
AR 253,131
Mvvv 244,248,176
WL 2
v_mul_f32 v12,v9,v12
v_mul_f32 v253,v9,v253
Maav 160,240,160
s_nop 0
Ta 252,252,49664
Ta 254,252,53760
Maav 164,240,164
CV 12,12,253
AW 129,12
Maav 168,240,168
AR 254,148
AR 253,149
Maav 172,240,172
v_mul_f32 v254,v9,v254
v_mul_f32 v253,v9,v253
Maav 176,240,176
CV 254,254,253
AW 130,254
Maav 180,244,160
Rv 220,6,18432
AR 12,150
Maav 184,244,164
Rv 224,7,18432
AR 253,151
Maav 188,244,168
Ra 240,6,19456
v_mul_f32 v12,v9,v12
Maav 192,244,172
v_mul_f32 v253,v9,v253
CV 12,12,253
Maav 196,244,176
WL 3
Ra 244,7,19456
AW 131,12
Maav 200,248,160
AR 254,132
AR 253,133
Maav 204,248,164
v_mul_f32 v254,v10,v254
v_mul_f32 v253,v10,v253
Maav 208,248,168
CV 254,254,253
AW 132,254
Maav 212,248,172
AR 12,134
AR 253,135
Maav 216,248,176
v_mul_f32 v12,v10,v12
v_mul_f32 v253,v10,v253
Maav 220,252,160
CV 12,12,253
AW 133,12
Maav 224,252,164
AR 254,152
AR 253,153
Maav 228,252,168
v_mul_f32 v254,v10,v254
v_mul_f32 v253,v10,v253
Maav 232,252,172
CV 254,254,253
AW 134,254
Maav 236,252,176
WL 0
AR 12,154
AR 253,155
Nvvv 100,220,20,80
v_mul_f32 v12,v10,v12
v_mul_f32 v253,v10,v253
Mvvv 100,224,40
CV 12,12,253
AW 135,12
Mvav 100,240,60
AR 254,136
AR 253,137
Mvav 100,244,200
v_mul_f32 v254,v11,v254
v_mul_f32 v253,v11,v253
Nvvv 104,220,24,84
CV 254,254,253
AW 136,254
Mvvv 104,224,44
s_nop 0
Ta 248,8,50176
Ta 250,8,54272
Mvav 104,240,64
AR 12,138
AR 253,139
Mvav 104,244,204
v_mul_f32 v12,v11,v12
v_mul_f32 v253,v11,v253
Nvvv 108,220,28,88
CV 12,12,253
AW 137,12
Mvvv 108,224,48
AR 254,156
AR 253,157
Mvav 108,240,68
s_nop 0
Ta 252,252,50176
Ta 254,252,54272
Mvav 108,244,208
v_mul_f32 v254,v11,v254
v_mul_f32 v253,v11,v253
Nvvv 112,220,32,92
CV 254,254,253
AW 138,254
Mvvv 112,224,52
AR 12,158
AR 253,159
Mvav 112,240,72
v_mul_f32 v12,v11,v12
v_mul_f32 v253,v11,v253
Mvav 112,244,212
CV 12,12,253
AW 139,12
Nvvv 116,220,36,96
s_nop 0
Tv 220,8,50688
Tv 222,8,54784
Mvvv 116,224,56
v_add_u32 v253,0x10000,v13
v_add_u32 v254,0x10000,v14
Mvav 116,240,76
EX 140
Mvav 116,244,216
WL 2
Tv 224,252,50688
Tv 226,252,54784
Maav 0,248,160
EX 141
Maav 4,248,164
EX 142
Maav 8,248,168
EX 143
Maav 12,248,172
CV 140,140,141
CV 141,142,143
Maav 16,248,176
EX 144
Maav 20,252,160
EX 145
Maav 24,252,164
EX 146
Maav 28,252,168
EX 147
Maav 32,252,172
CV 144,144,145
CV 145,146,147
Maav 36,252,176
s_waitcnt vmcnt(9) lgkmcnt(0)
s_barrier
Mavv 40,220,160
s_add_u32 m0,s76,16384
Ra 240,6,32768
Ra 244,7,32768
Mavv 44,220,164
s_cmp_eq_u32 s88,15
s_cselect_b32 s86,0x20000,0
Ra 248,6,33792
Mavv 48,220,168
Ra 252,7,33792
v_subrev_u32 v1,s86,v1
Mavv 52,220,172
Ra 100,16,53248
v_add_u32 v1,8192,v1
Mavv 56,220,176
LN 1,36,58,0
Mavv 60,224,160
EX 148
Mavv 64,224,164
EX 149
Mavv 68,224,168
EX 150
Mavv 72,224,172
LN 1,36,59,1024
Mavv 76,224,176
WL 2
EX 151
Nvav 160,240,20,80
CV 148,148,149
CV 149,150,151
Mvav 160,244,40
EX 152
Mvav 160,248,60
WL 0
LN 1,36,60,2048
Mvav 160,252,200
EX 153
Nvav 164,240,24,84
EX 154
Mvav 164,244,44
EX 155
Mvav 164,248,64
LN 1,36,61,3072
Mvav 164,252,204
s_add_u32 m0,s76,98304
s_cmp_eq_u32 s88,14
s_cselect_b32 s86,0x20000,0
Nvav 168,240,28,88
v_subrev_u32 v2,s86,v2
v_add_u32 v2,8192,v2
Mvav 168,244,48
CV 152,152,153
CV 153,154,155
Mvav 168,248,68
s_nop 0
Tv 220,8,32768
Tv 222,8,36864
Mvav 168,252,208
LN 2,96,62,0
Nvav 172,240,32,92
EX 156
Mvav 172,244,52
EX 157
Mvav 172,248,72
EX 158
Mvav 172,252,212
s_nop 0
Tv 224,252,32768
Tv 226,252,36864
Nvav 176,240,36,96
LN 2,96,63,1024
Mvav 176,244,56
EX 159
CV 156,156,157
CV 157,158,159
EX 100
EX 101
EX 102
EX 103
CV 142,100,101
CV 143,102,103
Mvav 176,248,76
EX 104
EX 105
EX 106
EX 107
CV 146,104,105
CV 147,106,107
Mvav 176,252,216
EX 108
EX 109
EX 110
EX 111
CV 150,108,109
CV 151,110,111
Mvvv 228,248,140
EX 112
EX 113
EX 114
EX 115
CV 154,112,113
CV 155,114,115
Mvvv 232,248,144
EX 116
EX 117
EX 118
EX 119
CV 158,116,117
CV 159,118,119
Mvvv 236,248,148
s_nop 0
Ta 240,8,33280
Ta 242,8,37376
Mvvv 240,248,152
LN 2,96,62,2048
Mvvv 244,248,156
WL 2
EX 160
Mavv 160,220,140
s_nop 0
Ta 244,252,33280
Ta 246,252,37376
Mavv 164,220,144
EX 161
Mavv 168,220,148
LN 2,96,63,3072
Mavv 172,220,152
EX 162
Mavv 176,220,156
EX 163
Mavv 180,224,140
Ra 248,6,34816
CV 160,160,161
Mavv 184,224,144
Ra 252,7,34816
CV 161,162,163
Mavv 188,224,148
s_nop 0
Rv 220,6,35840
Ra 104,16,54272
Mavv 192,224,152
EX 164
Mavv 196,224,156
WL 4
Rv 224,7,35840
Ra 108,16,55296
Maav 200,240,140
EX 165
Maav 204,240,144
EX 166
Maav 208,240,148
EX 167
Maav 212,240,152
CV 164,164,165
CV 165,166,167
Maav 216,240,156
EX 168
Maav 220,244,140
EX 169
Maav 224,244,144
EX 170
Maav 228,244,148
EX 171
Maav 232,244,152
CV 168,168,169
CV 169,170,171
Maav 236,244,156
WL 0
EX 172
Nvav 100,248,20,80
EX 173
Mvav 100,252,40
EX 174
Mvvv 100,220,60
EX 175
Mvvv 100,224,200
CV 172,172,173
CV 173,174,175
Nvav 104,248,24,84
EX 176
Mvav 104,252,44
s_nop 0
Ta 240,8,33792
Ta 242,8,37888
Mvvv 104,220,64
EX 177
Mvvv 104,224,204
EX 178
Nvav 108,248,28,88
EX 179
Mvav 108,252,48
CV 176,176,177
CV 177,178,179
Mvvv 108,220,68
s_nop 0
Ta 244,252,33792
Ta 246,252,37888
Mvvv 108,224,208
EX 100
Nvav 112,248,32,92
EX 101
Mvav 112,252,52
EX 102
Mvvv 112,220,72
EX 103
Mvvv 112,224,212
CV 162,100,101
CV 163,102,103
Nvav 116,248,36,96
s_nop 0
Ta 248,8,34304
Ta 250,8,38400
Mvav 116,252,56
EX 104
Mvvv 116,220,76
EX 105
Mvvv 116,224,216
WL 2
Ta 252,252,34304
Ta 254,252,38400
Maav 0,240,140
EX 106
Maav 4,240,144
EX 107
Maav 8,240,148
CV 166,104,105
CV 167,106,107
Maav 12,240,152
EX 108
Maav 16,240,156
EX 109
Maav 20,244,140
EX 110
Maav 24,244,144
EX 111
Maav 28,244,148
CV 170,108,109
CV 171,110,111
Maav 32,244,152
EX 112
Maav 36,244,156
s_waitcnt vmcnt(8) lgkmcnt(0)
s_barrier
Maav 40,248,140
s_add_u32 m0,s76,81920
Rv 220,6,0
Rv 224,7,0
Maav 44,248,144
s_cmp_eq_u32 s88,15
s_cselect_b32 s86,0x20000,0
Ra 240,6,1024
Maav 48,248,148
Ra 244,7,1024
v_subrev_u32 v2,s86,v2
Maav 52,248,152
Ra 112,16,56320
v_add_u32 v2,8192,v2
Maav 56,248,156
LN 2,96,62,0
Maav 60,252,140
EX 113
Maav 64,252,144
EX 114
Maav 68,252,148
EX 115
Maav 72,252,152
LN 2,96,63,1024
Maav 76,252,156
WL 2
CV 174,112,113
CV 175,114,115
Nvvv 140,220,20,80
EX 116
Mvvv 140,224,40
EX 117
Mvav 140,240,60
WL 0
LN 2,96,62,2048
Mvav 140,244,200
EX 118
Nvvv 144,220,24,84
EX 119
Mvvv 144,224,44
CV 178,116,117
CV 179,118,119
Mvav 144,240,64
LN 2,96,63,3072
Mvav 144,244,204
s_add_u32 m0,s78,32768
Ra 116,16,57344
Ra 140,16,58368
Nvvv 148,220,28,88
Ra 144,16,59392
Mvvv 148,224,48
Ra 148,16,60416
Mvav 148,240,68
Ta 248,8,16384
Mvav 148,244,208
s_nop 0
Ta 250,8,20480
Ra 152,16,61440
Nvvv 152,220,32,92
LD 4,40,68,0
Mvvv 152,224,52
Ra 156,16,62464
Mvav 152,240,72
EX 140
Mvav 152,244,212
s_nop 0
Ta 252,252,16384
Ta 254,252,20480
Nvvv 156,220,36,96
LD 4,40,69,1024
Mvvv 156,224,56
EX 141
Mvav 156,240,76
EX 142
Mvav 156,244,216
EX 143
Mvvv 228,248,160
s_nop 0
Tv 220,8,16896
Tv 222,8,20992
Mvvv 232,248,164
LD 4,40,70,2048
Mvvv 236,248,168
CV 140,140,141
CV 141,142,143
Mvvv 240,248,172
EX 144
Mvvv 244,248,176
WL 2
EX 145
Maav 160,248,160
s_nop 0
Tv 224,252,16896
Tv 226,252,20992
Maav 164,248,164
LD 4,40,71,3072
Maav 168,248,168
s_add_u32 m0,s78,36864
EX 146
Maav 172,248,172
EX 147
Maav 176,248,176
CV 144,144,145
CV 145,146,147
Maav 180,252,160
Ra 240,6,2048
Maav 184,252,164
Ra 244,7,2048
Maav 188,252,168
Ra 248,6,3072
Maav 192,252,172
LD 4,40,72,0
Maav 196,252,176
WL 3
Ra 252,7,3072
Mavv 200,220,160
EX 148
Mavv 204,220,164
EX 149
Mavv 208,220,168
LD 4,40,73,1024
Mavv 212,220,172
EX 150
Mavv 216,220,176
EX 151
Mavv 220,224,160
CV 148,148,149
CV 149,150,151
Mavv 224,224,164
LD 4,40,74,2048
Mavv 228,224,168
EX 152
Mavv 232,224,172
EX 153
Mavv 236,224,176
WL 0
EX 154
Nvav 100,240,20,80
LD 4,40,75,3072
Mvav 100,244,40
EX 155
Mvav 100,248,60
CV 152,152,153
CV 153,154,155
Mvav 100,252,200
s_and_b32 s86,%12,458752
s_cmp_eq_u32 s86,262144
s_cselect_b32 s86,s91,0x40000000
Nvav 104,240,24,84
s_add_u32 s86,s86,81920
EX 156
Mvav 104,244,44
s_nop 0
Tv 220,8,17408
Tv 222,8,21504
Mvav 104,248,64
buffer_load_dword v255,v17,s[92:95],s86 offen
Mvav 104,252,204
EX 157
Nvav 108,240,28,88
EX 158
Mvav 108,244,48
EX 159
Mvav 108,248,68
s_nop 0
Tv 224,252,17408
Tv 226,252,21504
Mvav 108,252,208
CV 156,156,157
CV 157,158,159
Nvav 112,240,32,92
EX 100
Mvav 112,244,52
EX 101
Mvav 112,248,72
EX 102
Mvav 112,252,212
EX 103
Nvav 116,240,36,96
s_nop 0
Ta 240,8,17920
Ta 242,8,22016
Mvav 116,244,56
CV 142,100,101
CV 143,102,103
Mvav 116,248,76
EX 104
Mvav 116,252,216
WL 2
Ta 244,252,17920
Ta 246,252,22016
Mavv 0,220,160
EX 105
Mavv 4,220,164
EX 106
Mavv 8,220,168
EX 107
Mavv 12,220,172
CV 146,104,105
CV 147,106,107
Mavv 16,220,176
EX 108
Mavv 20,224,160
EX 109
Mavv 24,224,164
EX 110
Mavv 28,224,168
EX 111
Mavv 32,224,172
CV 150,108,109
CV 151,110,111
Mavv 36,224,176
s_waitcnt vmcnt(13) lgkmcnt(0)
s_barrier
Maav 40,240,160
s_nop 0
Ra 248,6,16384
Ra 252,7,16384
Maav 44,240,164
Rv 220,6,17408
Maav 48,240,168
Rv 224,7,17408
Maav 52,240,172
EX 112
Maav 56,240,176
EX 113
Maav 60,244,160
EX 114
Maav 64,244,164
EX 115
Maav 68,244,168
CV 154,112,113
CV 155,114,115
Maav 72,244,172
EX 116
Maav 76,244,176
WL 1
EX 117
Nvav 160,248,20,80
EX 118
Mvav 160,252,40
EX 119
Mvvv 160,220,60
WL 0
CV 158,116,117
CV 159,118,119
Mvvv 160,224,200
ds_write_b128 v15,a[80:83] offset:53248
Nvav 164,248,24,84
Mvav 164,252,44
Mvvv 164,220,64
Mvvv 164,224,204
s_nop 2
EX 160
Nvav 168,248,28,88
EX 161
Mvav 168,252,48
EX 162
Mvvv 168,220,68
s_nop 0
Ta 240,8,49152
Ta 242,8,53248
Mvvv 168,224,208
EX 163
Nvav 172,248,32,92
CV 160,160,161
CV 161,162,163
Mvav 172,252,52
EX 164
Mvvv 172,220,72
EX 165
Mvvv 172,224,212
s_nop 0
Ta 244,252,49152
Ta 246,252,53248
Nvav 176,248,36,96
EX 166
Mvav 176,252,56
EX 167
Mvvv 176,220,76
CV 164,164,165
CV 165,166,167
Mvvv 176,224,216
EX 168
Mvvv 228,248,140
s_nop 0
Ta 248,8,49664
Ta 250,8,53760
Mvvv 232,248,144
EX 169
Mvvv 236,248,148
EX 170
Mvvv 240,248,152
EX 171
Mvvv 244,248,156
WL 2
CV 168,168,169
CV 169,170,171
Maav 160,240,140
s_nop 0
Ta 252,252,49664
Ta 254,252,53760
Maav 164,240,144
EX 172
Maav 168,240,148
EX 173
Maav 172,240,152
EX 174
Maav 176,240,156
EX 175
Maav 180,244,140
Rv 220,6,18432
CV 172,172,173
Maav 184,244,144
Rv 224,7,18432
CV 173,174,175
Maav 188,244,148
Ra 240,6,19456
Maav 192,244,152
EX 176
Maav 196,244,156
WL 3
Ra 244,7,19456
Maav 200,248,140
EX 177
Maav 204,248,144
EX 178
Maav 208,248,148
EX 179
Maav 212,248,152
CV 176,176,177
CV 177,178,179
Maav 216,248,156
ds_write_b128 v15,a[84:87] offset:54272
Maav 220,252,140
Maav 224,252,144
Maav 228,252,148
Maav 232,252,152
Maav 236,252,156
WL 1
ds_write_b128 v15,a[88:91] offset:55296
Nvvv 100,220,20,80
Mvvv 100,224,40
Mvav 100,240,60
Mvav 100,244,200
Nvvv 104,220,24,84
Mvvv 104,224,44
Ta 248,8,50176
Mvav 104,240,64
Ta 250,8,54272
Mvav 104,244,204
s_nop 1
EX 100
Nvvv 108,220,28,88
EX 101
Mvvv 108,224,48
EX 102
Mvav 108,240,68
s_nop 0
Ta 252,252,50176
Ta 254,252,54272
Mvav 108,244,208
EX 103
Nvvv 112,220,32,92
CV 162,100,101
CV 163,102,103
Mvvv 112,224,52
EX 104
Mvav 112,240,72
EX 105
Mvav 112,244,212
EX 106
Nvvv 116,220,36,96
s_nop 0
Tv 220,8,50688
Tv 222,8,54784
Mvvv 116,224,56
EX 107
Mvav 116,240,76
CV 166,104,105
CV 167,106,107
Mvav 116,244,216
WL 2
Tv 224,252,50688
Tv 226,252,54784
Maav 0,248,140
EX 108
Maav 4,248,144
EX 109
Maav 8,248,148
EX 110
Maav 12,248,152
EX 111
Maav 16,248,156
CV 170,108,109
CV 171,110,111
Maav 20,252,140
EX 112
Maav 24,252,144
EX 113
Maav 28,252,148
EX 114
Maav 32,252,152
EX 115
Maav 36,252,156
s_waitcnt vmcnt(9) lgkmcnt(0)
s_barrier
Mavv 40,220,140
s_add_u32 m0,s78,0
Ta 240,8,32768
Ta 242,8,36864
Mavv 44,220,144
s_cmp_le_u32 s90,1
s_cselect_b64 s[40:41],s[28:29],%6
v_add_u32 v4,128,v4
Mavv 48,220,148
LD 4,40,68,0
Mavv 52,220,152
CV 174,112,113
CV 175,114,115
Mavv 56,220,156
EX 116
Mavv 60,224,140
s_nop 0
Ta 244,252,32768
Ta 246,252,36864
Mavv 64,224,144
LD 4,40,69,1024
Mavv 68,224,148
EX 117
Mavv 72,224,152
EX 118
Mavv 76,224,156
EX 119
Mvvv 228,248,160
s_nop 0
Ta 248,8,33280
Ta 250,8,37376
Mvvv 232,248,164
CV 178,116,117
CV 179,118,119
Mvvv 236,248,168
LD 4,40,70,2048
Mvvv 240,248,172
ds_write_b128 v15,a[92:95] offset:56320
Mvvv 244,248,176
WL 3
Maav 160,240,160
Maav 164,240,164
LD 4,40,71,3072
Maav 168,240,168
s_add_u32 m0,s78,4096
Maav 172,240,172
Ta 252,252,33280
v_mul_f32 v229,0x4d800000,v229
Maav 176,240,176
Ta 254,252,37376
Maav 180,244,160
s_nop 0
Tv 220,8,33792
Tv 222,8,37888
Maav 184,244,164
LD 4,40,72,0
Maav 188,244,168
ds_write_b128 v15,a[96:99] offset:57344
Maav 192,244,172
v_rcp_f32 v228,v228
Maav 196,244,176
WL 5
v_mul_f32 v233,0x4d800000,v233
Maav 200,248,160
LD 4,40,73,1024
Maav 204,248,164
v_rcp_f32 v232,v232
Maav 208,248,168
Tv 224,252,33792
v_mul_f32 v237,0x4d800000,v237
Maav 212,248,172
Tv 226,252,37888
Maav 216,248,176
WL 3
LD 4,40,74,2048
Maav 220,252,160
s_nop 0
Ta 240,8,34304
Ta 242,8,38400
Maav 224,252,164
ds_write_b128 v15,a[120:123] offset:58368
Maav 228,252,168
v_rcp_f32 v236,v236
Maav 232,252,172
LD 4,40,75,3072
Maav 236,252,176
v_mul_f32 v241,0x4d800000,v241
Mavv 0,220,160
v_rcp_f32 v240,v240
Mavv 4,220,164
Ta 244,252,34304
v_mul_f32 v245,0x4d800000,v245
Mavv 8,220,168
Ta 246,252,38400
Mavv 12,220,172
ds_write_b128 v15,a[124:127] offset:59392
Mavv 16,220,176
WL 3
v_rcp_f32 v244,v244
Mavv 20,224,160
ds_write_b128 v15,a[128:131] offset:60416
ds_write_b128 v15,a[132:135] offset:61440
ds_write_b128 v15,a[136:139] offset:62464
Mavv 24,224,164
v_sub_f32 v229,v229,v229
v_sub_f32 v233,v233,v233
Mavv 28,224,168
v_sub_f32 v237,v237,v237
v_sub_f32 v241,v241,v241
Mavv 32,224,172
v_sub_f32 v245,v245,v245
v_add_f32 v228,v228,v229
Mavv 36,224,176
s_waitcnt vmcnt(9) lgkmcnt(0)
s_barrier
Maav 40,240,160
s_and_b32 s86,%12,458752
s_cmp_eq_u32 s86,327680
s_cselect_b32 s86,s91,0x40000000
Maav 44,240,164
s_add_u32 s86,s86,86016
v_add_f32 v232,v232,v233
v_add_f32 v236,v236,v237
Maav 48,240,168
buffer_load_dword v255,v17,s[92:95],s86 offen
Maav 52,240,172
s_nop 0
Ta 248,13,32768
Ta 250,13,34816
Maav 56,240,176
Ta 252,14,32768
v_add_f32 v240,v240,v241
Maav 60,244,160
s_nop 0
Ta 254,14,34816
Tv 220,13,36864
Maav 64,244,164
Tv 222,13,38912
v_add_f32 v244,v244,v245
Maav 68,244,168
AR 20,160
AR 21,161
Maav 72,244,172
AR 22,162
AR 23,163
Maav 76,244,176
WL 4
AR 248,180
AR 249,181
Zvav 100,248,120
s_nop 0
Tv 224,14,36864
Tv 226,14,38912
Zvav 104,248,124
AR 250,182
AR 251,183
Zvav 108,248,128
v_mul_f32 v20,v228,v20
v_mul_f32 v21,v228,v21
Zvav 112,248,132
v_mul_f32 v22,v228,v22
v_mul_f32 v23,v228,v23
Zvav 116,248,136
WL 2
v_mul_f32 v248,v228,v248
v_mul_f32 v249,v228,v249
Zvav 80,252,120
s_nop 0
Ta 240,13,40960
Ta 242,13,43008
Zvav 84,252,124
v_mul_f32 v250,v228,v250
v_mul_f32 v251,v228,v251
Zvav 88,252,128
CV 20,20,21
CV 21,22,23
Zvav 92,252,132
CV 22,248,249
CV 23,250,251
Zvav 96,252,136
ds_write_b128 v15,v[20:23] offset:0
Mvvv 100,220,180
AR 24,164
AR 25,165
Mvvv 104,220,184
AR 26,166
AR 27,167
Mvvv 108,220,188
AR 229,184
AR 230,185
Mvvv 112,220,192
AR 231,186
AR 233,187
Mvvv 116,220,196
WL 1
Ta 244,14,40960
v_mul_f32 v24,v232,v24
Mvvv 80,224,180
s_nop 0
Ta 246,14,43008
Ta 248,13,45056
Mvvv 84,224,184
Ta 250,13,47104
v_mul_f32 v25,v232,v25
Mvvv 88,224,188
v_mul_f32 v26,v232,v26
v_mul_f32 v27,v232,v27
Mvvv 92,224,192
v_mul_f32 v229,v232,v229
v_mul_f32 v230,v232,v230
Mvvv 96,224,196
v_mul_f32 v231,v232,v231
v_mul_f32 v233,v232,v233
Mvaa 100,240,80
s_nop 0
Ta 252,14,45056
Ta 254,14,47104
Mvaa 104,240,84
CV 24,24,25
CV 25,26,27
Mvaa 108,240,88
CV 26,229,230
CV 27,231,233
Mvaa 112,240,92
ds_write_b128 v15,v[24:27] offset:1024
Mvaa 116,240,96
WL 3
AR 28,168
AR 29,169
Mvaa 80,244,80
AR 30,170
AR 31,171
Mvaa 84,244,84
AR 248,188
AR 249,189
Mvaa 88,244,88
AR 250,190
AR 251,191
Mvaa 92,244,92
Tv 220,13,49152
v_mul_f32 v28,v236,v28
Mvaa 96,244,96
s_barrier
Mvaa 100,248,120
s_nop 0
Tv 222,13,51200
Tv 224,14,49152
Mvaa 104,248,124
Tv 226,14,51200
v_mul_f32 v29,v236,v29
Mvaa 108,248,128
v_mul_f32 v30,v236,v30
v_mul_f32 v31,v236,v31
Mvaa 112,248,132
v_mul_f32 v248,v236,v248
v_mul_f32 v249,v236,v249
Mvaa 116,248,136
WL 4
v_mul_f32 v250,v236,v250
v_mul_f32 v251,v236,v251
Mvaa 80,252,120
s_nop 0
Ta 240,13,53248
Ta 242,13,55296
Mvaa 84,252,124
CV 28,28,29
CV 29,30,31
Mvaa 88,252,128
CV 30,248,249
CV 31,250,251
Mvaa 92,252,132
ds_write_b128 v15,v[28:31] offset:2048
Mvaa 96,252,136
WL 3
AR 32,172
AR 33,173
Mvva 100,220,100
AR 34,174
AR 35,175
Mvva 104,220,104
AR 229,192
AR 230,193
Mvva 108,220,108
AR 231,194
AR 233,195
Mvva 112,220,112
Ta 244,14,53248
v_mul_f32 v32,v240,v32
Mvva 116,220,116
Ta 246,14,55296
v_mul_f32 v33,v240,v33
Mvva 80,224,100
s_nop 0
Ta 248,13,57344
Ta 250,13,59392
Mvva 84,224,104
v_mul_f32 v34,v240,v34
v_mul_f32 v35,v240,v35
Mvva 88,224,108
v_mul_f32 v229,v240,v229
v_mul_f32 v230,v240,v230
Mvva 92,224,112
v_mul_f32 v231,v240,v231
v_mul_f32 v233,v240,v233
Mvva 96,224,116
WL 4
CV 32,32,33
CV 33,34,35
Mvaa 100,240,140
s_nop 0
Ta 252,14,57344
Ta 254,14,59392
Mvaa 104,240,144
CV 34,229,230
CV 35,231,233
Mvaa 108,240,148
ds_write_b128 v15,v[32:35] offset:3072
Mvaa 112,240,152
AR 36,176
AR 37,177
AR 38,178
AR 39,179
AR 248,196
AR 249,197
AR 250,198
AR 251,199
Ra 160,16,53248
Mvaa 116,240,156
WL 4
Ra 164,16,54272
v_mul_f32 v36,v244,v36
Mvaa 80,244,140
Ra 168,16,55296
v_mul_f32 v37,v244,v37
Mvaa 84,244,144
Ra 172,16,56320
v_mul_f32 v38,v244,v38
Mvaa 88,244,148
Ra 176,16,57344
v_mul_f32 v39,v244,v39
Mvaa 92,244,152
v_mul_f32 v248,v244,v248
v_mul_f32 v249,v244,v249
Mvaa 96,244,156
WL 4
v_mul_f32 v250,v244,v250
v_mul_f32 v251,v244,v251
Mvaa 100,248,160
WL 3
Tv 220,13,61440
CV 36,36,37
Mvaa 104,248,164
WL 3
Tv 222,13,63488
Tv 224,14,61440
Mvaa 108,248,168
WL 4
Tv 226,14,63488
Ra 180,16,58368
Mvaa 112,248,172
s_nop 0
Ra 184,16,59392
Ra 188,16,60416
Ra 192,16,61440
Ra 196,16,62464
WL 9
Mvaa 116,248,176
CV 37,38,39
CV 38,248,249
Mvaa 80,252,160
CV 39,250,251
AR 40,200
Mvaa 84,252,164
ds_write_b128 v15,v[36:39] offset:4096
Mvaa 88,252,168
AR 41,201
AR 42,202
Mvaa 92,252,172
AR 43,203
AR 229,220
Mvaa 96,252,176
s_waitcnt vmcnt(1) lgkmcnt(1)
s_barrier
Mvva 100,220,180
s_add_u32 m0,s78,32768
s_cmp_le_u32 s90,0
s_cselect_b64 s[40:41],s[28:29],%6
Mvva 104,220,184
s_nop 0
v_add_u32 v4,294784,v4
AR 230,221
Mvva 108,220,188
s_nop 0
Ta 240,13,0
AR 231,222
Mvva 112,220,192
s_nop 0
Ta 242,13,2048
Ta 244,14,0
Mvva 116,220,196
s_nop 0
Ta 246,14,2048
AR 233,223
Mvva 80,224,180
s_nop 0
Ta 248,13,4096
Ta 250,13,6144
Mvva 84,224,184
LD 4,40,68,0
Mvva 88,224,188
s_nop 0
v_mul_f32 v40,v228,v40
v_mul_f32 v41,v228,v41
Mvva 92,224,192
v_mul_f32 v42,v228,v42
v_mul_f32 v43,v228,v43
Mvva 96,224,196
WL 4
v_mul_f32 v229,v228,v229
v_mul_f32 v230,v228,v230
Zvav 140,240,120
s_nop 0
Ta 252,14,4096
Ta 254,14,6144
Zvav 144,240,124
LD 4,40,69,1024
Zvav 148,240,128
v_mul_f32 v231,v228,v231
v_mul_f32 v233,v228,v233
Zvav 152,240,132
CV 40,40,41
CV 41,42,43
Zvav 156,240,136
WL 2
CV 42,229,230
CV 43,231,233
Zvav 160,244,120
s_nop 0
Tv 220,13,8192
Tv 222,13,10240
Zvav 164,244,124
LD 4,40,70,2048
Zvav 168,244,128
ds_write_b128 v15,v[40:43] offset:5120
Zvav 172,244,132
AR 44,204
AR 45,205
Zvav 176,244,136
AR 46,206
AR 47,207
Mvav 140,248,180
LD 4,40,71,3072
Mvav 144,248,184
s_add_u32 m0,s78,36864
AR 248,224
AR 249,225
Mvav 148,248,188
Tv 224,14,8192
AR 250,226
Mvav 152,248,192
Tv 226,14,10240
AR 251,227
Mvav 156,248,196
WL 3
LD 4,40,72,0
Mvav 160,252,180
s_nop 0
Ta 240,13,12288
Ta 242,13,14336
Mvav 164,252,184
v_mul_f32 v44,v232,v44
v_mul_f32 v45,v232,v45
Mvav 168,252,188
v_mul_f32 v46,v232,v46
v_mul_f32 v47,v232,v47
Mvav 172,252,192
LD 4,40,73,1024
Mvav 176,252,196
v_mul_f32 v248,v232,v248
v_mul_f32 v249,v232,v249
Mvva 140,220,80
s_nop 0
Ta 244,14,12288
Ta 246,14,14336
Mvva 144,220,84
v_mul_f32 v250,v232,v250
v_mul_f32 v251,v232,v251
Mvva 148,220,88
LD 4,40,74,2048
Mvva 152,220,92
CV 44,44,45
CV 45,46,47
CV 46,248,249
CV 47,250,251
AR 48,208
AR 49,209
AR 50,210
AR 51,211
AR 229,228
AR 230,229
AR 231,230
AR 233,231
v_mul_f32 v48,v236,v48
v_mul_f32 v49,v236,v49
v_mul_f32 v50,v236,v50
v_mul_f32 v51,v236,v51
v_mul_f32 v229,v236,v229
v_mul_f32 v230,v236,v230
v_mul_f32 v231,v236,v231
v_mul_f32 v233,v236,v233
CV 48,48,49
CV 49,50,51
CV 50,229,230
CV 51,231,233
AR 52,212
AR 53,213
AR 54,214
AR 55,215
AR 248,232
AR 249,233
AR 250,234
AR 251,235
v_mul_f32 v52,v240,v52
v_mul_f32 v53,v240,v53
v_mul_f32 v54,v240,v54
v_mul_f32 v55,v240,v55
v_mul_f32 v248,v240,v248
v_mul_f32 v249,v240,v249
v_mul_f32 v250,v240,v250
v_mul_f32 v251,v240,v251
CV 52,52,53
CV 53,54,55
CV 54,248,249
CV 55,250,251
AR 56,216
AR 57,217
AR 58,218
AR 59,219
AR 229,236
AR 230,237
AR 231,238
AR 233,239
v_mul_f32 v56,v244,v56
v_mul_f32 v57,v244,v57
v_mul_f32 v58,v244,v58
v_mul_f32 v59,v244,v59
v_mul_f32 v229,v244,v229
v_mul_f32 v230,v244,v230
v_mul_f32 v231,v244,v231
v_mul_f32 v233,v244,v233
CV 56,56,57
CV 57,58,59
CV 58,229,230
CV 59,231,233
Mvva 156,220,96
WL 2
ds_write_b128 v15,v[44:47] offset:6144
Mvva 160,224,80
ds_write_b128 v15,v[48:51] offset:7168
ds_write_b128 v15,v[52:55] offset:8192
ds_write_b128 v15,v[56:59] offset:9216
Mvva 164,224,84
LD 4,40,75,3072
Mvva 168,224,88
AR 60,0
AR 61,1
Mvva 172,224,92
AR 62,2
AR 63,3
Mvva 176,224,96
WL 0
s_barrier
Mvaa 140,240,120
AR 248,20
AR 249,21
Mvaa 144,240,124
AR 250,22
AR 251,23
Mvaa 148,240,128
v_mul_f32 v60,v228,v60
v_mul_f32 v61,v228,v61
Mvaa 152,240,132
s_nop 0
Ta 248,13,16384
Ta 250,13,18432
Mvaa 156,240,136
Ta 252,14,16384
v_mul_f32 v62,v228,v62
Mvaa 160,244,120
s_nop 0
Ta 254,14,18432
Tv 220,13,20480
Mvaa 164,244,124
Tv 222,13,22528
v_mul_f32 v63,v228,v63
Mvaa 168,244,128
v_mul_f32 v248,v228,v248
v_mul_f32 v249,v228,v249
Mvaa 172,244,132
v_mul_f32 v250,v228,v250
v_mul_f32 v251,v228,v251
Mvaa 176,244,136
WL 4
CV 60,60,61
CV 61,62,63
Mvaa 140,248,100
s_nop 0
Tv 224,14,20480
Tv 226,14,22528
Mvaa 144,248,104
CV 62,248,249
)ASM"
R"ASM(CV 63,250,251
Mvaa 148,248,108
ds_write_b128 v15,v[60:63] offset:53248
Mvaa 152,248,112
AR 64,4
AR 65,5
Mvaa 156,248,116
WL 3
AR 66,6
AR 67,7
Mvaa 160,252,100
AR 229,24
AR 230,25
Mvaa 164,252,104
AR 231,26
AR 233,27
Mvaa 168,252,108
Ta 240,13,24576
v_mul_f32 v64,v232,v64
Mvaa 172,252,112
Ta 242,13,26624
v_mul_f32 v65,v232,v65
Mvaa 176,252,116
v_mul_f32 v66,v232,v66
v_mul_f32 v67,v232,v67
Mvva 140,220,140
s_nop 0
Ta 244,14,24576
Ta 246,14,26624
Mvva 144,220,144
v_mul_f32 v229,v232,v229
v_mul_f32 v230,v232,v230
Mvva 148,220,148
v_mul_f32 v231,v232,v231
v_mul_f32 v233,v232,v233
Mvva 152,220,152
CV 64,64,65
CV 65,66,67
Mvva 156,220,156
WL 4
CV 66,229,230
CV 67,231,233
Mvva 160,224,140
s_nop 0
Ta 248,13,28672
Ta 250,13,30720
Mvva 164,224,144
ds_write_b128 v15,v[64:67] offset:54272
Mvva 168,224,148
AR 68,8
AR 69,9
Mvva 172,224,152
AR 70,10
AR 71,11
Mvva 176,224,156
WL 3
AR 248,28
AR 249,29
Mvaa 140,240,160
AR 250,30
AR 251,31
Mvaa 144,240,164
Ta 252,14,28672
v_mul_f32 v68,v236,v68
Mvaa 148,240,168
Ta 254,14,30720
v_mul_f32 v69,v236,v69
Mvaa 152,240,172
v_mul_f32 v70,v236,v70
v_mul_f32 v71,v236,v71
Mvaa 156,240,176
v_mul_f32 v248,v236,v248
v_mul_f32 v249,v236,v249
Mvaa 160,244,160
v_mul_f32 v250,v236,v250
v_mul_f32 v251,v236,v251
Mvaa 164,244,164
CV 68,68,69
CV 69,70,71
Mvaa 168,244,168
CV 70,248,249
CV 71,250,251
Mvaa 172,244,172
ds_write_b128 v15,v[68:71] offset:55296
Mvaa 176,244,176
s_waitcnt vmcnt(0) lgkmcnt(1)
s_barrier
Mvaa 140,248,180
s_add_u32 m0,s78,0
s_cmp_le_u32 s90,1
s_cselect_b64 s[40:41],s[28:29],%6
Mvaa 144,248,184
v_add_u32 v4,128,v4
AR 72,12
Mvaa 148,248,188
LD 4,40,68,0
Mvaa 152,248,192
s_nop 0
Tv 220,13,32768
Tv 222,13,34816
Mvaa 156,248,196
Tv 224,14,32768
AR 73,13
Mvaa 160,252,180
s_nop 0
Tv 226,14,34816
Ta 240,13,36864
Mvaa 164,252,184
Ta 242,13,38912
AR 74,14
Mvaa 168,252,188
LD 4,40,69,1024
Mvaa 172,252,192
AR 75,15
AR 229,32
Mvaa 176,252,196
WL 4
AR 230,33
AR 231,34
Mvvv 100,220,20
s_nop 0
Ta 244,14,36864
Ta 246,14,38912
Mvvv 104,220,24
LD 4,40,70,2048
Mvvv 108,220,28
AR 233,35
v_mul_f32 v72,v240,v72
Mvvv 112,220,32
v_mul_f32 v73,v240,v73
v_mul_f32 v74,v240,v74
Mvvv 116,220,36
WL 2
v_mul_f32 v75,v240,v75
v_mul_f32 v229,v240,v229
Mvvv 80,224,20
s_nop 0
Ta 248,13,40960
Ta 250,13,43008
Mvvv 84,224,24
LD 4,40,71,3072
Mvvv 88,224,28
s_add_u32 m0,s78,4096
v_mul_f32 v230,v240,v230
v_mul_f32 v231,v240,v231
Mvvv 92,224,32
v_mul_f32 v233,v240,v233
CV 72,72,73
Mvvv 96,224,36
CV 73,74,75
CV 74,229,230
Mvav 100,240,40
s_nop 0
Ta 252,14,40960
Ta 254,14,43008
Mvav 104,240,44
LD 4,40,72,0
Mvav 108,240,48
CV 75,231,233
AR 76,16
Mvav 112,240,52
ds_write_b128 v15,v[72:75] offset:56320
Mvav 116,240,56
WL 3
AR 77,17
AR 78,18
Mvav 80,244,40
LD 4,40,73,1024
Mvav 84,244,44
AR 79,19
AR 248,36
Mvav 88,244,48
AR 249,37
AR 250,38
AR 251,39
v_mul_f32 v76,v244,v76
v_mul_f32 v77,v244,v77
v_mul_f32 v78,v244,v78
v_mul_f32 v79,v244,v79
v_mul_f32 v248,v244,v248
v_mul_f32 v249,v244,v249
v_mul_f32 v250,v244,v250
v_mul_f32 v251,v244,v251
CV 76,76,77
CV 77,78,79
CV 78,248,249
CV 79,250,251
Mvav 92,244,52
Tv 220,13,45056
AR 200,40
Mvav 96,244,56
Tv 222,13,47104
AR 201,41
Mvav 100,248,60
s_nop 0
Tv 224,14,45056
Tv 226,14,47104
Mvav 104,248,64
LD 4,40,74,2048
Mvav 108,248,68
ds_write_b128 v15,v[76:79] offset:57344
Mvav 112,248,72
s_nop 0
Ra 200,16,0
Ra 204,16,1024
Ra 208,16,2048
Ra 212,16,3072
Ra 216,16,4096
Ra 220,16,5120
Ra 224,16,6144
Ra 228,16,7168
Ra 232,16,8192
Ra 236,16,9216
AR 202,42
AR 203,43
AR 229,60
AR 230,61
AR 231,62
AR 233,63
v_mul_f32 v200,v228,v200
v_mul_f32 v201,v228,v201
v_mul_f32 v202,v228,v202
v_mul_f32 v203,v228,v203
v_mul_f32 v229,v228,v229
v_mul_f32 v230,v228,v230
v_mul_f32 v231,v228,v231
v_mul_f32 v233,v228,v233
CV 200,200,201
CV 201,202,203
CV 202,229,230
CV 203,231,233
AR 204,44
AR 205,45
AR 206,46
AR 207,47
AR 248,64
AR 249,65
AR 250,66
AR 251,67
v_mul_f32 v204,v232,v204
v_mul_f32 v205,v232,v205
v_mul_f32 v206,v232,v206
v_mul_f32 v207,v232,v207
v_mul_f32 v248,v232,v248
v_mul_f32 v249,v232,v249
v_mul_f32 v250,v232,v250
v_mul_f32 v251,v232,v251
CV 204,204,205
CV 205,206,207
CV 206,248,249
CV 207,250,251
AR 208,48
AR 209,49
AR 210,50
AR 211,51
AR 229,68
AR 230,69
AR 231,70
AR 233,71
v_mul_f32 v208,v236,v208
v_mul_f32 v209,v236,v209
v_mul_f32 v210,v236,v210
v_mul_f32 v211,v236,v211
v_mul_f32 v229,v236,v229
v_mul_f32 v230,v236,v230
v_mul_f32 v231,v236,v231
v_mul_f32 v233,v236,v233
CV 208,208,209
CV 209,210,211
CV 210,229,230
CV 211,231,233
AR 212,52
AR 213,53
AR 214,54
AR 215,55
AR 248,72
AR 249,73
AR 250,74
AR 251,75
v_mul_f32 v212,v240,v212
v_mul_f32 v213,v240,v213
v_mul_f32 v214,v240,v214
v_mul_f32 v215,v240,v215
v_mul_f32 v248,v240,v248
v_mul_f32 v249,v240,v249
v_mul_f32 v250,v240,v250
v_mul_f32 v251,v240,v251
CV 212,212,213
CV 213,214,215
CV 214,248,249
CV 215,250,251
AR 216,56
AR 217,57
AR 218,58
AR 219,59
AR 229,76
AR 230,77
AR 231,78
AR 233,79
v_mul_f32 v216,v244,v216
v_mul_f32 v217,v244,v217
v_mul_f32 v218,v244,v218
v_mul_f32 v219,v244,v219
v_mul_f32 v229,v244,v229
v_mul_f32 v230,v244,v230
v_mul_f32 v231,v244,v231
v_mul_f32 v233,v244,v233
CV 216,216,217
CV 217,218,219
CV 218,229,230
CV 219,231,233
Mvav 116,248,76
WL 15
Mvav 80,252,60
ds_write_b128 v15,v[200:203] offset:58368
ds_write_b128 v15,v[204:207] offset:59392
ds_write_b128 v15,v[208:211] offset:60416
ds_write_b128 v15,v[212:215] offset:61440
ds_write_b128 v15,v[216:219] offset:62464
Mvav 84,252,64
LD 4,40,75,3072
Mvav 88,252,68
s_and_b32 s86,%12,458752
s_cmp_eq_u32 s86,393216
s_cselect_b32 s86,s91,0x40000000
Mvav 92,252,72
s_add_u32 s86,s86,90112
Mvav 96,252,76
WL 0
s_barrier
Mvvv 100,220,200
buffer_load_dword v255,v17,s[92:95],s86 offen
Mvvv 104,220,204
s_add_u32 m0,s78,65536
s_cmp_le_u32 s90,2
s_cselect_b64 s[40:41],s[28:29],%6
Mvvv 108,220,208
v_subrev_u32 v4,294784,v4
Mvvv 112,220,212
s_nop 0
Ta 240,13,49152
Ta 242,13,51200
Mvvv 116,220,216
Ta 244,14,49152
Mvvv 80,224,200
s_nop 0
Ta 246,14,51200
Ta 248,13,53248
Mvvv 84,224,204
s_nop 0
Ta 250,13,55296
Ra 0,16,53248
Mvvv 88,224,208
Ra 4,16,54272
Mvvv 92,224,212
LD 4,40,68,0
Mvvv 96,224,216
WL 6
Ra 8,16,55296
Ra 12,16,56320
Mvaa 100,240,200
Ta 252,14,53248
Mvaa 104,240,204
s_nop 0
Ta 254,14,55296
Ra 16,16,57344
Mvaa 108,240,208
LD 4,40,69,1024
Mvaa 112,240,212
s_nop 0
Ra 20,16,58368
Ra 24,16,59392
Mvaa 116,240,216
WL 8
Ra 28,16,60416
Mvaa 80,244,200
Tv 220,13,57344
Mvaa 84,244,204
s_nop 0
Tv 222,13,59392
Ra 32,16,61440
Mvaa 88,244,208
LD 4,40,70,2048
Mvaa 92,244,212
Ra 36,16,62464
Mvaa 96,244,216
Mvaa 100,248,220
s_nop 0
Tv 224,14,57344
Tv 226,14,59392
Mvaa 104,248,224
LD 4,40,71,3072
Mvaa 108,248,228
s_add_u32 m0,s78,69632
Mvaa 112,248,232
Mvaa 116,248,236
WL 3
Mvaa 80,252,220
s_nop 0
Ta 240,13,61440
Ta 242,13,63488
Mvaa 84,252,224
LD 4,40,72,0
Mvaa 88,252,228
Mvaa 92,252,232
Mvaa 96,252,236
s_nop 0
Ta 244,14,61440
Ta 246,14,63488
Mvva 100,220,0
LD 4,40,73,1024
Mvva 104,220,4
Mvva 108,220,8
Mvva 112,220,12
Mvva 116,220,16
WL 2
LD 4,40,74,2048
Mvva 80,224,0
Mvva 84,224,4
Mvva 88,224,8
Mvva 92,224,12
LD 4,40,75,3072
Mvva 96,224,16
s_waitcnt vmcnt(9) lgkmcnt(0)
s_barrier
Mvaa 100,240,20
s_add_u32 m0,s78,32768
Ta 248,13,0
Ta 250,13,2048
Mvaa 104,240,24
s_nop 0
Ta 252,14,0
Ta 254,14,2048
Mvaa 108,240,28
v_add_u32 v4,294912,v4
Mvaa 112,240,32
LD 4,40,68,0
Mvaa 116,240,36
Mvaa 80,244,20
s_nop 0
Tv 220,13,4096
Tv 222,13,6144
Mvaa 84,244,24
Mvaa 88,244,28
LD 4,40,69,1024
Mvaa 92,244,32
Mvaa 96,244,36
WL 2
CV 100,100,101
CV 101,102,103
Mvav 140,248,20
s_bfe_u32 s76,%12,0x30008
Tv 224,14,4096
Tv 226,14,6144
Mvav 144,248,24
LD 4,40,70,2048
Mvav 148,248,28
s_mul_i32 s77,s76,184320
CV 102,80,81
CV 103,82,83
Mvav 152,248,32
s_mov_b64 s[36:37],%7
CV 104,104,105
CV 105,106,107
Mvav 156,248,36
CV 106,84,85
CV 107,86,87
Mvav 160,252,20
s_sub_u32 s36,s36,s77
s_subb_u32 s37,s37,0
Ta 240,13,8192
Ta 242,13,10240
Mvav 164,252,24
LD 4,40,71,3072
Mvav 168,252,28
s_add_u32 m0,s78,36864
CV 108,108,109
CV 109,110,111
Mvav 172,252,32
s_and_b32 s37,s37,0xffff
CV 110,88,89
CV 111,90,91
Mvav 176,252,36
WL 2
CV 112,112,113
CV 113,114,115
Mvvv 140,220,40
s_mov_b32 s38,1474560
Ta 244,14,8192
Ta 246,14,10240
Mvvv 144,220,44
LD 4,40,72,0
Mvvv 148,220,48
s_mov_b64 s[96:97],%13
CV 114,92,93
CV 115,94,95
Mvvv 152,220,52
s_and_b32 s97,s97,0xffff
CV 116,116,117
CV 117,118,119
Mvvv 156,220,56
CV 118,96,97
CV 119,98,99
Mvvv 160,224,40
s_mov_b32 s98,184320
Ta 248,13,12288
Ta 250,13,14336
Mvvv 164,224,44
LD 4,40,73,1024
Mvvv 168,224,48
s_mov_b64 s[24:25],%11
s_and_b32 s25,s25,0xffff
s_mov_b32 s26,2048
Mvvv 172,224,52
s_lshl_b32 s66,s76,7
s_add_u32 s66,s66,128
s_mov_b32 s67,0x207
Mvvv 176,224,56
WL 2
s_lshl_b32 s76,s76,2
s_add_u32 s76,s76,%8
Mvav 140,240,60
s_add_u32 s77,s76,0
Ta 252,14,12288
Ta 254,14,14336
Mvav 144,240,64
LD 4,40,74,2048
Mvav 148,240,68
s_lshl_b32 s48,s77,10
s_mul_i32 s85,s77,205
s_lshr_b32 s85,s85,10
Mvav 152,240,72
s_mul_i32 s89,s85,5
s_sub_u32 s89,s77,s89
s_lshl_b32 s60,s89,4
Mvav 156,240,76
s_and_b32 s89,s85,1
s_mul_i32 s89,s89,80
Mvav 160,244,60
s_add_u32 s60,s60,s89
Tv 220,13,16384
Tv 222,13,18432
Mvav 164,244,64
LD 4,40,75,3072
Mvav 168,244,68
s_lshr_b32 s85,s85,1
s_lshl_b32 s85,s85,6
s_lshl_b32 s89,s90,7
Mvav 172,244,72
s_sub_u32 s85,s85,s89
s_cselect_b32 s89,1152,0
s_add_u32 s85,s85,s89
s_mul_i32 s54,s60,1152
Mvav 176,244,76
WL 2
s_add_u32 s54,s54,s85
s_add_u32 s77,s76,32
Mvav 140,248,200
s_lshl_b32 s49,s77,10
Tv 224,14,16384
Tv 226,14,18432
Mvav 144,248,204
buffer_store_dwordx4 v[100:103],v5,s[44:47],s79 offen offset:0 sc1
Mvav 148,248,208
s_mul_i32 s85,s77,205
s_lshr_b32 s85,s85,10
s_mul_i32 s89,s85,5
Mvav 152,248,212
s_sub_u32 s89,s77,s89
s_lshl_b32 s61,s89,4
s_and_b32 s89,s85,1
Mvav 156,248,216
s_mul_i32 s89,s89,80
s_add_u32 s61,s61,s89
Mvav 160,252,200
s_lshr_b32 s85,s85,1
Ta 240,13,20480
Ta 242,13,22528
Mvav 164,252,204
buffer_store_dwordx4 v[104:107],v5,s[44:47],s79 offen offset:1024 sc1
Mvav 168,252,208
s_lshl_b32 s85,s85,6
s_lshl_b32 s89,s90,7
s_sub_u32 s85,s85,s89
s_cselect_b32 s89,1152,0
Mvav 172,252,212
s_add_u32 s85,s85,s89
s_mul_i32 s55,s61,1152
s_add_u32 s55,s55,s85
Mvav 176,252,216
WL 2
s_add_u32 s77,s76,64
s_lshl_b32 s50,s77,10
Mvva 140,220,200
s_mul_i32 s85,s77,205
Ta 244,14,20480
Ta 246,14,22528
Mvva 144,220,204
buffer_store_dwordx4 v[108:111],v5,s[44:47],s79 offen offset:2048 sc1
Mvva 148,220,208
s_lshr_b32 s85,s85,10
s_mul_i32 s89,s85,5
s_sub_u32 s89,s77,s89
Mvva 152,220,212
s_lshl_b32 s62,s89,4
s_and_b32 s89,s85,1
s_mul_i32 s89,s89,80
Mvva 156,220,216
s_add_u32 s62,s62,s89
s_lshr_b32 s85,s85,1
Mvva 160,224,200
s_lshl_b32 s85,s85,6
Ta 248,13,24576
Ta 250,13,26624
Mvva 164,224,204
buffer_store_dwordx4 v[112:115],v5,s[44:47],s79 offen offset:3072 sc1
Mvva 168,224,208
s_lshl_b32 s89,s90,7
s_sub_u32 s85,s85,s89
s_cselect_b32 s89,1152,0
s_add_u32 s85,s85,s89
Mvva 172,224,212
s_mul_i32 s56,s62,1152
s_add_u32 s56,s56,s85
s_add_u32 s77,s76,96
Mvva 176,224,216
WL 2
s_lshl_b32 s51,s77,10
s_mul_i32 s85,s77,205
Mvaa 140,240,220
s_lshr_b32 s85,s85,10
Ta 252,14,24576
Ta 254,14,26624
Mvaa 144,240,224
buffer_store_dwordx4 v[116:119],v5,s[44:47],s84 offen offset:0 sc1
Mvaa 148,240,228
s_add_u32 s79,s79,20480
s_add_u32 s84,s84,20480
s_mul_i32 s89,s85,5
Mvaa 152,240,232
s_sub_u32 s89,s77,s89
s_lshl_b32 s63,s89,4
s_and_b32 s89,s85,1
Mvaa 156,240,236
s_mul_i32 s89,s89,80
s_add_u32 s63,s63,s89
Mvaa 160,244,220
s_lshr_b32 s85,s85,1
Tv 220,13,28672
Tv 222,13,30720
Mvaa 164,244,224
s_lshl_b32 s85,s85,6
s_lshl_b32 s89,s90,7
s_sub_u32 s85,s85,s89
s_cselect_b32 s89,1152,0
Mvaa 168,244,228
s_add_u32 s85,s85,s89
s_mul_i32 s57,s63,1152
s_add_u32 s57,s57,s85
Mvaa 172,244,232
s_add_u32 s77,s76,128
s_lshl_b32 s52,s77,10
s_mul_i32 s85,s77,205
Mvaa 176,244,236
WL 2
Tv 224,14,28672
Tv 226,14,30720
Mvaa 140,248,0
s_lshr_b32 s85,s85,10
s_mul_i32 s89,s85,5
s_sub_u32 s89,s77,s89
Mvaa 144,248,4
s_lshl_b32 s64,s89,4
s_and_b32 s89,s85,1
s_mul_i32 s89,s89,80
Mvaa 148,248,8
s_add_u32 s64,s64,s89
s_lshr_b32 s85,s85,1
s_lshl_b32 s85,s85,6
Mvaa 152,248,12
s_lshl_b32 s89,s90,7
s_sub_u32 s85,s85,s89
s_cselect_b32 s89,1152,0
s_add_u32 s85,s85,s89
Mvaa 156,248,16
s_mul_i32 s58,s64,1152
s_add_u32 s58,s58,s85
Mvaa 160,252,0
s_add_u32 s77,s76,160
s_lshl_b32 s53,s77,10
s_mul_i32 s85,s77,205
Mvaa 164,252,4
s_lshr_b32 s85,s85,10
s_mul_i32 s89,s85,5
s_sub_u32 s89,s77,s89
Mvaa 168,252,8
s_lshl_b32 s65,s89,4
s_and_b32 s89,s85,1
s_mul_i32 s89,s89,80
Mvaa 172,252,12
s_add_u32 s65,s65,s89
s_lshr_b32 s85,s85,1
s_lshl_b32 s85,s85,6
Mvaa 176,252,16
s_waitcnt vmcnt(13) lgkmcnt(0)
s_barrier
Mvva 140,220,20
s_add_u32 m0,s78,0
Ta 76,253,0
Ta 78,253,2048
Mvva 144,220,24
s_lshl_b32 s89,s90,7
Ta 72,253,4096
Ta 74,253,6144
Mvva 148,220,28
s_sub_u32 s85,s85,s89
s_cselect_b32 s89,1152,0
Ta 240,253,8192
Ta 242,253,10240
Mvva 152,220,32
s_add_u32 s85,s85,s89
Ta 244,253,12288
Ta 246,253,14336
Mvva 156,220,36
s_cmp_le_u32 s90,3
s_cselect_b64 s[40:41],s[28:29],%6
Mvva 160,224,20
s_mul_i32 s59,s65,1152
s_add_u32 s59,s59,s85
v_subrev_u32 v4,294784,v4
Mvva 164,224,24
LD 4,40,68,0
Mvva 168,224,28
s_cmp_ge_u32 s77,180
s_cselect_b32 s85,0x40000000,0
s_or_b32 s53,s53,s85
s_or_b32 s59,s59,s85
Mvva 172,224,32
v_bfe_u32 v9,v5,4,4
v_mul_u32_u24 v6,1152,v9
Mvva 176,224,36
WL 4
v_lshrrev_b32 v7,8,v5
v_lshl_add_u32 v6,v7,4,v6
Zvav 100,76,120
LD 4,40,69,1024
Mvav 100,72,180
WL 0
v_lshrrev_b32 v8,2,v9
v_xor_b32 v8,v8,v7
Mvaa 100,240,80
v_and_b32 v7,1,v9
v_lshlrev_b32 v7,4,v7
Mvaa 100,244,120
v_mov_b32 v228,0x3f80
v_lshlrev_b32 v7,v7,v228
Zvav 104,76,124
LD 4,40,70,2048
Mvav 104,72,184
s_nop 0
Ta 248,254,0
Ta 250,254,2048
Mvaa 104,240,84
s_nop 0
Ta 252,254,4096
Ta 254,254,6144
Mvaa 104,244,124
s_nop 0
Tv 220,254,8192
Tv 222,254,10240
Zvav 108,76,128
s_nop 0
Tv 224,254,12288
Tv 226,254,14336
Mvav 108,72,188
LD 4,40,71,3072
Mvaa 108,240,88
s_add_u32 m0,s78,4096
v_bfe_u32 v9,v9,1,1
v_lshl_or_b32 v8,v8,1,v9
Mvaa 108,244,128
v_cmp_eq_u32 vcc,0,v8
s_nop 1
v_cndmask_b32 v230,0,v7,vcc
v_cmp_eq_u32 vcc,1,v8
s_nop 1
v_cndmask_b32 v231,0,v7,vcc
Zvav 112,76,132
v_mov_b32 v228,0
v_mov_b32 v229,0
Mvav 112,72,192
LD 4,40,72,0
Mvaa 112,240,92
v_mov_b32 v232,0
v_mov_b32 v233,0
Mvaa 112,244,132
v_bfe_u32 v7,v5,4,3
v_lshlrev_b32 v7,7,v7
Zvav 116,76,136
v_or_b32 v8,s87,v5
v_min_u32 v8,1,v8
Mvav 116,72,196
LD 4,40,73,1024
Mvaa 116,240,96
v_lshlrev_b32 v8,30,v8
v_mov_b32 v3,%1
Mvaa 116,244,136
WL 0
CV 140,140,141
CV 141,142,143
Zvav 80,248,120
CV 142,160,161
CV 143,162,163
Mvav 80,252,180
LD 4,40,74,2048
Mvva 80,220,80
CV 144,144,145
CV 145,146,147
Mvva 80,224,120
CV 146,164,165
CV 147,166,167
Zvav 84,248,124
CV 148,148,149
CV 149,150,151
Mvav 84,252,184
s_nop 0
Ta 76,253,16384
Ta 78,253,18432
Mvva 84,220,84
s_nop 0
Ta 72,253,20480
Ta 74,253,22528
Mvva 84,224,124
s_nop 0
Ta 240,253,24576
Ta 242,253,26624
Zvav 88,248,128
s_nop 0
Ta 244,253,28672
Ta 246,253,30720
Mvav 88,252,188
LD 4,40,75,3072
Mvva 88,220,88
s_and_b32 s86,%12,458752
s_cmp_eq_u32 s86,458752
s_cselect_b32 s86,s91,0x40000000
Mvva 88,224,128
s_add_u32 s86,s86,94208
CV 150,168,169
CV 151,170,171
Zvav 92,248,132
CV 152,152,153
CV 153,154,155
Mvav 92,252,192
buffer_load_dword v255,v17,s[92:95],s86 offen
Mvva 92,220,92
CV 154,172,173
CV 155,174,175
Mvva 92,224,132
CV 156,156,157
CV 157,158,159
Zvav 96,248,136
CV 158,176,177
CV 159,178,179
Mvav 96,252,196
Mvva 96,220,96
Mvva 96,224,136
WL 0
Mvaa 100,76,100
Mvaa 100,72,140
buffer_store_dwordx4 v[140:143],v5,s[44:47],s79 offen offset:0 sc1
Mvaa 100,240,160
Mvaa 100,244,180
Mvaa 104,76,104
Mvaa 104,72,144
s_nop 0
Ta 248,254,16384
Ta 250,254,18432
Mvaa 104,240,164
s_nop 0
Ta 252,254,20480
Ta 254,254,22528
Mvaa 104,244,184
s_nop 0
Tv 220,254,24576
Tv 222,254,26624
Mvaa 108,76,108
s_nop 0
Tv 224,254,28672
Tv 226,254,30720
Mvaa 108,72,148
buffer_store_dwordx4 v[144:147],v5,s[44:47],s79 offen offset:1024 sc1
Mvaa 108,240,168
Mvaa 108,244,188
Mvaa 112,76,112
Mvaa 112,72,152
Mvaa 112,240,172
buffer_store_dwordx4 v[148:151],v5,s[44:47],s79 offen offset:2048 sc1
Mvaa 112,244,192
Mvaa 116,76,116
Mvaa 116,72,156
Mvaa 116,240,176
Mvaa 116,244,196
WL 0
buffer_store_dwordx4 v[152:155],v5,s[44:47],s79 offen offset:3072 sc1
Mvaa 80,248,100
Mvaa 80,252,140
Mvva 80,220,160
Mvva 80,224,180
Mvaa 84,248,104
buffer_store_dwordx4 v[156:159],v5,s[44:47],s84 offen offset:0 sc1
Mvaa 84,252,144
s_add_u32 s79,s79,20480
s_add_u32 s84,s84,20480
Mvva 84,220,164
Mvva 84,224,184
Mvaa 88,248,108
Mvaa 88,252,148
s_waitcnt vmcnt(19)
s_barrier
Mvva 88,220,168
s_add_u32 m0,s78,65536
Ta 76,13,32768
Ta 78,13,34816
Mvva 88,224,188
s_nop 0
Ta 72,13,36864
Ta 74,13,38912
Mvaa 92,248,112
s_nop 0
Ta 240,13,40960
Ta 242,13,43008
Mvaa 92,252,152
s_nop 0
Ta 244,13,45056
Ta 246,13,47104
Mvva 92,220,172
v_add_u32 v4,294912,v4
Mvva 92,224,192
LD 4,40,68,0
Mvaa 96,248,116
Mvaa 96,252,156
Mvva 96,220,176
Mvva 96,224,196
WL 4
LD 4,40,69,1024
Mvav 100,76,20
Mvav 100,72,40
WL 0
Mvav 100,240,60
Mvav 100,244,200
LD 4,40,70,2048
Mvav 104,76,24
Mvav 104,72,44
s_nop 0
Ta 248,14,32768
Ta 250,14,34816
Mvav 104,240,64
s_nop 0
Ta 252,14,36864
Ta 254,14,38912
Mvav 104,244,204
s_nop 0
Tv 220,14,40960
Tv 222,14,43008
Mvav 108,76,28
s_nop 0
Tv 224,14,45056
Tv 226,14,47104
Mvav 108,72,48
LD 4,40,71,3072
Mvav 108,240,68
s_add_u32 m0,s78,69632
Mvav 108,244,208
Mvav 112,76,32
Mvav 112,72,52
LD 4,40,72,0
Mvav 112,240,72
Mvav 112,244,212
Mvav 116,76,36
Mvav 116,72,56
LD 4,40,73,1024
Mvav 116,240,76
Mvav 116,244,216
WL 0
Mvav 80,248,20
Mvav 80,252,40
LD 4,40,74,2048
Mvvv 80,220,60
Mvvv 80,224,200
Mvav 84,248,24
Mvav 84,252,44
s_nop 0
Ta 76,13,49152
Ta 78,13,51200
Mvvv 84,220,64
s_nop 0
Ta 72,13,53248
Ta 74,13,55296
Mvvv 84,224,204
s_nop 0
Ta 240,13,57344
Ta 242,13,59392
Mvav 88,248,28
s_nop 0
Ta 244,13,61440
Ta 246,13,63488
Mvav 88,252,48
LD 4,40,75,3072
Mvvv 88,220,68
Mvvv 88,224,208
Mvav 92,248,32
Mvav 92,252,52
Mvvv 92,220,72
Mvvv 92,224,212
Mvav 96,248,36
Mvav 96,252,56
Mvvv 96,220,76
Mvvv 96,224,216
WL 0
Mvaa 100,76,200
Mvaa 100,72,220
Mvaa 100,240,0
Mvaa 100,244,20
Mvaa 104,76,204
Mvaa 104,72,224
s_nop 0
Ta 248,14,49152
Ta 250,14,51200
Mvaa 104,240,4
s_nop 0
Ta 252,14,53248
Ta 254,14,55296
Mvaa 104,244,24
s_nop 0
Tv 220,14,57344
Tv 222,14,59392
Mvaa 108,76,208
s_nop 0
Tv 224,14,61440
Tv 226,14,63488
Mvaa 108,72,228
Mvaa 108,240,8
Mvaa 108,244,28
Mvaa 112,76,212
Mvaa 112,72,232
Mvaa 112,240,12
Mvaa 112,244,32
Mvaa 116,76,216
Mvaa 116,72,236
Mvaa 116,240,16
Mvaa 116,244,36
WL 0
Mvaa 80,248,200
Mvaa 80,252,220
Mvva 80,220,0
Mvva 80,224,20
Mvaa 84,248,204
Mvaa 84,252,224
Mvva 84,220,4
Mvva 84,224,24
CV 100,100,101
CV 101,102,103
Mvaa 88,248,208
s_nop 0
CV 102,80,81
CV 103,82,83
Mvaa 88,252,228
s_waitcnt vmcnt(0)
s_barrier
Mvva 88,220,8
s_add_u32 m0,s78,32768
Ta 76,13,0
Ta 78,13,2048
Mvva 88,224,28
v_mov_b32 v2,%0
v_add_u32 v2,2,v2
Mvaa 92,248,212
buffer_store_dwordx2 v[2:3],v8,s[24:27],s66 offen sc1
Mvaa 92,252,232
s_nop 0
Ta 72,13,4096
Ta 74,13,6144
Mvva 92,220,12
s_nop 0
Ta 240,13,8192
Ta 242,13,10240
Mvva 92,224,32
s_nop 0
Ta 244,13,12288
Ta 246,13,14336
Mvaa 96,248,216
s_cmp_le_u32 s90,4
s_cselect_b64 s[40:41],s[28:29],%6
v_subrev_u32 v4,294784,v4
Mvaa 96,252,236
LD 4,40,68,0
Mvva 96,220,16
CV 104,104,105
CV 105,106,107
Mvva 96,224,36
WL 6
CV 106,84,85
CV 107,86,87
Zvav 140,76,120
WL 4
CV 108,108,109
CV 109,110,111
Mvav 140,72,180
WL 2
LD 4,40,69,1024
Mvaa 140,240,80
WL 0
CV 110,88,89
CV 111,90,91
Mvaa 140,244,120
CV 112,112,113
CV 113,114,115
Zvav 144,76,124
CV 114,92,93
CV 115,94,95
Mvav 144,72,184
s_nop 0
Ta 248,14,0
Ta 250,14,2048
Mvaa 144,240,84
s_nop 0
Ta 252,14,4096
Ta 254,14,6144
Mvaa 144,244,124
s_nop 0
Tv 220,14,8192
Tv 222,14,10240
Zvav 148,76,128
s_nop 0
Tv 224,14,12288
Tv 226,14,14336
Mvav 148,72,188
LD 4,40,70,2048
Mvaa 148,240,88
CV 116,116,117
CV 117,118,119
Mvaa 148,244,128
CV 118,96,97
CV 119,98,99
Zvav 152,76,132
Mvav 152,72,192
LD 4,40,71,3072
Mvaa 152,240,92
s_add_u32 m0,s78,36864
Mvaa 152,244,132
Zvav 156,76,136
Mvav 156,72,196
LD 4,40,72,0
Mvaa 156,240,96
Mvaa 156,244,136
WL 0
Zvav 160,248,120
Mvav 160,252,180
LD 4,40,73,1024
Mvva 160,220,80
Mvva 160,224,120
Zvav 164,248,124
Mvav 164,252,184
s_nop 0
Ta 76,13,16384
Ta 78,13,18432
Mvva 164,220,84
s_nop 0
Ta 72,13,20480
Ta 74,13,22528
Mvva 164,224,124
s_nop 0
Ta 240,13,24576
Ta 242,13,26624
Zvav 168,248,128
s_nop 0
Ta 244,13,28672
Ta 246,13,30720
Mvav 168,252,188
LD 4,40,74,2048
Mvva 168,220,88
Mvva 168,224,128
Zvav 172,248,132
Mvav 172,252,192
LD 4,40,75,3072
Mvva 172,220,92
Mvva 172,224,132
Zvav 176,248,136
Mvav 176,252,196
buffer_store_dwordx4 v[100:103],v5,s[44:47],s79 offen offset:0 sc1
Mvva 176,220,96
Mvva 176,224,136
WL 0
Mvaa 140,76,100
Mvaa 140,72,140
Mvaa 140,240,160
buffer_store_dwordx4 v[104:107],v5,s[44:47],s79 offen offset:1024 sc1
Mvaa 140,244,180
Mvaa 144,76,104
Mvaa 144,72,144
s_nop 0
Ta 248,14,16384
Ta 250,14,18432
Mvaa 144,240,164
s_nop 0
Ta 252,14,20480
Ta 254,14,22528
Mvaa 144,244,184
s_nop 0
Tv 220,14,24576
Tv 222,14,26624
Mvaa 148,76,108
s_nop 0
Tv 224,14,28672
Tv 226,14,30720
Mvaa 148,72,148
buffer_store_dwordx4 v[108:111],v5,s[44:47],s79 offen offset:2048 sc1
Mvaa 148,240,168
Mvaa 148,244,188
Mvaa 152,76,112
Mvaa 152,72,152
Mvaa 152,240,172
buffer_store_dwordx4 v[112:115],v5,s[44:47],s79 offen offset:3072 sc1
Mvaa 152,244,192
Mvaa 156,76,116
Mvaa 156,72,156
Mvaa 156,240,176
Mvaa 156,244,196
WL 0
buffer_store_dwordx4 v[116:119],v5,s[44:47],s84 offen offset:0 sc1
Mvaa 160,248,100
s_add_u32 s79,s79,20480
s_add_u32 s84,s84,20480
Mvaa 160,252,140
Mvva 160,220,160
Mvva 160,224,180
Mvaa 164,248,104
Mvaa 164,252,144
Mvva 164,220,164
Mvva 164,224,184
Mvaa 168,248,108
Mvaa 168,252,148
s_barrier
Mvva 168,220,168
s_add_u32 m0,s78,0
Ta 76,253,0
Ta 78,253,2048
Mvva 168,224,188
s_nop 0
Ta 72,253,4096
Ta 74,253,6144
Mvaa 172,248,112
s_nop 0
Ta 240,253,8192
Ta 242,253,10240
Mvaa 172,252,152
s_nop 0
Ta 244,253,12288
Ta 246,253,14336
Mvva 172,220,172
v_add_u32 v4,294912,v4
Mvva 172,224,192
LD 4,40,68,0
Mvaa 176,248,116
Mvaa 176,252,156
Mvva 176,220,176
Mvva 176,224,196
WL 4
LD 4,40,69,1024
Mvav 140,76,20
Mvav 140,72,40
WL 0
Mvav 140,240,60
Mvav 140,244,200
LD 4,40,70,2048
Mvav 144,76,24
Mvav 144,72,44
s_nop 0
Ta 248,254,0
Ta 250,254,2048
Mvav 144,240,64
s_nop 0
Ta 252,254,4096
Ta 254,254,6144
Mvav 144,244,204
s_nop 0
Tv 220,254,8192
Tv 222,254,10240
Mvav 148,76,28
s_nop 0
Tv 224,254,12288
Tv 226,254,14336
Mvav 148,72,48
LD 4,40,71,3072
Mvav 148,240,68
s_add_u32 m0,s78,4096
Mvav 148,244,208
Mvav 152,76,32
Mvav 152,72,52
LD 4,40,72,0
Mvav 152,240,72
Mvav 152,244,212
Mvav 156,76,36
Mvav 156,72,56
buffer_load_dwordx2 v[0:1],v7,s[24:27],0 offen offset:128 sc1
Mvav 156,240,76
Mvav 156,244,216
WL 0
Mvav 160,248,20
Mvav 160,252,40
LD 4,40,73,1024
Mvvv 160,220,60
Mvvv 160,224,200
Mvav 164,248,24
Mvav 164,252,44
s_nop 0
Ta 76,253,16384
Ta 78,253,18432
Mvvv 164,220,64
s_nop 0
Ta 72,253,20480
Ta 74,253,22528
Mvvv 164,224,204
s_nop 0
Ta 240,253,24576
Ta 242,253,26624
Mvav 168,248,28
s_nop 0
Ta 244,253,28672
Ta 246,253,30720
Mvav 168,252,48
LD 4,40,74,2048
Mvvv 168,220,68
Mvvv 168,224,208
Mvav 172,248,32
Mvav 172,252,52
LD 4,40,75,3072
Mvvv 172,220,72
s_and_b32 s86,%12,458752
s_cmp_eq_u32 s86,0
s_cselect_b32 s86,s91,0x40000000
Mvvv 172,224,212
s_add_u32 s86,s86,98304
Mvav 176,248,36
Mvav 176,252,56
buffer_load_dword v255,v17,s[92:95],s86 offen
Mvvv 176,220,76
Mvvv 176,224,216
WL 0
Mvaa 140,76,200
Mvaa 140,72,220
Mvaa 140,240,0
Mvaa 140,244,20
Mvaa 144,76,204
Mvaa 144,72,224
s_nop 0
Ta 248,254,16384
Ta 250,254,18432
Mvaa 144,240,4
s_nop 0
Ta 252,254,20480
Ta 254,254,22528
Mvaa 144,244,24
s_nop 0
Tv 220,254,24576
Tv 222,254,26624
Mvaa 148,76,208
s_nop 0
Tv 224,254,28672
Tv 226,254,30720
Mvaa 148,72,228
Mvaa 148,240,8
Mvaa 148,244,28
Mvaa 152,76,212
Mvaa 152,72,232
Mvaa 152,240,12
Mvaa 152,244,32
Mvaa 156,76,216
Mvaa 156,72,236
Mvaa 156,240,16
Mvaa 156,244,36
WL 0
Mvaa 160,248,200
Mvaa 160,252,220
Mvva 160,220,0
Mvva 160,224,20
Mvaa 164,248,204
Mvaa 164,252,224
Mvva 164,220,4
Mvva 164,224,24
CV 140,140,141
CV 141,142,143
Mvaa 168,248,208
s_nop 0
CV 142,160,161
CV 143,162,163
Mvaa 168,252,228
s_waitcnt vmcnt(15)
s_barrier
Mvva 168,220,8
s_add_u32 m0,s78,65536
Ta 76,13,32768
Ta 78,13,34816
Mvva 168,224,28
s_nop 0
Ta 72,13,36864
Ta 74,13,38912
Mvaa 172,248,212
s_nop 0
Ta 240,13,40960
Ta 242,13,43008
Mvaa 172,252,232
s_nop 0
Ta 244,13,45056
Ta 246,13,47104
Mvva 172,220,12
s_cmp_le_u32 s90,5
s_cselect_b64 s[40:41],s[28:29],%6
v_subrev_u32 v4,294784,v4
Mvva 172,224,32
LD 4,40,68,0
Mvaa 176,248,216
CV 144,144,145
CV 145,146,147
Mvaa 176,252,236
CV 146,164,165
CV 147,166,167
Mvva 176,220,16
CV 148,148,149
CV 149,150,151
Mvva 176,224,36
WL 4
LD 4,40,69,1024
Zvav 100,76,120
CV 150,168,169
CV 151,170,171
Mvav 100,72,180
WL 0
CV 152,152,153
CV 153,154,155
Mvaa 100,240,80
CV 154,172,173
CV 155,174,175
Mvaa 100,244,120
LD 4,40,70,2048
Zvav 104,76,124
CV 156,156,157
CV 157,158,159
Mvav 104,72,184
s_nop 0
Ta 248,14,32768
Ta 250,14,34816
Mvaa 104,240,84
s_nop 0
Ta 252,14,36864
Ta 254,14,38912
Mvaa 104,244,124
s_nop 0
Tv 220,14,40960
Tv 222,14,43008
Zvav 108,76,128
s_nop 0
Tv 224,14,45056
Tv 226,14,47104
Mvav 108,72,188
LD 4,40,71,3072
Mvaa 108,240,88
s_add_u32 m0,s78,69632
CV 158,176,177
CV 159,178,179
Mvaa 108,244,128
Zvav 112,76,132
Mvav 112,72,192
LD 4,40,72,0
Mvaa 112,240,92
Mvaa 112,244,132
Zvav 116,76,136
Mvav 116,72,196
LD 4,40,73,1024
Mvaa 116,240,96
Mvaa 116,244,136
s_waitcnt vmcnt(10) lgkmcnt(0)
s_add_u32 s80,%0,2
s_mov_b32 s81,0x40000
1:
v_subrev_u32 v9,s80,v0
v_cmp_eq_u32 s[82:83],%1,v1
v_cmp_ge_u32 vcc,13,v9
s_nop 1
s_and_b64 vcc,vcc,s[82:83]
s_cmp_eq_u64 vcc,-1
s_cbranch_scc1 2f
s_sleep 1
buffer_load_dwordx2 v[0:1],v7,s[24:27],0 offen offset:128 sc1
s_sub_u32 s81,s81,1
s_waitcnt vmcnt(0)
s_cmp_lg_u32 s81,0
s_cbranch_scc1 1b
2:
Zvav 80,248,120
s_add_u32 s30,s48,0
Mvav 80,252,180
LD 4,40,74,2048
Mvva 80,220,80
Mvva 80,224,120
Zvav 84,248,124
Mvav 84,252,184
s_nop 0
Ta 76,13,49152
Ta 78,13,51200
Mvva 84,220,84
s_nop 0
Ta 72,13,53248
Ta 74,13,55296
Mvva 84,224,124
s_nop 0
Ta 240,13,57344
Ta 242,13,59392
Zvav 88,248,128
s_nop 0
Ta 244,13,61440
Ta 246,13,63488
Mvav 88,252,188
LD 4,40,75,3072
Mvva 88,220,88
Mvva 88,224,128
Zvav 92,248,132
Mvav 92,252,192
buffer_load_dwordx4 a[40:43],v5,s[36:39],s30 offen sc1
Mvva 92,220,92
s_add_u32 s30,s48,184320
Mvva 92,224,132
Zvav 96,248,136
Mvav 96,252,196
buffer_load_dwordx4 a[44:47],v5,s[36:39],s30 offen sc1
Mvva 96,220,96
s_add_u32 s30,s48,368640
Mvva 96,224,136
WL 0
Mvaa 100,76,100
Mvaa 100,72,140
buffer_load_dwordx4 a[48:51],v5,s[36:39],s30 offen sc1
Mvaa 100,240,160
s_add_u32 s30,s48,552960
Mvaa 100,244,180
Mvaa 104,76,104
Mvaa 104,72,144
s_nop 0
Ta 248,14,49152
Ta 250,14,51200
Mvaa 104,240,164
s_nop 0
Ta 252,14,53248
Ta 254,14,55296
Mvaa 104,244,184
s_nop 0
Tv 220,14,57344
Tv 222,14,59392
Mvaa 108,76,108
s_nop 0
Tv 224,14,61440
Tv 226,14,63488
Mvaa 108,72,148
buffer_load_dwordx4 a[52:55],v5,s[36:39],s30 offen sc1
Mvaa 108,240,168
s_add_u32 s30,s48,737280
Mvaa 108,244,188
Mvaa 112,76,112
Mvaa 112,72,152
buffer_load_dwordx4 a[56:59],v5,s[36:39],s30 offen sc1
Mvaa 112,240,172
s_add_u32 s30,s48,921600
Mvaa 112,244,192
Mvaa 116,76,116
Mvaa 116,72,156
buffer_load_dwordx4 a[60:63],v5,s[36:39],s30 offen sc1
Mvaa 116,240,176
s_add_u32 s30,s48,1105920
Mvaa 116,244,196
WL 0
Mvaa 80,248,100
Mvaa 80,252,140
buffer_load_dwordx4 a[64:67],v5,s[36:39],s30 offen sc1
Mvva 80,220,160
s_add_u32 s30,s48,1290240
Mvva 80,224,180
Mvaa 84,248,104
Mvaa 84,252,144
buffer_load_dwordx4 a[68:71],v5,s[36:39],s30 offen sc1
Mvva 84,220,164
Mvva 84,224,184
Mvaa 88,248,108
Mvaa 88,252,148
s_waitcnt vmcnt(17)
s_barrier
Mvva 88,220,168
s_add_u32 m0,s78,32768
Ta 76,13,0
Ta 78,13,2048
Mvva 88,224,188
s_nop 0
Ta 72,13,4096
Ta 74,13,6144
Mvaa 92,248,112
s_nop 0
Ta 240,13,8192
Ta 242,13,10240
Mvaa 92,252,152
s_nop 0
Ta 244,13,12288
Ta 246,13,14336
Mvva 92,220,172
v_add_u32 v4,294912,v4
Mvva 92,224,192
LD 4,40,68,0
Mvaa 96,248,116
Mvaa 96,252,156
Mvva 96,220,176
Mvva 96,224,196
WL 4
LD 4,40,69,1024
Mvav 100,76,20
Mvav 100,72,40
WL 0
Mvav 100,240,60
Mvav 100,244,200
LD 4,40,70,2048
Mvav 104,76,24
Mvav 104,72,44
s_nop 0
Ta 248,14,0
Ta 250,14,2048
Mvav 104,240,64
s_nop 0
Ta 252,14,4096
Ta 254,14,6144
Mvav 104,244,204
s_nop 0
Tv 220,14,8192
Tv 222,14,10240
Mvav 108,76,28
s_nop 0
Tv 224,14,12288
Tv 226,14,14336
Mvav 108,72,48
LD 4,40,71,3072
Mvav 108,240,68
s_add_u32 m0,s78,36864
Mvav 108,244,208
Mvav 112,76,32
Mvav 112,72,52
LD 4,40,72,0
Mvav 112,240,72
Mvav 112,244,212
Mvav 116,76,36
Mvav 116,72,56
LD 4,40,73,1024
Mvav 116,240,76
Mvav 116,244,216
WL 0
Mvav 80,248,20
Mvav 80,252,40
LD 4,40,74,2048
Mvvv 80,220,60
Mvvv 80,224,200
Mvav 84,248,24
Mvav 84,252,44
s_nop 0
Ta 76,13,16384
Ta 78,13,18432
Mvvv 84,220,64
s_nop 0
Ta 72,13,20480
Ta 74,13,22528
Mvvv 84,224,204
s_nop 0
Ta 240,13,24576
Ta 242,13,26624
Mvav 88,248,28
s_nop 0
Ta 244,13,28672
Ta 246,13,30720
Mvav 88,252,48
LD 4,40,75,3072
Mvvv 88,220,68
Mvvv 88,224,208
Mvav 92,248,32
Mvav 92,252,52
buffer_store_dwordx4 v[140:143],v5,s[44:47],s79 offen offset:0 sc1
Mvvv 92,220,72
Mvvv 92,224,212
Mvav 96,248,36
Mvav 96,252,56
Mvvv 96,220,76
buffer_store_dwordx4 v[144:147],v5,s[44:47],s79 offen offset:1024 sc1
Mvvv 96,224,216
WL 0
Mvaa 100,76,200
Mvaa 100,72,220
Mvaa 100,240,0
Mvaa 100,244,20
buffer_store_dwordx4 v[148:151],v5,s[44:47],s79 offen offset:2048 sc1
Mvaa 104,76,204
Mvaa 104,72,224
s_nop 0
Ta 248,14,16384
Ta 250,14,18432
Mvaa 104,240,4
s_nop 0
Ta 252,14,20480
Ta 254,14,22528
Mvaa 104,244,24
s_nop 0
Tv 220,14,24576
Tv 222,14,26624
Mvaa 108,76,208
s_nop 0
Tv 224,14,28672
Tv 226,14,30720
Mvaa 108,72,228
buffer_store_dwordx4 v[152:155],v5,s[44:47],s79 offen offset:3072 sc1
Mvaa 108,240,8
Mvaa 108,244,28
Mvaa 112,76,212
Mvaa 112,72,232
Mvaa 112,240,12
buffer_store_dwordx4 v[156:159],v5,s[44:47],s84 offen offset:0 sc1
Mvaa 112,244,32
s_add_u32 s79,s79,20480
s_add_u32 s84,s84,20480
Mvaa 116,76,216
Mvaa 116,72,236
Mvaa 116,240,16
Mvaa 116,244,36
WL 0
Mvaa 80,248,200
Mvaa 80,252,220
Mvva 80,220,0
Mvva 80,224,20
Mvaa 84,248,204
Mvaa 84,252,224
Mvva 84,220,4
Mvva 84,224,24
CV 100,100,101
CV 101,102,103
Mvaa 88,248,208
s_nop 0
CV 102,80,81
CV 103,82,83
Mvaa 88,252,228
s_waitcnt vmcnt(21)
s_barrier
Mvva 88,220,8
s_add_u32 m0,s78,0
Ta 76,253,0
Ta 78,253,2048
Mvva 88,224,28
s_nop 0
Ta 72,253,4096
Ta 74,253,6144
Mvaa 92,248,212
s_nop 0
Ta 240,253,8192
Ta 242,253,10240
Mvaa 92,252,232
s_nop 0
Ta 244,253,12288
Ta 246,253,14336
Mvva 92,220,12
s_cmp_le_u32 s90,6
s_cselect_b64 s[40:41],s[28:29],%6
v_subrev_u32 v4,294784,v4
Mvva 92,224,32
LD 4,40,68,0
Mvaa 96,248,216
CV 104,104,105
CV 105,106,107
Mvaa 96,252,236
CV 106,84,85
CV 107,86,87
Mvva 96,220,16
CV 108,108,109
CV 109,110,111
Mvva 96,224,36
WL 4
LD 4,40,69,1024
Zvav 140,76,120
CV 110,88,89
CV 111,90,91
Mvav 140,72,180
WL 0
CV 112,112,113
CV 113,114,115
Mvaa 140,240,80
CV 114,92,93
CV 115,94,95
Mvaa 140,244,120
LD 4,40,70,2048
Zvav 144,76,124
CV 116,116,117
CV 117,118,119
Mvav 144,72,184
s_nop 0
Ta 248,254,0
Ta 250,254,2048
Mvaa 144,240,84
s_nop 0
Ta 252,254,4096
Ta 254,254,6144
Mvaa 144,244,124
s_nop 0
Tv 220,254,8192
Tv 222,254,10240
Zvav 148,76,128
s_nop 0
Tv 224,254,12288
Tv 226,254,14336
Mvav 148,72,188
LD 4,40,71,3072
Mvaa 148,240,88
s_add_u32 m0,s78,4096
CV 118,96,97
CV 119,98,99
Mvaa 148,244,128
Zvav 152,76,132
Mvav 152,72,192
LD 4,40,72,0
Mvaa 152,240,92
Mvaa 152,244,132
Zvav 156,76,136
Mvav 156,72,196
LD 4,40,73,1024
Mvaa 156,240,96
Mvaa 156,244,136
WL 0
Zvav 160,248,120
Mvav 160,252,180
LD 4,40,74,2048
Mvva 160,220,80
Mvva 160,224,120
Zvav 164,248,124
Mvav 164,252,184
s_nop 0
Ta 76,253,16384
Ta 78,253,18432
Mvva 164,220,84
s_nop 0
Ta 72,253,20480
Ta 74,253,22528
Mvva 164,224,124
s_nop 0
Ta 240,253,24576
Ta 242,253,26624
Zvav 168,248,128
s_nop 0
Ta 244,253,28672
Ta 246,253,30720
Mvav 168,252,188
LD 4,40,75,3072
Mvva 168,220,88
Mvva 168,224,128
Zvav 172,248,132
Mvav 172,252,192
buffer_store_dwordx4 v[100:103],v5,s[44:47],s79 offen offset:0 sc1
Mvva 172,220,92
Mvva 172,224,132
Zvav 176,248,136
Mvav 176,252,196
Mvva 176,220,96
buffer_store_dwordx4 v[104:107],v5,s[44:47],s79 offen offset:1024 sc1
Mvva 176,224,136
WL 0
Mvaa 140,76,100
Mvaa 140,72,140
Mvaa 140,240,160
Mvaa 140,244,180
buffer_store_dwordx4 v[108:111],v5,s[44:47],s79 offen offset:2048 sc1
Mvaa 144,76,104
Mvaa 144,72,144
s_nop 0
Ta 248,254,16384
Ta 250,254,18432
Mvaa 144,240,164
s_nop 0
Ta 252,254,20480
Ta 254,254,22528
Mvaa 144,244,184
s_nop 0
Tv 220,254,24576
Tv 222,254,26624
Mvaa 148,76,108
s_nop 0
Tv 224,254,28672
Tv 226,254,30720
Mvaa 148,72,148
buffer_store_dwordx4 v[112:115],v5,s[44:47],s79 offen offset:3072 sc1
Mvaa 148,240,168
Mvaa 148,244,188
Mvaa 152,76,112
Mvaa 152,72,152
Mvaa 152,240,172
buffer_store_dwordx4 v[116:119],v5,s[44:47],s84 offen offset:0 sc1
Mvaa 152,244,192
s_add_u32 s79,s79,20480
s_add_u32 s84,s84,20480
Mvaa 156,76,116
Mvaa 156,72,156
Mvaa 156,240,176
Mvaa 156,244,196
WL 0
Mvaa 160,248,100
Mvaa 160,252,140
Mvva 160,220,160
Mvva 160,224,180
Mvaa 164,248,104
Mvaa 164,252,144
Mvva 164,220,164
Mvva 164,224,184
Mvaa 168,248,108
Mvaa 168,252,148
s_waitcnt vmcnt(18)
s_barrier
Mvva 168,220,168
s_add_u32 m0,s78,65536
v_add_u32 v4,294912,v4
Mvva 168,224,188
LD 4,40,68,0
Mvaa 172,248,112
Mvaa 172,252,152
Mvva 172,220,172
Mvva 172,224,192
LD 4,40,69,1024
Mvaa 176,248,116
Mvaa 176,252,156
Mvva 176,220,176
Mvva 176,224,196
LD 4,40,70,2048
Zvva 236,230,40
Zvva 240,228,40
s_nop 0
Ta 76,13,32768
Ta 78,13,34816
Mvva 236,230,44
s_nop 0
Ta 72,13,36864
Ta 74,13,38912
Mvva 240,228,44
s_nop 0
Ta 240,13,40960
Ta 242,13,43008
Mvva 236,230,48
s_nop 0
Ta 244,13,45056
Ta 246,13,47104
Mvva 240,228,48
LD 4,40,71,3072
Mvva 236,230,52
s_add_u32 m0,s78,69632
Mvva 240,228,52
Mvva 236,230,56
Mvva 240,228,56
LD 4,40,72,0
Mvva 236,230,60
Mvva 240,228,60
Mvva 236,230,64
Mvva 240,228,64
LD 4,40,73,1024
Mvva 236,230,68
Mvva 240,228,68
WL 0
Mvav 140,76,20
Mvav 140,72,40
LD 4,40,74,2048
Mvav 140,240,60
Mvav 140,244,200
s_nop 0
v_cmp_class_f32 vcc,v236,s67
s_nop 1
s_cmp_eq_u64 vcc,0
s_cbranch_scc1 3f
s_lshl_b32 s80,s60,2
s_add_u32 s80,s80,%9
s_add_u32 s80,s80,159744
v_bfe_u32 v9,v5,4,4
v_lshl_add_u32 v9,v9,2,s80
v_mov_b32 v0,1
s_mov_b64 exec,vcc
ds_write_b32 v9,v0
s_add_u32 s80,%9,160384
v_mov_b32 v9,s80
ds_write_b32 v9,v0
s_mov_b64 exec,-1
s_waitcnt lgkmcnt(0)
s_nop 4
3:
Mvav 144,76,24
CV 236,236,237
CV 237,238,239
Mvav 144,72,44
s_nop 0
Ta 248,14,32768
Ta 250,14,34816
Mvav 144,240,64
s_nop 0
Ta 252,14,36864
Ta 254,14,38912
Mvav 144,244,204
s_nop 0
Tv 220,14,40960
Tv 222,14,43008
Mvav 148,76,28
s_nop 0
Tv 224,14,45056
Tv 226,14,47104
Mvav 148,72,48
LD 4,40,75,3072
Mvav 148,240,68
s_and_b32 s86,%12,458752
s_cmp_eq_u32 s86,65536
s_cselect_b32 s86,s91,0x40000000
Mvav 148,244,208
s_add_u32 s86,s86,102400
CV 238,240,241
CV 239,242,243
Mvav 152,76,32
v_permlane32_swap_b32 v236,v238
Mvav 152,72,52
buffer_load_dword v255,v17,s[92:95],s86 offen
Mvav 152,240,72
v_permlane32_swap_b32 v237,v239
Mvav 152,244,212
v_permlane16_swap_b32 v236,v238
Mvav 156,76,36
v_permlane16_swap_b32 v237,v239
Mvav 156,72,56
Mvav 156,240,76
Mvav 156,244,216
WL 0
Mvav 160,248,20
Mvav 160,252,40
buffer_store_dwordx4 v[236:239],v6,s[96:99],s54 offen
Mvvv 160,220,60
Mvvv 160,224,200
Mvav 164,248,24
Mvav 164,252,44
s_nop 0
Ta 76,13,49152
Ta 78,13,51200
Mvvv 164,220,64
s_nop 0
Ta 72,13,53248
Ta 74,13,55296
Mvvv 164,224,204
s_nop 0
Ta 240,13,57344
Ta 242,13,59392
Mvav 168,248,28
s_nop 0
Ta 244,13,61440
)ASM"
R"ASM(Ta 246,13,63488
Mvav 168,252,48
Mvvv 168,220,68
Mvvv 168,224,208
Mvav 172,248,32
Mvav 172,252,52
Mvvv 172,220,72
Mvvv 172,224,212
Mvav 176,248,36
Mvav 176,252,56
Mvvv 176,220,76
Mvvv 176,224,216
WL 0
Mvaa 140,76,200
Mvaa 140,72,220
Mvaa 140,240,0
Mvaa 140,244,20
Mvaa 144,76,204
Mvaa 144,72,224
s_nop 0
Ta 248,14,49152
Ta 250,14,51200
Mvaa 144,240,4
s_nop 0
Ta 252,14,53248
Ta 254,14,55296
Mvaa 144,244,24
s_nop 0
Tv 220,14,57344
Tv 222,14,59392
Mvaa 148,76,208
s_nop 0
Tv 224,14,61440
Tv 226,14,63488
Mvaa 148,72,228
Mvaa 148,240,8
Mvaa 148,244,28
Mvaa 152,76,212
Mvaa 152,72,232
Mvaa 152,240,12
Mvaa 152,244,32
Mvaa 156,76,216
Mvaa 156,72,236
Mvaa 156,240,16
Mvaa 156,244,36
WL 0
Mvaa 160,248,200
Mvaa 160,252,220
Mvva 160,220,0
Mvva 160,224,20
Mvaa 164,248,204
Mvaa 164,252,224
Mvva 164,220,4
Mvva 164,224,24
CV 140,140,141
CV 141,142,143
Mvaa 168,248,208
s_nop 0
CV 142,160,161
CV 143,162,163
Mvaa 168,252,228
s_waitcnt vmcnt(0)
s_barrier
Mvva 168,220,8
s_add_u32 m0,s78,32768
Ta 76,13,0
Ta 78,13,2048
Mvva 168,224,28
v_mov_b32 v2,%0
v_add_u32 v2,5,v2
Mvaa 172,248,212
buffer_store_dwordx2 v[2:3],v8,s[24:27],s66 offen sc1
Mvaa 172,252,232
s_nop 0
Ta 72,13,4096
Ta 74,13,6144
Mvva 172,220,12
s_nop 0
Ta 240,13,8192
Ta 242,13,10240
Mvva 172,224,32
s_nop 0
Ta 244,13,12288
Ta 246,13,14336
Mvaa 176,248,216
s_cmp_le_u32 s90,7
s_cselect_b64 s[40:41],s[28:29],%6
v_subrev_u32 v4,294784,v4
Mvaa 176,252,236
LD 4,40,68,0
Mvva 176,220,16
CV 144,144,145
CV 145,146,147
Mvva 176,224,36
WL 6
CV 146,164,165
CV 147,166,167
Zvav 100,76,120
WL 4
CV 148,148,149
CV 149,150,151
Mvav 100,72,180
WL 2
LD 4,40,69,1024
Mvaa 100,240,80
WL 0
CV 150,168,169
CV 151,170,171
Mvaa 100,244,120
CV 152,152,153
CV 153,154,155
Zvav 104,76,124
CV 154,172,173
CV 155,174,175
Mvav 104,72,184
s_nop 0
Ta 248,14,0
Ta 250,14,2048
Mvaa 104,240,84
s_nop 0
Ta 252,14,4096
Ta 254,14,6144
Mvaa 104,244,124
s_nop 0
Tv 220,14,8192
Tv 222,14,10240
Zvav 108,76,128
s_nop 0
Tv 224,14,12288
Tv 226,14,14336
Mvav 108,72,188
LD 4,40,70,2048
Mvaa 108,240,88
CV 156,156,157
CV 157,158,159
Mvaa 108,244,128
CV 158,176,177
CV 159,178,179
Zvav 112,76,132
Mvav 112,72,192
LD 4,40,71,3072
Mvaa 112,240,92
s_add_u32 m0,s78,36864
Mvaa 112,244,132
Zvav 116,76,136
Mvav 116,72,196
LD 4,40,72,0
Mvaa 116,240,96
Mvaa 116,244,136
WL 0
Zvav 80,248,120
Mvav 80,252,180
LD 4,40,73,1024
Mvva 80,220,80
Mvva 80,224,120
Zvav 84,248,124
Mvav 84,252,184
s_nop 0
Ta 76,13,16384
Ta 78,13,18432
Mvva 84,220,84
s_nop 0
Ta 72,13,20480
Ta 74,13,22528
Mvva 84,224,124
s_nop 0
Ta 240,13,24576
Ta 242,13,26624
Zvav 88,248,128
s_nop 0
Ta 244,13,28672
Ta 246,13,30720
Mvav 88,252,188
LD 4,40,74,2048
Mvva 88,220,88
Mvva 88,224,128
Zvav 92,248,132
Mvav 92,252,192
LD 4,40,75,3072
Mvva 92,220,92
Mvva 92,224,132
Zvav 96,248,136
Mvav 96,252,196
buffer_store_dwordx4 v[140:143],v5,s[44:47],s79 offen offset:0 sc1
Mvva 96,220,96
Mvva 96,224,136
WL 0
Mvaa 100,76,100
Mvaa 100,72,140
Mvaa 100,240,160
buffer_store_dwordx4 v[144:147],v5,s[44:47],s79 offen offset:1024 sc1
Mvaa 100,244,180
Mvaa 104,76,104
Mvaa 104,72,144
s_nop 0
Ta 248,14,16384
Ta 250,14,18432
Mvaa 104,240,164
s_nop 0
Ta 252,14,20480
Ta 254,14,22528
Mvaa 104,244,184
s_nop 0
Tv 220,14,24576
Tv 222,14,26624
Mvaa 108,76,108
s_nop 0
Tv 224,14,28672
Tv 226,14,30720
Mvaa 108,72,148
buffer_store_dwordx4 v[148:151],v5,s[44:47],s79 offen offset:2048 sc1
Mvaa 108,240,168
Mvaa 108,244,188
Mvaa 112,76,112
Mvaa 112,72,152
Mvaa 112,240,172
buffer_store_dwordx4 v[152:155],v5,s[44:47],s79 offen offset:3072 sc1
Mvaa 112,244,192
Mvaa 116,76,116
Mvaa 116,72,156
Mvaa 116,240,176
Mvaa 116,244,196
WL 0
buffer_store_dwordx4 v[156:159],v5,s[44:47],s84 offen offset:0 sc1
Mvaa 80,248,100
s_add_u32 s79,s79,20480
s_add_u32 s84,s84,20480
Mvaa 80,252,140
Mvva 80,220,160
Mvva 80,224,180
Mvaa 84,248,104
Mvaa 84,252,144
Mvva 84,220,164
Mvva 84,224,184
Mvaa 88,248,108
Mvaa 88,252,148
s_barrier
Mvva 88,220,168
s_add_u32 m0,s78,0
Ta 76,253,0
Ta 78,253,2048
Mvva 88,224,188
s_nop 0
Ta 72,253,4096
Ta 74,253,6144
Mvaa 92,248,112
s_nop 0
Ta 240,253,8192
Ta 242,253,10240
Mvaa 92,252,152
s_nop 0
Ta 244,253,12288
Ta 246,253,14336
Mvva 92,220,172
v_add_u32 v4,294912,v4
Mvva 92,224,192
LD 4,40,68,0
Mvaa 96,248,116
Mvaa 96,252,156
Mvva 96,220,176
Mvva 96,224,196
WL 4
LD 4,40,69,1024
Mvav 100,76,20
Mvav 100,72,40
WL 0
Mvav 100,240,60
Mvav 100,244,200
LD 4,40,70,2048
Mvav 104,76,24
Mvav 104,72,44
s_nop 0
Ta 248,254,0
Ta 250,254,2048
Mvav 104,240,64
s_nop 0
Ta 252,254,4096
Ta 254,254,6144
Mvav 104,244,204
s_nop 0
Tv 220,254,8192
Tv 222,254,10240
Mvav 108,76,28
s_nop 0
Tv 224,254,12288
Tv 226,254,14336
Mvav 108,72,48
LD 4,40,71,3072
Mvav 108,240,68
s_add_u32 m0,s78,4096
Mvav 108,244,208
Mvav 112,76,32
Mvav 112,72,52
LD 4,40,72,0
Mvav 112,240,72
Mvav 112,244,212
Mvav 116,76,36
Mvav 116,72,56
buffer_load_dwordx2 v[0:1],v7,s[24:27],0 offen offset:128 sc1
Mvav 116,240,76
Mvav 116,244,216
WL 0
Mvav 80,248,20
Mvav 80,252,40
LD 4,40,73,1024
Mvvv 80,220,60
Mvvv 80,224,200
Mvav 84,248,24
Mvav 84,252,44
s_nop 0
Ta 76,253,16384
Ta 78,253,18432
Mvvv 84,220,64
s_nop 0
Ta 72,253,20480
Ta 74,253,22528
Mvvv 84,224,204
s_nop 0
Ta 240,253,24576
Ta 242,253,26624
Mvav 88,248,28
s_nop 0
Ta 244,253,28672
Ta 246,253,30720
Mvav 88,252,48
LD 4,40,74,2048
Mvvv 88,220,68
Mvvv 88,224,208
Mvav 92,248,32
Mvav 92,252,52
LD 4,40,75,3072
Mvvv 92,220,72
Mvvv 92,224,212
Mvav 96,248,36
Mvav 96,252,56
Mvvv 96,220,76
Mvvv 96,224,216
WL 0
Mvaa 100,76,200
Mvaa 100,72,220
Mvaa 100,240,0
Mvaa 100,244,20
Mvaa 104,76,204
Mvaa 104,72,224
s_nop 0
Ta 248,254,16384
Ta 250,254,18432
Mvaa 104,240,4
s_nop 0
Ta 252,254,20480
Ta 254,254,22528
Mvaa 104,244,24
s_nop 0
Tv 220,254,24576
Tv 222,254,26624
Mvaa 108,76,208
s_nop 0
Tv 224,254,28672
Tv 226,254,30720
Mvaa 108,72,228
Mvaa 108,240,8
Mvaa 108,244,28
Mvaa 112,76,212
Mvaa 112,72,232
Mvaa 112,240,12
Mvaa 112,244,32
Mvaa 116,76,216
Mvaa 116,72,236
Mvaa 116,240,16
Mvaa 116,244,36
WL 0
Mvaa 80,248,200
Mvaa 80,252,220
Mvva 80,220,0
Mvva 80,224,20
Mvaa 84,248,204
Mvaa 84,252,224
Mvva 84,220,4
Mvva 84,224,24
CV 100,100,101
CV 101,102,103
Mvaa 88,248,208
s_nop 0
CV 102,80,81
CV 103,82,83
Mvaa 88,252,228
s_waitcnt vmcnt(14)
s_barrier
Mvva 88,220,8
s_add_u32 m0,s78,65536
Ta 76,13,32768
Ta 78,13,34816
Mvva 88,224,28
s_nop 0
Ta 72,13,36864
Ta 74,13,38912
Mvaa 92,248,212
s_nop 0
Ta 240,13,40960
Ta 242,13,43008
Mvaa 92,252,232
s_nop 0
Ta 244,13,45056
Ta 246,13,47104
Mvva 92,220,12
s_cmp_le_u32 s90,8
s_cselect_b64 s[40:41],s[28:29],%6
v_subrev_u32 v4,294784,v4
Mvva 92,224,32
LD 4,40,68,0
Mvaa 96,248,216
CV 104,104,105
CV 105,106,107
Mvaa 96,252,236
CV 106,84,85
CV 107,86,87
Mvva 96,220,16
CV 108,108,109
CV 109,110,111
Mvva 96,224,36
WL 4
LD 4,40,69,1024
Zvav 140,76,120
CV 110,88,89
CV 111,90,91
Mvav 140,72,180
WL 0
CV 112,112,113
CV 113,114,115
Mvaa 140,240,80
CV 114,92,93
CV 115,94,95
Mvaa 140,244,120
LD 4,40,70,2048
Zvav 144,76,124
CV 116,116,117
CV 117,118,119
Mvav 144,72,184
s_nop 0
Ta 248,14,32768
Ta 250,14,34816
Mvaa 144,240,84
s_nop 0
Ta 252,14,36864
Ta 254,14,38912
Mvaa 144,244,124
s_nop 0
Tv 220,14,40960
Tv 222,14,43008
Zvav 148,76,128
s_nop 0
Tv 224,14,45056
Tv 226,14,47104
Mvav 148,72,188
s_waitcnt vmcnt(6)
s_add_u32 s80,%0,5
s_mov_b32 s81,0x40000
1:
v_subrev_u32 v9,s80,v0
v_cmp_eq_u32 s[82:83],%1,v1
v_cmp_ge_u32 vcc,10,v9
s_nop 1
s_and_b64 vcc,vcc,s[82:83]
s_cmp_eq_u64 vcc,-1
s_cbranch_scc1 2f
s_sleep 1
buffer_load_dwordx2 v[0:1],v7,s[24:27],0 offen offset:128 sc1
s_sub_u32 s81,s81,1
s_waitcnt vmcnt(0)
s_cmp_lg_u32 s81,0
s_cbranch_scc1 1b
2:
Mvaa 148,240,88
LD 4,40,71,3072
Mvaa 148,244,128
s_add_u32 m0,s78,69632
s_add_u32 s30,s49,0
CV 118,96,97
Zvav 152,76,132
CV 119,98,99
Mvav 152,72,192
Mvaa 152,240,92
LD 4,40,72,0
Mvaa 152,244,132
Zvav 156,76,136
Mvav 156,72,196
Mvaa 156,240,96
LD 4,40,73,1024
Mvaa 156,244,136
WL 0
Zvav 160,248,120
Mvav 160,252,180
Mvva 160,220,80
LD 4,40,74,2048
Mvva 160,224,120
Zvav 164,248,124
Mvav 164,252,184
s_nop 0
Ta 76,13,49152
Ta 78,13,51200
Mvva 164,220,84
s_nop 0
Ta 72,13,53248
Ta 74,13,55296
Mvva 164,224,124
s_nop 0
Ta 240,13,57344
Ta 242,13,59392
Zvav 168,248,128
s_nop 0
Ta 244,13,61440
Ta 246,13,63488
Mvav 168,252,188
LD 4,40,75,3072
Mvva 168,220,88
s_and_b32 s86,%12,458752
s_cmp_eq_u32 s86,131072
s_cselect_b32 s86,s91,0x40000000
Mvva 168,224,128
s_add_u32 s86,s86,106496
Zvav 172,248,132
Mvav 172,252,192
buffer_load_dword v255,v17,s[92:95],s86 offen
Mvva 172,220,92
Mvva 172,224,132
Zvav 176,248,136
Mvav 176,252,196
Mvva 176,220,96
Mvva 176,224,136
WL 0
Mvaa 140,76,100
Mvaa 140,72,140
buffer_load_dwordx4 a[40:43],v5,s[36:39],s30 offen sc1
Mvaa 140,240,160
s_add_u32 s30,s49,184320
Mvaa 140,244,180
Mvaa 144,76,104
Mvaa 144,72,144
s_nop 0
Ta 248,14,49152
Ta 250,14,51200
Mvaa 144,240,164
s_nop 0
Ta 252,14,53248
Ta 254,14,55296
Mvaa 144,244,184
s_nop 0
Tv 220,14,57344
Tv 222,14,59392
Mvaa 148,76,108
s_nop 0
Tv 224,14,61440
Tv 226,14,63488
Mvaa 148,72,148
buffer_load_dwordx4 a[44:47],v5,s[36:39],s30 offen sc1
Mvaa 148,240,168
s_add_u32 s30,s49,368640
Mvaa 148,244,188
Mvaa 152,76,112
Mvaa 152,72,152
buffer_load_dwordx4 a[48:51],v5,s[36:39],s30 offen sc1
Mvaa 152,240,172
s_add_u32 s30,s49,552960
Mvaa 152,244,192
Mvaa 156,76,116
Mvaa 156,72,156
buffer_load_dwordx4 a[52:55],v5,s[36:39],s30 offen sc1
Mvaa 156,240,176
s_add_u32 s30,s49,737280
Mvaa 156,244,196
WL 0
Mvaa 160,248,100
Mvaa 160,252,140
buffer_load_dwordx4 a[56:59],v5,s[36:39],s30 offen sc1
Mvva 160,220,160
s_add_u32 s30,s49,921600
Mvva 160,224,180
Mvaa 164,248,104
Mvaa 164,252,144
buffer_load_dwordx4 a[60:63],v5,s[36:39],s30 offen sc1
Mvva 164,220,164
s_add_u32 s30,s49,1105920
Mvva 164,224,184
Mvaa 168,248,108
Mvaa 168,252,148
s_waitcnt vmcnt(15)
s_barrier
Mvva 168,220,168
s_add_u32 m0,s78,32768
Ta 76,13,0
Ta 78,13,2048
Mvva 168,224,188
s_nop 0
Ta 72,13,4096
Ta 74,13,6144
Mvaa 172,248,112
s_nop 0
Ta 240,13,8192
Ta 242,13,10240
Mvaa 172,252,152
buffer_store_dwordx4 v[100:103],v5,s[44:47],s79 offen offset:0 sc1
buffer_store_dwordx4 v[104:107],v5,s[44:47],s79 offen offset:1024 sc1
buffer_store_dwordx4 v[108:111],v5,s[44:47],s79 offen offset:2048 sc1
buffer_store_dwordx4 v[112:115],v5,s[44:47],s79 offen offset:3072 sc1
buffer_store_dwordx4 v[116:119],v5,s[44:47],s84 offen offset:0 sc1
s_add_u32 s79,s79,20480
s_add_u32 s84,s84,20480
Mvva 172,220,172
s_nop 0
Ta 244,13,12288
Ta 246,13,14336
Mvva 172,224,192
v_add_u32 v4,294912,v4
Mvaa 176,248,116
Mvaa 176,252,156
Mvva 176,220,176
Mvva 176,224,196
WL 4
Mvav 140,76,20
Mvav 140,72,40
WL 2
Mvav 140,240,60
WL 0
Mvav 140,244,200
Mvav 144,76,24
Mvav 144,72,44
s_nop 0
Ta 248,14,0
Ta 250,14,2048
Mvav 144,240,64
s_nop 0
Ta 252,14,4096
Ta 254,14,6144
Mvav 144,244,204
s_nop 0
Tv 220,14,8192
Tv 222,14,10240
Mvav 148,76,28
s_nop 0
Tv 224,14,12288
Tv 226,14,14336
Mvav 148,72,48
buffer_load_dwordx4 a[64:67],v5,s[36:39],s30 offen sc1
s_add_u32 s30,s49,1290240
buffer_load_dwordx4 a[68:71],v5,s[36:39],s30 offen sc1
Mvav 148,240,68
Mvav 148,244,208
Mvav 152,76,32
Mvav 152,72,52
Mvav 152,240,72
Mvav 152,244,212
Mvav 156,76,36
Mvav 156,72,56
Mvav 156,240,76
Mvav 156,244,216
WL 0
Mvav 160,248,20
Mvav 160,252,40
LD 4,40,68,0
Mvvv 160,220,60
Mvvv 160,224,200
Mvav 164,248,24
Mvav 164,252,44
s_nop 0
Ta 76,13,16384
Ta 78,13,18432
Mvvv 164,220,64
s_nop 0
Ta 72,13,20480
Ta 74,13,22528
Mvvv 164,224,204
s_nop 0
Ta 240,13,24576
Ta 242,13,26624
Mvav 168,248,28
s_nop 0
Ta 244,13,28672
Ta 246,13,30720
Mvav 168,252,48
LD 4,40,69,1024
Mvvv 168,220,68
Mvvv 168,224,208
Mvav 172,248,32
Mvav 172,252,52
LD 4,40,70,2048
Mvvv 172,220,72
Mvvv 172,224,212
Mvav 176,248,36
Mvav 176,252,56
LD 4,40,71,3072
Mvvv 176,220,76
s_add_u32 m0,s78,36864
Mvvv 176,224,216
WL 0
Mvaa 140,76,200
Mvaa 140,72,220
LD 4,40,72,0
Mvaa 140,240,0
Mvaa 140,244,20
Mvaa 144,76,204
Mvaa 144,72,224
s_nop 0
Ta 248,14,16384
Ta 250,14,18432
Mvaa 144,240,4
s_nop 0
Ta 252,14,20480
Ta 254,14,22528
Mvaa 144,244,24
s_nop 0
Tv 220,14,24576
Tv 222,14,26624
Mvaa 148,76,208
s_nop 0
Tv 224,14,28672
Tv 226,14,30720
Mvaa 148,72,228
LD 4,40,73,1024
Mvaa 148,240,8
Mvaa 148,244,28
Mvaa 152,76,212
Mvaa 152,72,232
LD 4,40,74,2048
Mvaa 152,240,12
Mvaa 152,244,32
Mvaa 156,76,216
Mvaa 156,72,236
LD 4,40,75,3072
Mvaa 156,240,16
Mvaa 156,244,36
WL 0
Mvaa 160,248,200
Mvaa 160,252,220
Mvva 160,220,0
Mvva 160,224,20
Mvaa 164,248,204
Mvaa 164,252,224
Mvva 164,220,4
Mvva 164,224,24
CV 140,140,141
CV 141,142,143
Mvaa 168,248,208
s_nop 0
CV 142,160,161
CV 143,162,163
Mvaa 168,252,228
s_waitcnt vmcnt(22)
s_barrier
Mvva 168,220,8
s_nop 0
Ta 76,253,0
Ta 78,253,2048
Mvva 168,224,28
s_nop 0
Ta 72,253,4096
Ta 74,253,6144
Mvaa 172,248,212
s_nop 0
Ta 240,253,8192
Ta 242,253,10240
Mvaa 172,252,232
s_nop 0
Ta 244,253,12288
Ta 246,253,14336
Mvva 172,220,12
buffer_store_dwordx4 v[140:143],v5,s[44:47],s79 offen offset:0 sc1
Mvva 172,224,32
CV 144,144,145
CV 145,146,147
Mvaa 176,248,216
CV 146,164,165
CV 147,166,167
Mvaa 176,252,236
CV 148,148,149
CV 149,150,151
Mvva 176,220,16
CV 150,168,169
CV 151,170,171
Mvva 176,224,36
WL 4
buffer_store_dwordx4 v[144:147],v5,s[44:47],s79 offen offset:1024 sc1
Zvav 100,76,120
CV 152,152,153
CV 153,154,155
Mvav 100,72,180
WL 0
CV 154,172,173
CV 155,174,175
Mvaa 100,240,80
Mvaa 100,244,120
CV 156,156,157
CV 157,158,159
Zvav 104,76,124
buffer_store_dwordx4 v[148:151],v5,s[44:47],s79 offen offset:2048 sc1
Mvav 104,72,184
s_nop 0
Ta 248,254,0
Ta 250,254,2048
Mvaa 104,240,84
s_nop 0
Ta 252,254,4096
Ta 254,254,6144
Mvaa 104,244,124
s_nop 0
Tv 220,254,8192
Tv 222,254,10240
Zvav 108,76,128
s_nop 0
Tv 224,254,12288
Tv 226,254,14336
Mvav 108,72,188
buffer_store_dwordx4 v[152:155],v5,s[44:47],s79 offen offset:3072 sc1
Mvaa 108,240,88
CV 158,176,177
CV 159,178,179
Mvaa 108,244,128
Zvav 112,76,132
Mvav 112,72,192
Mvaa 112,240,92
buffer_store_dwordx4 v[156:159],v5,s[44:47],s84 offen offset:0 sc1
Mvaa 112,244,132
s_add_u32 s79,s79,20480
s_add_u32 s84,s84,20480
Zvav 116,76,136
Mvav 116,72,196
Mvaa 116,240,96
Mvaa 116,244,136
WL 0
Zvav 80,248,120
Mvav 80,252,180
Mvva 80,220,80
Mvva 80,224,120
Zvav 84,248,124
Mvav 84,252,184
Mvva 84,220,84
Mvva 84,224,124
Zvav 88,248,128
Mvav 88,252,188
Mvva 88,220,88
Mvva 88,224,128
Zvav 92,248,132
Mvav 92,252,192
Mvva 92,220,92
Mvva 92,224,132
Zvav 96,248,136
Mvav 96,252,196
Mvva 96,220,96
Mvva 96,224,136
s_waitcnt vmcnt(25)
Zvva 236,230,40
Zvva 240,228,40
s_nop 0
Ta 76,253,16384
Ta 78,253,18432
s_waitcnt vmcnt(24)
Mvva 236,230,44
s_nop 0
Ta 72,253,20480
Ta 74,253,22528
Mvva 240,228,44
s_nop 0
Ta 240,253,24576
Ta 242,253,26624
s_waitcnt vmcnt(23)
Mvva 236,230,48
s_nop 0
Ta 244,253,28672
Ta 246,253,30720
Mvva 240,228,48
s_waitcnt vmcnt(22)
Mvva 236,230,52
Mvva 240,228,52
s_waitcnt vmcnt(21)
Mvva 236,230,56
Mvva 240,228,56
s_waitcnt vmcnt(20)
Mvva 236,230,60
Mvva 240,228,60
s_waitcnt vmcnt(14)
Mvva 236,230,64
Mvva 240,228,64
s_waitcnt vmcnt(13)
Mvva 236,230,68
Mvva 240,228,68
WL 0
s_add_u32 s30,s50,0
Mvaa 100,76,100
buffer_load_dwordx4 a[40:43],v5,s[36:39],s30 offen sc1
Mvaa 100,72,140
s_add_u32 s30,s50,184320
Mvaa 100,240,160
Mvaa 100,244,180
v_cmp_class_f32 vcc,v236,s67
s_nop 1
s_cmp_eq_u64 vcc,0
s_cbranch_scc1 3f
s_lshl_b32 s80,s61,2
s_add_u32 s80,s80,%9
s_add_u32 s80,s80,159744
v_bfe_u32 v9,v5,4,4
v_lshl_add_u32 v9,v9,2,s80
v_mov_b32 v0,1
s_mov_b64 exec,vcc
ds_write_b32 v9,v0
s_add_u32 s80,%9,160384
v_mov_b32 v9,s80
ds_write_b32 v9,v0
s_mov_b64 exec,-1
s_waitcnt lgkmcnt(0)
s_nop 4
3:
Mvaa 104,76,104
buffer_load_dwordx4 a[44:47],v5,s[36:39],s30 offen sc1
Mvaa 104,72,144
s_add_u32 s30,s50,368640
Ta 248,254,16384
Ta 250,254,18432
Mvaa 104,240,164
s_nop 0
Ta 252,254,20480
Ta 254,254,22528
Mvaa 104,244,184
s_nop 0
Tv 220,254,24576
Tv 222,254,26624
Mvaa 108,76,108
s_nop 0
Tv 224,254,28672
Tv 226,254,30720
Mvaa 108,72,148
buffer_load_dwordx4 a[48:51],v5,s[36:39],s30 offen sc1
Mvaa 108,240,168
s_add_u32 s30,s50,552960
CV 236,236,237
CV 237,238,239
Mvaa 108,244,188
CV 238,240,241
CV 239,242,243
Mvaa 112,76,112
v_permlane32_swap_b32 v236,v238
Mvaa 112,72,152
buffer_load_dwordx4 a[52:55],v5,s[36:39],s30 offen sc1
Mvaa 112,240,172
s_add_u32 s30,s50,737280
v_permlane32_swap_b32 v237,v239
Mvaa 112,244,192
v_permlane16_swap_b32 v236,v238
Mvaa 116,76,116
v_permlane16_swap_b32 v237,v239
Mvaa 116,72,156
buffer_load_dwordx4 a[56:59],v5,s[36:39],s30 offen sc1
Mvaa 116,240,176
s_add_u32 s30,s50,921600
Mvaa 116,244,196
WL 0
Mvaa 80,248,100
Mvaa 80,252,140
buffer_load_dwordx4 a[60:63],v5,s[36:39],s30 offen sc1
Mvva 80,220,160
s_add_u32 s30,s50,1105920
Mvva 80,224,180
Mvaa 84,248,104
Mvaa 84,252,144
buffer_load_dwordx4 a[64:67],v5,s[36:39],s30 offen sc1
Mvva 84,220,164
s_add_u32 s30,s50,1290240
Mvva 84,224,184
Mvaa 88,248,108
Mvaa 88,252,148
s_waitcnt vmcnt(12)
s_barrier
Mvva 88,220,168
s_nop 0
Ta 76,13,32768
Ta 78,13,34816
Mvva 88,224,188
s_nop 0
Ta 72,13,36864
Ta 74,13,38912
Mvaa 92,248,112
s_nop 0
Ta 240,13,40960
Ta 242,13,43008
Mvaa 92,252,152
s_nop 0
Ta 244,13,45056
Ta 246,13,47104
Mvva 92,220,172
buffer_load_dwordx4 a[68:71],v5,s[36:39],s30 offen sc1
Mvva 92,224,192
Mvaa 96,248,116
Mvaa 96,252,156
Mvva 96,220,176
buffer_store_dwordx4 v[236:239],v6,s[96:99],s55 offen
Mvva 96,224,196
WL 4
Mvav 100,76,20
Mvav 100,72,40
WL 0
Mvav 100,240,60
Mvav 100,244,200
Mvav 104,76,24
Mvav 104,72,44
s_nop 0
Ta 248,14,32768
Ta 250,14,34816
Mvav 104,240,64
s_nop 0
Ta 252,14,36864
Ta 254,14,38912
Mvav 104,244,204
s_nop 0
Tv 220,14,40960
Tv 222,14,43008
Mvav 108,76,28
s_nop 0
Tv 224,14,45056
Tv 226,14,47104
Mvav 108,72,48
Mvav 108,240,68
Mvav 108,244,208
Mvav 112,76,32
Mvav 112,72,52
Mvav 112,240,72
Mvav 112,244,212
Mvav 116,76,36
Mvav 116,72,56
Mvav 116,240,76
Mvav 116,244,216
WL 0
Mvav 80,248,20
Mvav 80,252,40
Mvvv 80,220,60
Mvvv 80,224,200
Mvav 84,248,24
Mvav 84,252,44
s_nop 0
Ta 76,13,49152
Ta 78,13,51200
Mvvv 84,220,64
s_nop 0
Ta 72,13,53248
Ta 74,13,55296
Mvvv 84,224,204
s_nop 0
Ta 240,13,57344
Ta 242,13,59392
Mvav 88,248,28
s_nop 0
Ta 244,13,61440
Ta 246,13,63488
Mvav 88,252,48
Mvvv 88,220,68
Mvvv 88,224,208
Mvav 92,248,32
Mvav 92,252,52
Mvvv 92,220,72
Mvvv 92,224,212
Mvav 96,248,36
Mvav 96,252,56
Mvvv 96,220,76
Mvvv 96,224,216
WL 0
Mvaa 100,76,200
Mvaa 100,72,220
Mvaa 100,240,0
Mvaa 100,244,20
Mvaa 104,76,204
Mvaa 104,72,224
s_nop 0
Ta 248,14,49152
Ta 250,14,51200
Mvaa 104,240,4
s_nop 0
Ta 252,14,53248
Ta 254,14,55296
Mvaa 104,244,24
s_nop 0
Tv 220,14,57344
Tv 222,14,59392
Mvaa 108,76,208
s_nop 0
Tv 224,14,61440
Tv 226,14,63488
Mvaa 108,72,228
Mvaa 108,240,8
Mvaa 108,244,28
Mvaa 112,76,212
Mvaa 112,72,232
Mvaa 112,240,12
Mvaa 112,244,32
Mvaa 116,76,216
Mvaa 116,72,236
Mvaa 116,240,16
Mvaa 116,244,36
WL 0
Mvaa 80,248,200
Mvaa 80,252,220
Mvva 80,220,0
Mvva 80,224,20
Mvaa 84,248,204
Mvaa 84,252,224
Mvva 84,220,4
Mvva 84,224,24
CV 100,100,101
CV 101,102,103
Mvaa 88,248,208
s_nop 0
CV 102,80,81
CV 103,82,83
Mvaa 88,252,228
s_barrier
Mvva 88,220,8
buffer_store_dwordx4 v[100:103],v5,s[44:47],s79 offen offset:0 sc1
Mvva 88,224,28
CV 104,104,105
CV 105,106,107
Mvaa 92,248,212
CV 106,84,85
CV 107,86,87
Mvaa 92,252,232
Mvva 92,220,12
Mvva 92,224,32
buffer_store_dwordx4 v[104:107],v5,s[44:47],s79 offen offset:1024 sc1
Mvaa 96,248,216
CV 108,108,109
CV 109,110,111
Mvaa 96,252,236
CV 110,88,89
CV 111,90,91
Mvva 96,220,16
Mvva 96,224,36
buffer_store_dwordx4 v[108:111],v5,s[44:47],s79 offen offset:2048 sc1
CV 112,112,113
CV 113,114,115
CV 114,92,93
CV 115,94,95
buffer_store_dwordx4 v[112:115],v5,s[44:47],s79 offen offset:3072 sc1
CV 116,116,117
CV 117,118,119
CV 118,96,97
CV 119,98,99
buffer_store_dwordx4 v[116:119],v5,s[44:47],s84 offen offset:0 sc1
s_add_u32 s79,s79,20480
s_add_u32 s84,s84,20480
s_waitcnt vmcnt(0)
s_barrier
v_mov_b32 v2,%0
v_add_u32 v2,9,v2
buffer_store_dwordx2 v[2:3],v8,s[24:27],s66 offen sc1
buffer_load_dwordx2 v[0:1],v7,s[24:27],0 offen offset:128 sc1
Zvva 236,230,40
Zvva 240,228,40
Mvva 236,230,44
Mvva 240,228,44
Mvva 236,230,48
Mvva 240,228,48
Mvva 236,230,52
Mvva 240,228,52
Mvva 236,230,56
Mvva 240,228,56
Mvva 236,230,60
Mvva 240,228,60
Mvva 236,230,64
Mvva 240,228,64
Mvva 236,230,68
Mvva 240,228,68
s_waitcnt vmcnt(0)
s_add_u32 s80,%0,9
s_mov_b32 s81,0x40000
1:
v_subrev_u32 v9,s80,v0
v_cmp_eq_u32 s[82:83],%1,v1
v_cmp_ge_u32 vcc,6,v9
s_nop 1
s_and_b64 vcc,vcc,s[82:83]
s_cmp_eq_u64 vcc,-1
s_cbranch_scc1 2f
s_sleep 1
buffer_load_dwordx2 v[0:1],v7,s[24:27],0 offen offset:128 sc1
s_sub_u32 s81,s81,1
s_waitcnt vmcnt(0)
s_cmp_lg_u32 s81,0
s_cbranch_scc1 1b
2:
s_add_u32 s30,s51,0
buffer_load_dwordx4 a[80:83],v5,s[36:39],s30 offen sc1
s_add_u32 s30,s51,184320
buffer_load_dwordx4 a[84:87],v5,s[36:39],s30 offen sc1
s_add_u32 s30,s51,368640
buffer_load_dwordx4 a[88:91],v5,s[36:39],s30 offen sc1
s_add_u32 s30,s51,552960
buffer_load_dwordx4 a[92:95],v5,s[36:39],s30 offen sc1
s_add_u32 s30,s51,737280
buffer_load_dwordx4 a[96:99],v5,s[36:39],s30 offen sc1
s_add_u32 s30,s51,921600
buffer_load_dwordx4 a[100:103],v5,s[36:39],s30 offen sc1
s_add_u32 s30,s51,1105920
buffer_load_dwordx4 a[104:107],v5,s[36:39],s30 offen sc1
s_add_u32 s30,s51,1290240
buffer_load_dwordx4 a[108:111],v5,s[36:39],s30 offen sc1
s_add_u32 s30,s52,0
buffer_load_dwordx4 a[112:115],v5,s[36:39],s30 offen sc1
s_add_u32 s30,s52,184320
buffer_load_dwordx4 a[116:119],v5,s[36:39],s30 offen sc1
s_add_u32 s30,s52,368640
buffer_load_dwordx4 a[120:123],v5,s[36:39],s30 offen sc1
s_add_u32 s30,s52,552960
buffer_load_dwordx4 a[124:127],v5,s[36:39],s30 offen sc1
s_add_u32 s30,s52,737280
buffer_load_dwordx4 a[128:131],v5,s[36:39],s30 offen sc1
s_add_u32 s30,s52,921600
buffer_load_dwordx4 a[132:135],v5,s[36:39],s30 offen sc1
s_add_u32 s30,s52,1105920
buffer_load_dwordx4 a[136:139],v5,s[36:39],s30 offen sc1
s_add_u32 s30,s52,1290240
buffer_load_dwordx4 a[140:143],v5,s[36:39],s30 offen sc1
s_add_u32 s30,s53,0
buffer_load_dwordx4 a[144:147],v5,s[36:39],s30 offen sc1
s_add_u32 s30,s53,184320
buffer_load_dwordx4 a[148:151],v5,s[36:39],s30 offen sc1
s_add_u32 s30,s53,368640
buffer_load_dwordx4 a[152:155],v5,s[36:39],s30 offen sc1
s_add_u32 s30,s53,552960
buffer_load_dwordx4 a[156:159],v5,s[36:39],s30 offen sc1
s_add_u32 s30,s53,737280
buffer_load_dwordx4 a[160:163],v5,s[36:39],s30 offen sc1
s_add_u32 s30,s53,921600
buffer_load_dwordx4 a[164:167],v5,s[36:39],s30 offen sc1
s_add_u32 s30,s53,1105920
buffer_load_dwordx4 a[168:171],v5,s[36:39],s30 offen sc1
s_add_u32 s30,s53,1290240
buffer_load_dwordx4 a[172:175],v5,s[36:39],s30 offen sc1
v_cmp_class_f32 vcc,v236,s67
s_nop 1
s_cmp_eq_u64 vcc,0
s_cbranch_scc1 3f
s_lshl_b32 s80,s62,2
s_add_u32 s80,s80,%9
s_add_u32 s80,s80,159744
v_bfe_u32 v9,v5,4,4
v_lshl_add_u32 v9,v9,2,s80
v_mov_b32 v0,1
s_mov_b64 exec,vcc
ds_write_b32 v9,v0
s_add_u32 s80,%9,160384
v_mov_b32 v9,s80
ds_write_b32 v9,v0
s_mov_b64 exec,-1
s_waitcnt lgkmcnt(0)
s_nop 4
3:
CV 236,236,237
CV 237,238,239
CV 238,240,241
CV 239,242,243
s_nop 0
v_permlane32_swap_b32 v236,v238
v_permlane32_swap_b32 v237,v239
s_nop 0
v_permlane16_swap_b32 v236,v238
v_permlane16_swap_b32 v237,v239
buffer_store_dwordx4 v[236:239],v6,s[96:99],s56 offen
s_cmp_eq_u32 s66,128
s_cselect_b32 s80,0,0x20000000
v_add_u32 v2,7,v2
buffer_store_dwordx2 v[2:3],v8,s[24:27],s80 offen sc1
s_waitcnt vmcnt(25)
Zvva 20,230,80
Zvva 24,228,80
s_waitcnt vmcnt(24)
Mvva 20,230,84
Mvva 24,228,84
s_waitcnt vmcnt(23)
Mvva 20,230,88
Mvva 24,228,88
s_waitcnt vmcnt(22)
Mvva 20,230,92
Mvva 24,228,92
s_waitcnt vmcnt(21)
Mvva 20,230,96
Mvva 24,228,96
s_waitcnt vmcnt(20)
Mvva 20,230,100
Mvva 24,228,100
s_waitcnt vmcnt(19)
Mvva 20,230,104
Mvva 24,228,104
s_waitcnt vmcnt(18)
Mvva 20,230,108
Mvva 24,228,108
s_waitcnt vmcnt(17)
Zvva 28,230,112
Zvva 32,228,112
s_waitcnt vmcnt(16)
Mvva 28,230,116
Mvva 32,228,116
s_nop 0
v_cmp_class_f32 vcc,v20,s67
s_nop 1
s_cmp_eq_u64 vcc,0
s_cbranch_scc1 3f
s_lshl_b32 s80,s63,2
s_add_u32 s80,s80,%9
s_add_u32 s80,s80,159744
v_bfe_u32 v9,v5,4,4
v_lshl_add_u32 v9,v9,2,s80
v_mov_b32 v0,1
s_mov_b64 exec,vcc
ds_write_b32 v9,v0
s_add_u32 s80,%9,160384
v_mov_b32 v9,s80
ds_write_b32 v9,v0
s_mov_b64 exec,-1
s_waitcnt lgkmcnt(0)
s_nop 4
3:
s_waitcnt vmcnt(15)
Mvva 28,230,120
CV 20,20,21
CV 21,22,23
Mvva 32,228,120
CV 22,24,25
CV 23,26,27
s_waitcnt vmcnt(14)
Mvva 28,230,124
v_permlane32_swap_b32 v20,v22
Mvva 32,228,124
v_permlane32_swap_b32 v21,v23
s_waitcnt vmcnt(13)
Mvva 28,230,128
v_permlane16_swap_b32 v20,v22
Mvva 32,228,128
v_permlane16_swap_b32 v21,v23
s_waitcnt vmcnt(12)
Mvva 28,230,132
buffer_store_dwordx4 v[20:23],v6,s[96:99],s57 offen
Mvva 32,228,132
s_waitcnt vmcnt(12)
Mvva 28,230,136
Mvva 32,228,136
s_waitcnt vmcnt(11)
Mvva 28,230,140
Mvva 32,228,140
s_waitcnt vmcnt(10)
Zvva 36,230,144
Zvva 40,228,144
s_waitcnt vmcnt(9)
Mvva 36,230,148
Mvva 40,228,148
s_nop 0
v_cmp_class_f32 vcc,v28,s67
s_nop 1
s_cmp_eq_u64 vcc,0
s_cbranch_scc1 3f
s_lshl_b32 s80,s64,2
s_add_u32 s80,s80,%9
s_add_u32 s80,s80,159744
v_bfe_u32 v9,v5,4,4
v_lshl_add_u32 v9,v9,2,s80
v_mov_b32 v0,1
s_mov_b64 exec,vcc
ds_write_b32 v9,v0
s_add_u32 s80,%9,160384
v_mov_b32 v9,s80
ds_write_b32 v9,v0
s_mov_b64 exec,-1
s_waitcnt lgkmcnt(0)
s_nop 4
3:
s_waitcnt vmcnt(8)
Mvva 36,230,152
CV 28,28,29
CV 29,30,31
Mvva 40,228,152
CV 30,32,33
CV 31,34,35
s_waitcnt vmcnt(7)
Mvva 36,230,156
v_permlane32_swap_b32 v28,v30
Mvva 40,228,156
v_permlane32_swap_b32 v29,v31
s_waitcnt vmcnt(6)
Mvva 36,230,160
v_permlane16_swap_b32 v28,v30
Mvva 40,228,160
v_permlane16_swap_b32 v29,v31
s_waitcnt vmcnt(5)
Mvva 36,230,164
buffer_store_dwordx4 v[28:31],v6,s[96:99],s58 offen
Mvva 40,228,164
s_waitcnt vmcnt(5)
Mvva 36,230,168
Mvva 40,228,168
s_waitcnt vmcnt(4)
Mvva 36,230,172
Mvva 40,228,172
s_nop 6
v_cmp_class_f32 vcc,v36,s67
s_nop 1
s_cmp_eq_u64 vcc,0
s_cbranch_scc1 3f
s_lshl_b32 s80,s65,2
s_add_u32 s80,s80,%9
s_add_u32 s80,s80,159744
v_bfe_u32 v9,v5,4,4
v_lshl_add_u32 v9,v9,2,s80
v_mov_b32 v0,1
s_mov_b64 exec,vcc
ds_write_b32 v9,v0
s_add_u32 s80,%9,160384
v_mov_b32 v9,s80
ds_write_b32 v9,v0
s_mov_b64 exec,-1
s_waitcnt lgkmcnt(0)
s_nop 4
3:
CV 36,36,37
CV 37,38,39
CV 38,40,41
CV 39,42,43
s_nop 0
v_permlane32_swap_b32 v36,v38
v_permlane32_swap_b32 v37,v39
s_nop 0
v_permlane16_swap_b32 v36,v38
v_permlane16_swap_b32 v37,v39
buffer_store_dwordx4 v[36:39],v6,s[96:99],s59 offen
s_add_u32 s80,%9,160384
v_mov_b32 v9,s80
s_barrier
ds_read_b32 v9,v9
s_waitcnt lgkmcnt(0)
v_readfirstlane_b32 s80,v9
s_nop 3
s_cmp_lg_u32 s80,0
s_cbranch_scc1 4f
s_endpgm
4:
s_branch .Lmha_end
.p2align 12
.Lmha_r1:
s_waitcnt vmcnt(13)
s_barrier
Ta 240,9,32768
Ta 242,10,33024
Ta 244,9,32776
Ta 246,10,33032
Rv 180,11,0
Rv 184,11,2048
Rv 188,11,4096
Rv 192,11,6144
Rv 196,11,8192
WL 4
Zvav 20,240,180
WL 3
s_add_u32 m0,s76,57344
v_add_u32 v3,262144,v3
Zvav 24,240,184
WL 2
Zvav 28,240,188
WL 1
Ta 248,9,33280
Zvav 32,240,192
WL 1
Ta 250,10,33536
Ta 252,9,33288
Zvav 36,240,196
s_nop 0
Ta 254,10,33544
Rv 200,12,0
Zvav 100,244,180
Tv 220,9,33792
Zvav 104,244,184
s_nop 0
Tv 222,10,34048
Rv 204,12,2048
Zvav 108,244,188
Rv 208,12,4096
Zvav 112,244,192
Rv 212,12,6144
Zvav 116,244,196
WL 8
Rv 216,12,8192
Zvav 40,248,180
Tv 224,9,33800
Zvav 44,248,184
Tv 226,10,34056
Zvav 48,248,188
Zvav 52,248,192
Zvav 56,248,196
WL 5
Zvav 120,252,180
s_nop 0
Ta 240,9,34304
Ta 242,10,34560
Zvav 124,252,184
LD 3,28,64,0
Zvav 128,252,188
Zvav 132,252,192
Zvav 136,252,196
s_nop 0
Ta 244,9,34312
Ta 246,10,34568
Zvvv 60,220,180
LD 3,28,65,1024
Zvvv 64,220,184
Zvvv 68,220,188
Zvvv 72,220,192
Zvvv 76,220,196
WL 2
LD 3,28,66,2048
Zvvv 140,224,180
Zvvv 144,224,184
Zvvv 148,224,188
Zvvv 152,224,192
LD 3,28,67,3072
Zvvv 156,224,196
s_waitcnt vmcnt(8) lgkmcnt(0)
s_barrier
Zvav 80,240,180
s_nop 0
Ta 248,9,49152
Ta 250,10,49408
Zvav 84,240,184
s_nop 0
Ta 252,9,49160
Ta 254,10,49416
Zvav 88,240,188
s_cmp_le_u32 s89,2
s_cselect_b64 s[24:25],s[68:69],%2
v_add_u32 v0,128,v0
Zvav 92,240,192
s_add_u32 m0,s77,118784
Zvav 96,240,196
LD 0,24,49,2048
Zvav 160,244,180
s_add_u32 m0,s77,122880
Tv 220,9,49664
Tv 222,10,49920
Zvav 164,244,184
Zvav 168,244,188
Zvav 172,244,192
LD 0,24,51,2048
Zvav 176,244,196
WL 2
s_add_u32 m0,s77,126976
Rv 180,11,40960
Mvav 20,248,200
s_nop 0
Tv 224,9,49672
Tv 226,10,49928
Mvav 24,248,204
s_nop 0
Rv 184,11,43008
Rv 188,11,45056
Mvav 28,248,208
LD 0,24,53,2048
Mvav 32,248,212
s_add_u32 m0,s77,131072
Rv 192,11,47104
Rv 196,11,49152
Mvav 36,248,216
Mvav 100,252,200
s_nop 0
Ta 240,9,50176
Ta 242,10,50432
Mvav 104,252,204
LD 0,24,55,2048
Mvav 108,252,208
s_add_u32 m0,s77,135168
Mvav 112,252,212
Mvav 116,252,216
WL 4
Mvvv 40,220,200
s_nop 0
Ta 244,9,50184
Ta 246,10,50440
Mvvv 44,220,204
LD 0,24,57,2048
Mvvv 48,220,208
s_add_u32 m0,s76,73728
s_cmp_le_u32 s89,2
s_cselect_b64 s[28:29],s[70:71],%3
Mvvv 52,220,212
v_add_u32 v3,262144,v3
Mvvv 56,220,216
Mvvv 120,224,200
s_nop 0
Ta 248,9,50688
Ta 250,10,50944
Mvvv 124,224,204
LD 3,28,64,0
Mvvv 128,224,208
Mvvv 132,224,212
Mvvv 136,224,216
WL 2
Ta 252,9,50696
Ta 254,10,50952
Mvav 60,240,200
LD 3,28,65,1024
Mvav 64,240,204
Mvav 68,240,208
Mvav 72,240,212
Mvav 76,240,216
LD 3,28,66,2048
Mvav 140,244,200
Mvav 144,244,204
Mvav 148,244,208
Mvav 152,244,212
LD 3,28,67,3072
Mvav 156,244,216
s_waitcnt vmcnt(13) lgkmcnt(0)
s_barrier
Mvav 80,248,200
s_nop 0
Tv 220,9,0
Tv 222,10,256
Mvav 84,248,204
s_nop 0
Tv 224,9,8
Tv 226,10,264
Mvav 88,248,208
s_cmp_le_u32 s89,3
s_cselect_b64 s[24:25],s[68:69],%2
v_add_u32 v0,128,v0
Mvav 92,248,212
s_add_u32 m0,s77,98304
Mvav 96,248,216
LD 0,24,49,2048
Mvav 160,252,200
s_add_u32 m0,s77,102400
Ta 240,9,512
Ta 242,10,768
Mvav 164,252,204
Mvav 168,252,208
Mvav 172,252,212
LD 0,24,51,2048
Mvav 176,252,216
WL 2
s_add_u32 m0,s77,106496
Rv 200,12,40960
Mvvv 20,220,180
s_nop 0
Ta 244,9,520
Ta 246,10,776
Mvvv 24,220,184
s_nop 0
Rv 204,12,43008
Rv 208,12,45056
Mvvv 28,220,188
LD 0,24,53,2048
Mvvv 32,220,192
s_add_u32 m0,s77,110592
Rv 212,12,47104
Rv 216,12,49152
Mvvv 36,220,196
Mvvv 100,224,180
s_nop 0
Ta 248,9,1024
Ta 250,10,1280
Mvvv 104,224,184
LD 0,24,55,2048
Mvvv 108,224,188
s_add_u32 m0,s77,114688
Mvvv 112,224,192
Mvvv 116,224,196
WL 4
Mvav 40,240,180
s_nop 0
Ta 252,9,1032
Ta 254,10,1288
Mvav 44,240,184
LD 0,24,57,2048
Mvav 48,240,188
s_add_u32 m0,s76,8192
v_add_u32 v1,262144,v1
Mvav 52,240,192
Mvav 56,240,196
Mvav 120,244,180
s_nop 0
Tv 220,9,1536
Tv 222,10,1792
Mvav 124,244,184
LN 1,36,58,0
Mvav 128,244,188
Mvav 132,244,192
Mvav 136,244,196
WL 2
LN 1,36,59,1024
LN 1,36,60,2048
LN 1,36,61,3072
Mvav 60,248,180
s_add_u32 m0,s76,90112
Tv 224,9,1544
Tv 226,10,1800
Mvav 64,248,184
v_add_u32 v3,262144,v3
Mvav 68,248,188
Mvav 72,248,192
Mvav 76,248,196
Mvav 140,252,180
Mvav 144,252,184
Mvav 148,252,188
Mvav 152,252,192
Mvav 156,252,196
s_waitcnt vmcnt(13) lgkmcnt(0)
s_barrier
Mvvv 80,220,180
s_nop 0
Ta 240,9,16384
Ta 242,10,16640
Mvvv 84,220,184
s_nop 0
Ta 244,9,16392
Ta 246,10,16648
Mvvv 88,220,188
LD 3,28,64,0
Mvvv 92,220,192
Mvvv 96,220,196
Mvvv 160,224,180
s_nop 0
Ta 248,9,16896
Ta 250,10,17152
Mvvv 164,224,184
LD 3,28,65,1024
Mvvv 168,224,188
Mvvv 172,224,192
Mvvv 176,224,196
WL 2
Rv 180,11,20480
Rv 184,11,22528
Mvav 20,240,200
Ta 252,9,16904
Mvav 24,240,204
s_nop 0
Ta 254,10,17160
Rv 188,11,24576
Mvav 28,240,208
LD 3,28,66,2048
Mvav 32,240,212
s_nop 0
Rv 192,11,26624
Rv 196,11,28672
Mvav 36,240,216
Mvav 100,244,200
s_nop 0
Tv 220,9,17408
Tv 222,10,17664
Mvav 104,244,204
LD 3,28,67,3072
Mvav 108,244,208
s_add_u32 m0,s76,40960
s_cmp_le_u32 s89,3
s_cselect_b64 s[28:29],s[70:71],%3
Mvav 112,244,212
v_add_u32 v3,262144,v3
Mvav 116,244,216
WL 4
Mvav 40,248,200
s_nop 0
Tv 224,9,17416
Tv 226,10,17672
Mvav 44,248,204
LD 3,28,64,0
Mvav 48,248,208
Mvav 52,248,212
Mvav 56,248,216
Mvav 120,252,200
s_nop 0
Ta 240,9,17920
Ta 242,10,18176
Mvav 124,252,204
LD 3,28,65,1024
Mvav 128,252,208
Mvav 132,252,212
Mvav 136,252,216
WL 2
Ta 244,9,17928
Ta 246,10,18184
Mvvv 60,220,200
LD 3,28,66,2048
Mvvv 64,220,204
Mvvv 68,220,208
Mvvv 72,220,212
Mvvv 76,220,216
LD 3,28,67,3072
Mvvv 140,224,200
s_cmp_le_u32 s89,4
s_cselect_b64 s[24:25],s[68:69],%2
v_add_u32 v0,128,v0
Mvvv 144,224,204
s_add_u32 m0,s77,139264
Mvvv 148,224,208
Mvvv 152,224,212
LD 0,24,49,2048
Mvvv 156,224,216
s_waitcnt vmcnt(18) lgkmcnt(0)
s_barrier
Mvav 80,240,200
s_add_u32 m0,s77,143360
Ta 248,9,32768
Ta 250,10,33024
Mvav 84,240,204
s_nop 0
Ta 252,9,32776
Ta 254,10,33032
Mvav 88,240,208
LD 0,24,51,2048
Mvav 92,240,212
s_add_u32 m0,s77,147456
Mvav 96,240,216
Mvav 160,244,200
s_nop 0
Tv 220,9,33280
Tv 222,10,33536
Mvav 164,244,204
LD 0,24,53,2048
Mvav 168,244,208
s_add_u32 m0,s77,151552
Mvav 172,244,212
Mvav 176,244,216
WL 2
Rv 200,12,20480
Rv 204,12,22528
Mvav 20,248,180
Tv 224,9,33288
Mvav 24,248,184
s_nop 0
Tv 226,10,33544
Rv 208,12,24576
Mvav 28,248,188
LD 0,24,55,2048
Mvav 32,248,192
s_add_u32 m0,s77,155648
Rv 212,12,26624
Rv 216,12,28672
Mvav 36,248,196
Mvav 100,252,180
s_nop 0
Ta 240,9,33792
Ta 242,10,34048
Mvav 104,252,184
LD 0,24,57,2048
Mvav 108,252,188
v_mov_b32 v238,0
Mvav 112,252,192
Mvav 116,252,196
WL 4
Mvvv 40,220,180
s_nop 0
Ta 244,9,33800
Ta 246,10,34056
Mvvv 44,220,184
global_load_dwordx2 v[240:241],v238,%11 sc1
Mvvv 48,220,188
s_add_u32 m0,s76,57344
v_add_u32 v3,262144,v3
Mvvv 52,220,192
Mvvv 56,220,196
Mvvv 120,224,180
s_nop 0
Ta 248,9,34304
Ta 250,10,34560
Mvvv 124,224,184
Mvvv 128,224,188
Mvvv 132,224,192
Mvvv 136,224,196
WL 2
Ta 252,9,34312
Ta 254,10,34568
Mvav 60,240,180
LD 3,28,64,0
Mvav 64,240,184
Mvav 68,240,188
Mvav 72,240,192
Mvav 76,240,196
LD 3,28,65,1024
Mvav 140,244,180
Mvav 144,244,184
Mvav 148,244,188
Mvav 152,244,192
LD 3,28,66,2048
Mvav 156,244,196
s_waitcnt vmcnt(13) lgkmcnt(0)
s_barrier
Mvav 80,248,180
s_nop 0
Tv 220,9,49152
Tv 222,10,49408
Mvav 84,248,184
s_nop 0
Tv 224,9,49160
Tv 226,10,49416
Mvav 88,248,188
LD 3,28,67,3072
Mvav 92,248,192
s_add_u32 m0,s76,24576
s_cmp_eq_u32 s88,1
s_cselect_b32 s86,0x20000,0
Mvav 96,248,196
s_nop 0
v_subrev_u32 v1,s86,v1
v_add_u32 v1,8192,v1
Mvav 160,252,180
s_nop 0
Ta 240,9,49664
Ta 242,10,49920
Mvav 164,252,184
LN 1,36,58,0
Mvav 168,252,188
s_nop 0
Mvav 172,252,192
Mvav 176,252,196
WL 2
Rv 180,11,0
Rv 184,11,2048
Mvvv 20,220,200
s_nop 0
Ta 244,9,49672
Mvvv 24,220,204
s_nop 0
Ta 246,10,49928
Rv 188,11,4096
Mvvv 28,220,208
LN 1,36,59,1024
Mvvv 32,220,212
s_nop 0
Rv 192,11,6144
Rv 196,11,8192
Mvvv 36,220,216
s_nop 0
Mvvv 100,224,200
s_nop 0
Ta 248,9,50176
Ta 250,10,50432
Mvvv 104,224,204
LN 1,36,60,2048
Mvvv 108,224,208
s_nop 0
Mvvv 112,224,212
Mvvv 116,224,216
WL 4
Mvav 40,240,200
s_nop 0
Ta 252,9,50184
Ta 254,10,50440
Mvav 44,240,204
LN 1,36,61,3072
Mvav 48,240,208
s_add_u32 m0,s76,73728
s_cmp_le_u32 s89,4
s_cselect_b64 s[28:29],s[70:71],%3
Mvav 52,240,212
s_nop 0
v_add_u32 v3,262144,v3
Mvav 56,240,216
Mvav 120,244,200
s_nop 0
Tv 220,9,50688
Tv 222,10,50944
Mvav 124,244,204
LD 3,28,64,0
Mvav 128,244,208
Mvav 132,244,212
Mvav 136,244,216
WL 2
Tv 224,9,50696
Tv 226,10,50952
Mvav 60,248,200
LD 3,28,65,1024
Mvav 64,248,204
Mvav 68,248,208
Mvav 72,248,212
Mvav 76,248,216
LD 3,28,66,2048
Mvav 140,252,200
Mvav 144,252,204
Mvav 148,252,208
Mvav 152,252,212
LD 3,28,67,3072
Mvav 156,252,216
s_waitcnt vmcnt(18) lgkmcnt(0)
s_barrier
Mvvv 80,220,200
s_nop 0
Ta 240,9,0
Ta 242,10,256
Mvvv 84,220,204
s_nop 0
Ta 244,9,8
Ta 246,10,264
Mvvv 88,220,208
s_cmp_le_u32 s89,5
s_cselect_b64 s[24:25],s[68:69],%2
v_add_u32 v0,128,v0
Mvvv 92,220,212
s_add_u32 m0,s77,118784
Mvvv 96,220,216
LD 0,24,49,2048
Mvvv 160,224,200
s_add_u32 m0,s77,122880
Ta 248,9,512
Ta 250,10,768
Mvvv 164,224,204
Mvvv 168,224,208
Mvvv 172,224,212
LD 0,24,51,2048
Mvvv 176,224,216
WL 2
s_add_u32 m0,s77,126976
Rv 200,12,0
Mvav 20,240,180
s_nop 0
Ta 252,9,520
Ta 254,10,776
Mvav 24,240,184
s_nop 0
Rv 204,12,2048
Rv 208,12,4096
Mvav 28,240,188
LD 0,24,53,2048
Mvav 32,240,192
s_add_u32 m0,s77,131072
Rv 212,12,6144
Rv 216,12,8192
Mvav 36,240,196
Mvav 100,244,180
s_nop 0
Tv 220,9,1024
Tv 222,10,1280
Mvav 104,244,184
LD 0,24,55,2048
Mvav 108,244,188
s_add_u32 m0,s77,135168
Mvav 112,244,192
Mvav 116,244,196
WL 4
Mvav 40,248,180
s_nop 0
Tv 224,9,1032
Tv 226,10,1288
Mvav 44,248,184
LD 0,24,57,2048
Mvav 48,248,188
s_add_u32 m0,s76,90112
v_add_u32 v3,262144,v3
Mvav 52,248,192
Mvav 56,248,196
Mvav 120,252,180
s_nop 0
Ta 240,9,1536
Ta 242,10,1792
Mvav 124,252,184
LD 3,28,64,0
Mvav 128,252,188
Mvav 132,252,192
Mvav 136,252,196
WL 2
Ta 244,9,1544
Ta 246,10,1800
Mvvv 60,220,180
LD 3,28,65,1024
Mvvv 64,220,184
Mvvv 68,220,188
Mvvv 72,220,192
Mvvv 76,220,196
LD 3,28,66,2048
Mvvv 140,224,180
Mvvv 144,224,184
Mvvv 148,224,188
Mvvv 152,224,192
LD 3,28,67,3072
Mvvv 156,224,196
s_waitcnt vmcnt(17) lgkmcnt(0)
s_barrier
Mvav 80,240,180
s_add_u32 m0,s76,40960
Ta 248,9,16384
Ta 250,10,16640
Mvav 84,240,184
s_nop 0
Ta 252,9,16392
Ta 254,10,16648
Mvav 88,240,188
s_cmp_le_u32 s89,5
s_cselect_b64 s[28:29],s[70:71],%3
v_add_u32 v3,262144,v3
Mvav 92,240,192
LD 3,28,64,0
Mvav 96,240,196
Mvav 160,244,180
s_nop 0
Tv 220,9,16896
Tv 222,10,17152
Mvav 164,244,184
Mvav 168,244,188
LD 3,28,65,1024
Mvav 172,244,192
Mvav 176,244,196
WL 2
Rv 180,11,40960
Rv 184,11,43008
Mvav 20,248,200
Tv 224,9,16904
Mvav 24,248,204
s_nop 0
Tv 226,10,17160
Rv 188,11,45056
Mvav 28,248,208
LD 3,28,66,2048
Mvav 32,248,212
s_nop 0
Rv 192,11,47104
Rv 196,11,49152
Mvav 36,248,216
Mvav 100,252,200
s_nop 0
Ta 240,9,17408
Ta 242,10,17664
Mvav 104,252,204
LD 3,28,67,3072
Mvav 108,252,208
s_cmp_le_u32 s89,6
s_cselect_b64 s[24:25],s[68:69],%2
v_add_u32 v0,128,v0
Mvav 112,252,212
s_add_u32 m0,s77,98304
Mvav 116,252,216
WL 4
Mvvv 40,220,200
s_nop 0
Ta 244,9,17416
Ta 246,10,17672
Mvvv 44,220,204
LD 0,24,49,2048
Mvvv 48,220,208
s_add_u32 m0,s77,102400
Mvvv 52,220,212
Mvvv 56,220,216
Mvvv 120,224,200
s_nop 0
Ta 248,9,17920
Ta 250,10,18176
Mvvv 124,224,204
LD 0,24,51,2048
Mvvv 128,224,208
)ASM"
R"ASM(s_add_u32 m0,s77,106496
Mvvv 132,224,212
Mvvv 136,224,216
WL 2
Ta 252,9,17928
Ta 254,10,18184
Mvav 60,240,200
LD 0,24,53,2048
Mvav 64,240,204
s_add_u32 m0,s77,110592
Mvav 68,240,208
Mvav 72,240,212
Mvav 76,240,216
LD 0,24,55,2048
Mvav 140,244,200
s_add_u32 m0,s77,114688
Mvav 144,244,204
Mvav 148,244,208
Mvav 152,244,212
LD 0,24,57,2048
Mvav 156,244,216
s_waitcnt vmcnt(18) lgkmcnt(0)
s_barrier
Mvav 80,248,200
s_add_u32 m0,s76,57344
Tv 220,9,32768
Tv 222,10,33024
Mvav 84,248,204
s_nop 0
Tv 224,9,32776
Tv 226,10,33032
Mvav 88,248,208
s_nop 0
v_add_u32 v3,262144,v3
Mvav 92,248,212
LD 3,28,64,0
Mvav 96,248,216
s_nop 0
Mvav 160,252,200
s_nop 0
Ta 240,9,33280
Ta 242,10,33536
Mvav 164,252,204
s_nop 0
Mvav 168,252,208
LD 3,28,65,1024
Mvav 172,252,212
s_nop 0
Mvav 176,252,216
WL 2
Rv 200,12,40960
Rv 204,12,43008
Mvvv 20,220,180
s_nop 0
Ta 244,9,33288
Mvvv 24,220,184
s_nop 0
Ta 246,10,33544
Rv 208,12,45056
Mvvv 28,220,188
LD 3,28,66,2048
Mvvv 32,220,192
s_nop 0
Rv 212,12,47104
Rv 216,12,49152
Mvvv 36,220,196
s_nop 0
Mvvv 100,224,180
s_nop 0
Ta 248,9,33792
Ta 250,10,34048
Mvvv 104,224,184
LD 3,28,67,3072
Mvvv 108,224,188
s_nop 0
Mvvv 112,224,192
Mvvv 116,224,196
WL 4
Mvav 40,240,180
s_nop 0
Ta 252,9,33800
Ta 254,10,34056
Mvav 44,240,184
s_nop 0
Mvav 48,240,188
Mvav 52,240,192
Mvav 56,240,196
Mvav 120,244,180
s_nop 0
Tv 220,9,34304
Tv 222,10,34560
Mvav 124,244,184
Mvav 128,244,188
Mvav 132,244,192
Mvav 136,244,196
WL 2
Tv 224,9,34312
Tv 226,10,34568
Mvav 60,248,180
Mvav 64,248,184
Mvav 68,248,188
Mvav 72,248,192
Mvav 76,248,196
Mvav 140,252,180
Mvav 144,252,184
Mvav 148,252,188
Mvav 152,252,192
Mvav 156,252,196
s_waitcnt vmcnt(13) lgkmcnt(0)
s_barrier
Mvvv 80,220,180
s_add_u32 m0,s76,73728
Ta 240,9,49152
Ta 242,10,49408
Mvvv 84,220,184
s_nop 0
Ta 244,9,49160
Ta 246,10,49416
Mvvv 88,220,188
s_cmp_le_u32 s89,6
s_cselect_b64 s[28:29],s[70:71],%3
v_add_u32 v3,262144,v3
Mvvv 92,220,192
LD 3,28,64,0
Mvvv 96,220,196
Mvvv 160,224,180
s_nop 0
Ta 248,9,49664
Ta 250,10,49920
Mvvv 164,224,184
Mvvv 168,224,188
LD 3,28,65,1024
Mvvv 172,224,192
Mvvv 176,224,196
WL 2
Rv 180,11,20480
Rv 184,11,22528
Mvav 20,240,200
Ta 252,9,49672
Mvav 24,240,204
s_nop 0
Ta 254,10,49928
Rv 188,11,24576
Mvav 28,240,208
LD 3,28,66,2048
Mvav 32,240,212
s_nop 0
Rv 192,11,26624
Rv 196,11,28672
Mvav 36,240,216
Mvav 100,244,200
s_nop 0
Tv 220,9,50176
Tv 222,10,50432
Mvav 104,244,204
LD 3,28,67,3072
Mvav 108,244,208
s_cmp_le_u32 s89,7
s_cselect_b64 s[24:25],s[68:69],%2
v_add_u32 v0,128,v0
Mvav 112,244,212
s_add_u32 m0,s77,139264
Mvav 116,244,216
WL 4
Mvav 40,248,200
s_nop 0
Tv 224,9,50184
Tv 226,10,50440
Mvav 44,248,204
LD 0,24,49,2048
Mvav 48,248,208
s_add_u32 m0,s77,143360
Mvav 52,248,212
Mvav 56,248,216
Mvav 120,252,200
s_nop 0
Ta 240,9,50688
Ta 242,10,50944
Mvav 124,252,204
LD 0,24,51,2048
Mvav 128,252,208
s_add_u32 m0,s77,147456
Mvav 132,252,212
Mvav 136,252,216
WL 2
Ta 244,9,50696
Ta 246,10,50952
Mvvv 60,220,200
LD 0,24,53,2048
Mvvv 64,220,204
s_add_u32 m0,s77,151552
Mvvv 68,220,208
Mvvv 72,220,212
Mvvv 76,220,216
LD 0,24,55,2048
Mvvv 140,224,200
s_add_u32 m0,s77,155648
Mvvv 144,224,204
Mvvv 148,224,208
Mvvv 152,224,212
LD 0,24,57,2048
Mvvv 156,224,216
s_waitcnt vmcnt(18) lgkmcnt(0)
s_barrier
Mvav 80,240,200
s_add_u32 m0,s76,90112
Ta 248,9,0
Ta 250,10,256
Mvav 84,240,204
s_nop 0
Ta 252,9,8
Ta 254,10,264
Mvav 88,240,208
s_nop 0
v_add_u32 v3,262144,v3
Mvav 92,240,212
LD 3,28,64,0
Mvav 96,240,216
s_nop 0
Mvav 160,244,200
s_nop 0
Tv 220,9,512
Tv 222,10,768
Mvav 164,244,204
s_nop 0
Mvav 168,244,208
LD 3,28,65,1024
Mvav 172,244,212
s_nop 0
Mvav 176,244,216
WL 2
Rv 200,12,20480
Rv 204,12,22528
Mvav 20,248,180
s_nop 0
Tv 224,9,520
Mvav 24,248,184
s_nop 0
Tv 226,10,776
Rv 208,12,24576
Mvav 28,248,188
LD 3,28,66,2048
Mvav 32,248,192
s_nop 0
Rv 212,12,26624
Rv 216,12,28672
Mvav 36,248,196
s_nop 0
Mvav 100,252,180
s_nop 0
Ta 240,9,1024
Ta 242,10,1280
Mvav 104,252,184
LD 3,28,67,3072
Mvav 108,252,188
s_nop 0
Mvav 112,252,192
Mvav 116,252,196
WL 4
Mvvv 40,220,180
s_nop 0
Ta 244,9,1032
Ta 246,10,1288
Mvvv 44,220,184
s_nop 0
Mvvv 48,220,188
Mvvv 52,220,192
Mvvv 56,220,196
s_nop 0
Mvvv 120,224,180
s_nop 0
Ta 248,9,1536
Ta 250,10,1792
Mvvv 124,224,184
s_nop 0
Mvvv 128,224,188
Mvvv 132,224,192
Mvvv 136,224,196
WL 2
Ta 252,9,1544
Ta 254,10,1800
Mvav 60,240,180
Mvav 64,240,184
Mvav 68,240,188
Mvav 72,240,192
Mvav 76,240,196
Mvav 140,244,180
Mvav 144,244,184
Mvav 148,244,188
Mvav 152,244,192
Mvav 156,244,196
s_waitcnt vmcnt(13) lgkmcnt(0)
s_barrier
Mvav 80,248,180
s_add_u32 m0,s76,40960
Tv 220,9,16384
Tv 222,10,16640
Mvav 84,248,184
s_nop 0
Tv 224,9,16392
Tv 226,10,16648
Mvav 88,248,188
s_cmp_le_u32 s89,7
s_cselect_b64 s[28:29],s[70:71],%3
v_add_u32 v3,262144,v3
Mvav 92,248,192
LD 3,28,64,0
Mvav 96,248,196
Mvav 160,252,180
s_nop 0
Ta 240,9,16896
Ta 242,10,17152
Mvav 164,252,184
Mvav 168,252,188
LD 3,28,65,1024
Mvav 172,252,192
Mvav 176,252,196
WL 2
Rv 180,11,0
Rv 184,11,2048
Mvvv 20,220,200
Ta 244,9,16904
Mvvv 24,220,204
s_nop 0
Ta 246,10,17160
Rv 188,11,4096
Mvvv 28,220,208
LD 3,28,66,2048
Mvvv 32,220,212
s_nop 0
Rv 192,11,6144
Rv 196,11,8192
Mvvv 36,220,216
Mvvv 100,224,200
s_nop 0
Ta 248,9,17408
Ta 250,10,17664
Mvvv 104,224,204
LD 3,28,67,3072
Mvvv 108,224,208
s_cmp_le_u32 s89,8
s_cselect_b64 s[24:25],s[68:69],%2
v_add_u32 v0,128,v0
Mvvv 112,224,212
s_add_u32 m0,s77,118784
Mvvv 116,224,216
WL 4
Mvav 40,240,200
s_nop 0
Ta 252,9,17416
Ta 254,10,17672
Mvav 44,240,204
LD 0,24,49,2048
Mvav 48,240,208
s_add_u32 m0,s77,122880
Mvav 52,240,212
Mvav 56,240,216
Mvav 120,244,200
s_nop 0
Tv 220,9,17920
Tv 222,10,18176
Mvav 124,244,204
LD 0,24,51,2048
Mvav 128,244,208
s_add_u32 m0,s77,126976
Mvav 132,244,212
Mvav 136,244,216
WL 2
Tv 224,9,17928
Tv 226,10,18184
Mvav 60,248,200
LD 0,24,53,2048
Mvav 64,248,204
s_add_u32 m0,s77,131072
Mvav 68,248,208
Mvav 72,248,212
Mvav 76,248,216
LD 0,24,55,2048
Mvav 140,252,200
s_add_u32 m0,s77,135168
Mvav 144,252,204
Mvav 148,252,208
Mvav 152,252,212
LD 0,24,57,2048
Mvav 156,252,216
s_waitcnt vmcnt(18) lgkmcnt(0)
s_barrier
Mvvv 80,220,200
s_add_u32 m0,s76,57344
Ta 240,9,32768
Ta 242,10,33024
Mvvv 84,220,204
s_nop 0
Ta 244,9,32776
Ta 246,10,33032
Mvvv 88,220,208
v_add_u32 v3,262144,v3
Mvvv 92,220,212
LD 3,28,64,0
Mvvv 96,220,216
Mvvv 160,224,200
s_nop 0
Ta 248,9,33280
Ta 250,10,33536
Mvvv 164,224,204
Mvvv 168,224,208
LD 3,28,65,1024
Mvvv 172,224,212
Mvvv 176,224,216
WL 2
Rv 200,12,0
Rv 204,12,2048
Mvav 20,240,180
Ta 252,9,33288
Mvav 24,240,184
s_nop 0
Ta 254,10,33544
Rv 208,12,4096
Mvav 28,240,188
LD 3,28,66,2048
Mvav 32,240,192
s_nop 0
Rv 212,12,6144
Rv 216,12,8192
Mvav 36,240,196
Mvav 100,244,180
s_nop 0
Tv 220,9,33792
Tv 222,10,34048
Mvav 104,244,184
LD 3,28,67,3072
Mvav 108,244,188
Mvav 112,244,192
Mvav 116,244,196
WL 4
Mvav 40,248,180
s_nop 0
Tv 224,9,33800
Tv 226,10,34056
Mvav 44,248,184
Mvav 48,248,188
Mvav 52,248,192
Mvav 56,248,196
Mvav 120,252,180
s_nop 0
Ta 240,9,34304
Ta 242,10,34560
Mvav 124,252,184
Mvav 128,252,188
Mvav 132,252,192
Mvav 136,252,196
WL 2
Ta 244,9,34312
Ta 246,10,34568
Mvvv 60,220,180
Mvvv 64,220,184
Mvvv 68,220,188
Mvvv 72,220,192
Mvvv 76,220,196
Mvvv 140,224,180
Mvvv 144,224,184
Mvvv 148,224,188
Mvvv 152,224,192
Mvvv 156,224,196
s_waitcnt vmcnt(13) lgkmcnt(0)
s_barrier
Mvav 80,240,180
s_add_u32 m0,s76,73728
Ta 248,9,49152
Ta 250,10,49408
Mvav 84,240,184
s_nop 0
Ta 252,9,49160
Ta 254,10,49416
Mvav 88,240,188
s_cmp_le_u32 s89,8
s_cselect_b64 s[28:29],s[70:71],%3
v_add_u32 v3,262144,v3
Mvav 92,240,192
LD 3,28,64,0
Mvav 96,240,196
s_nop 0
Mvav 160,244,180
s_nop 0
Tv 220,9,49664
Tv 222,10,49920
Mvav 164,244,184
s_nop 0
Mvav 168,244,188
LD 3,28,65,1024
Mvav 172,244,192
s_nop 0
Mvav 176,244,196
WL 2
Rv 180,11,40960
Rv 184,11,43008
Mvav 20,248,200
s_nop 0
Tv 224,9,49672
Mvav 24,248,204
s_nop 0
Tv 226,10,49928
Rv 188,11,45056
Mvav 28,248,208
LD 3,28,66,2048
Mvav 32,248,212
s_nop 0
Rv 192,11,47104
Rv 196,11,49152
Mvav 36,248,216
s_nop 0
Mvav 100,252,200
s_nop 0
Ta 240,9,50176
Ta 242,10,50432
Mvav 104,252,204
LD 3,28,67,3072
Mvav 108,252,208
s_add_u32 m0,s76,106496
v_add_u32 v2,262144,v2
Mvav 112,252,212
s_nop 0
Mvav 116,252,216
WL 4
Mvvv 40,220,200
s_nop 0
Ta 244,9,50184
Ta 246,10,50440
Mvvv 44,220,204
LN 2,96,62,0
Mvvv 48,220,208
s_nop 0
Mvvv 52,220,212
Mvvv 56,220,216
Mvvv 120,224,200
s_nop 0
Ta 248,9,50688
Ta 250,10,50944
Mvvv 124,224,204
LN 2,96,63,1024
Mvvv 128,224,208
s_nop 0
Mvvv 132,224,212
Mvvv 136,224,216
WL 2
Ta 252,9,50696
Ta 254,10,50952
Mvav 60,240,200
LN 2,96,62,2048
Mvav 64,240,204
s_nop 0
Mvav 68,240,208
Mvav 72,240,212
Mvav 76,240,216
LN 2,96,63,3072
Mvav 140,244,200
s_nop 0
Mvav 144,244,204
Mvav 148,244,208
Mvav 152,244,212
s_nop 0
Mvav 156,244,216
s_waitcnt vmcnt(17) lgkmcnt(0)
s_barrier
Mvav 80,248,200
s_add_u32 m0,s76,90112
Tv 220,9,0
Tv 222,10,256
Mvav 84,248,204
s_nop 0
Tv 224,9,8
Tv 226,10,264
Mvav 88,248,208
s_nop 0
v_add_u32 v3,262144,v3
Mvav 92,248,212
LD 3,28,64,0
Mvav 96,248,216
s_nop 0
Mvav 160,252,200
s_nop 0
Ta 240,9,512
Ta 242,10,768
Mvav 164,252,204
s_nop 0
Mvav 168,252,208
LD 3,28,65,1024
Mvav 172,252,212
s_nop 0
Mvav 176,252,216
WL 2
Rv 200,12,40960
Rv 204,12,43008
Mvvv 20,220,180
s_nop 0
Ta 244,9,520
Mvvv 24,220,184
s_nop 0
Ta 246,10,776
Rv 208,12,45056
Mvvv 28,220,188
LD 3,28,66,2048
Mvvv 32,220,192
s_nop 0
Rv 212,12,47104
Rv 216,12,49152
Mvvv 36,220,196
Mvvv 100,224,180
s_nop 0
Ta 248,9,1024
Ta 250,10,1280
Mvvv 104,224,184
LD 3,28,67,3072
Mvvv 108,224,188
Mvvv 112,224,192
Mvvv 116,224,196
WL 4
Mvav 40,240,180
s_nop 0
Ta 252,9,1032
Ta 254,10,1288
Mvav 44,240,184
Mvav 48,240,188
Mvav 52,240,192
Mvav 56,240,196
Mvav 120,244,180
s_nop 0
Tv 220,9,1536
Tv 222,10,1792
Mvav 124,244,184
Mvav 128,244,188
Mvav 132,244,192
Mvav 136,244,196
WL 2
Tv 224,9,1544
Tv 226,10,1800
Mvav 60,248,180
Mvav 64,248,184
Mvav 68,248,188
Mvav 72,248,192
Mvav 76,248,196
Mvav 140,252,180
Mvav 144,252,184
Mvav 148,252,188
Mvav 152,252,192
Mvav 156,252,196
s_waitcnt vmcnt(12) lgkmcnt(0)
s_barrier
Mvvv 80,220,180
s_add_u32 m0,s76,40960
Ta 240,9,16384
Ta 242,10,16640
Mvvv 84,220,184
s_nop 0
Ta 244,9,16392
Ta 246,10,16648
Mvvv 88,220,188
s_cmp_eq_u32 s88,2
s_cselect_b32 s86,0x20000,0
v_subrev_u32 v1,s86,v1
Mvvv 92,220,192
v_add_u32 v1,8192,v1
Mvvv 96,220,196
LN 1,36,58,0
Mvvv 160,224,180
s_nop 0
Ta 248,9,16896
Ta 250,10,17152
Mvvv 164,224,184
Mvvv 168,224,188
Mvvv 172,224,192
LN 1,36,59,1024
Mvvv 176,224,196
WL 2
Rv 180,11,20480
Rv 184,11,22528
Mvav 20,240,200
Ta 252,9,16904
Mvav 24,240,204
s_nop 0
Ta 254,10,17160
Rv 188,11,24576
Mvav 28,240,208
LN 1,36,60,2048
Mvav 32,240,212
s_nop 0
Rv 192,11,26624
Rv 196,11,28672
Mvav 36,240,216
Mvav 100,244,200
s_nop 0
Tv 220,9,17408
Tv 222,10,17664
Mvav 104,244,204
LN 1,36,61,3072
Mvav 108,244,208
Mvav 112,244,212
Mvav 116,244,216
WL 4
Mvav 40,248,200
s_nop 0
Tv 224,9,17416
Tv 226,10,17672
Mvav 44,248,204
Mvav 48,248,208
Mvav 52,248,212
Mvav 56,248,216
Mvav 120,252,200
s_nop 0
Ta 240,9,17920
Ta 242,10,18176
Mvav 124,252,204
Mvav 128,252,208
Mvav 132,252,212
Mvav 136,252,216
WL 2
Ta 244,9,17928
Ta 246,10,18184
Mvvv 60,220,200
Mvvv 64,220,204
Mvvv 68,220,208
Mvvv 72,220,212
Mvvv 76,220,216
Mvvv 140,224,200
Mvvv 144,224,204
Mvvv 148,224,208
Mvvv 152,224,212
Mvvv 156,224,216
s_waitcnt vmcnt(12) lgkmcnt(0)
s_barrier
Mvav 80,240,200
s_add_u32 m0,s76,57344
Ta 248,9,32768
Ta 250,10,33024
Mvav 84,240,204
s_nop 0
Ta 252,9,32776
Ta 254,10,33032
Mvav 88,240,208
v_add_u32 v3,512,v3
Mvav 92,240,212
LD 3,28,64,0
Mvav 96,240,216
Mvav 160,244,200
s_nop 0
Tv 220,9,33280
Tv 222,10,33536
Mvav 164,244,204
Mvav 168,244,208
LD 3,28,66,2048
Mvav 172,244,212
s_add_u32 m0,s76,58368
v_subrev_u32 v3,262144,v3
Mvav 176,244,216
WL 2
Rv 200,12,20480
Rv 204,12,22528
Mvav 20,248,180
Tv 224,9,33288
Mvav 24,248,184
s_nop 0
Tv 226,10,33544
Rv 208,12,24576
Mvav 28,248,188
LD 3,28,64,0
Mvav 32,248,192
s_nop 0
Rv 212,12,26624
Rv 216,12,28672
Mvav 36,248,196
Mvav 100,252,180
s_nop 0
Ta 240,9,33792
Ta 242,10,34048
Mvav 104,252,184
LD 3,28,66,2048
Mvav 108,252,188
Mvav 112,252,192
Mvav 116,252,196
WL 4
Mvvv 40,220,180
s_nop 0
Ta 244,9,33800
Ta 246,10,34056
Mvvv 44,220,184
Mvvv 48,220,188
Mvvv 52,220,192
Mvvv 56,220,196
Mvvv 120,224,180
s_nop 0
Ta 248,9,34304
Ta 250,10,34560
Mvvv 124,224,184
Mvvv 128,224,188
Mvvv 132,224,192
Mvvv 136,224,196
WL 2
Ta 252,9,34312
Ta 254,10,34568
Mvav 60,240,180
Mvav 64,240,184
Mvav 68,240,188
Mvav 72,240,192
Mvav 76,240,196
Mvav 140,244,180
Mvav 144,244,184
Mvav 148,244,188
Mvav 152,244,192
Mvav 156,244,196
s_waitcnt vmcnt(8) lgkmcnt(0)
s_barrier
Mvav 80,248,180
s_add_u32 m0,s76,73728
Tv 220,9,49152
Tv 222,10,49408
Mvav 84,248,184
s_nop 0
Tv 224,9,49160
Tv 226,10,49416
Mvav 88,248,188
s_cmp_le_u32 s89,7
s_cselect_b64 s[28:29],s[70:71],%3
v_subrev_u32 v3,262144,v3
Mvav 92,248,192
LD 3,28,64,0
Mvav 96,248,196
Mvav 160,252,180
s_nop 0
Ta 240,9,49664
Ta 242,10,49920
Mvav 164,252,184
Mvav 168,252,188
LD 3,28,66,2048
Mvav 172,252,192
s_add_u32 m0,s76,74752
v_subrev_u32 v3,262144,v3
Mvav 176,252,196
WL 2
Mvvv 20,220,200
s_nop 0
Ta 244,9,49672
Ta 246,10,49928
Mvvv 24,220,204
LD 3,28,64,0
Mvvv 28,220,208
Mvvv 32,220,212
Mvvv 36,220,216
Mvvv 100,224,200
s_nop 0
Ta 248,9,50176
Ta 250,10,50432
Mvvv 104,224,204
LD 3,28,66,2048
Mvvv 108,224,208
s_cmp_le_u32 s89,6
s_cselect_b64 s[24:25],s[68:69],%2
v_subrev_u32 v0,256,v0
Mvvv 112,224,212
s_add_u32 m0,s77,118784
Mvvv 116,224,216
WL 2
v_mul_f32 v20,s85,v20
v_mul_f32 v21,s85,v21
Mvav 40,240,200
s_nop 0
Ta 252,9,50184
Ta 254,10,50440
Mvav 44,240,204
LD 0,24,49,2048
Mvav 48,240,208
s_add_u32 m0,s77,122880
v_mul_f32 v22,s85,v22
v_mul_f32 v23,s85,v23
Mvav 52,240,212
v_mul_f32 v100,s85,v100
v_mul_f32 v101,s85,v101
Mvav 56,240,216
v_mul_f32 v102,s85,v102
v_mul_f32 v103,s85,v103
Mvav 120,244,200
s_nop 0
Tv 220,9,50688
Tv 222,10,50944
Mvav 124,244,204
LD 0,24,51,2048
Mvav 128,244,208
s_add_u32 m0,s77,126976
CV 20,20,21
CV 21,22,23
Mvav 132,244,212
CV 22,100,101
CV 23,102,103
Mvav 136,244,216
WL 2
Tv 224,9,50696
Tv 226,10,50952
Mvav 60,248,200
LD 0,24,53,2048
Mvav 64,248,204
s_add_u32 m0,s77,131072
v_mul_f32 v24,s85,v24
v_mul_f32 v25,s85,v25
Mvav 68,248,208
v_mul_f32 v26,s85,v26
v_mul_f32 v27,s85,v27
Mvav 72,248,212
v_mul_f32 v104,s85,v104
v_mul_f32 v105,s85,v105
Mvav 76,248,216
LD 0,24,55,2048
Mvav 140,252,200
s_add_u32 m0,s77,135168
v_mul_f32 v106,s85,v106
v_mul_f32 v107,s85,v107
Mvav 144,252,204
CV 24,24,25
CV 25,26,27
Mvav 148,252,208
CV 26,104,105
CV 27,106,107
Mvav 152,252,212
LD 0,24,57,2048
Mvav 156,252,216
s_waitcnt vmcnt(9) lgkmcnt(0)
s_barrier
Mvvv 80,220,200
s_add_u32 m0,s76,90112
Ta 240,9,16384
Ta 242,10,16640
Mvvv 84,220,204
s_nop 0
Ta 244,9,16392
Ta 246,10,16648
Mvvv 88,220,208
s_cmp_eq_u32 s88,1
s_cselect_b32 s86,0x20000,0
v_subrev_u32 v2,s86,v2
Mvvv 92,220,212
s_nop 0
v_add_u32 v2,8192,v2
v_mul_f32 v28,s85,v28
Mvvv 96,220,216
LN 2,96,62,0
Mvvv 160,224,200
s_nop 0
Ta 248,9,16896
Ta 250,10,17152
Mvvv 164,224,204
s_nop 0
v_mul_f32 v29,s85,v29
v_mul_f32 v30,s85,v30
Mvvv 168,224,208
s_nop 0
v_mul_f32 v31,s85,v31
v_mul_f32 v108,s85,v108
Mvvv 172,224,212
s_nop 0
v_mul_f32 v109,s85,v109
v_mul_f32 v110,s85,v110
v_mul_f32 v111,s85,v111
CV 28,28,29
CV 29,30,31
CV 30,108,109
CV 31,110,111
v_mul_f32 v32,s85,v32
v_mul_f32 v33,s85,v33
v_mul_f32 v34,s85,v34
v_mul_f32 v35,s85,v35
v_mul_f32 v112,s85,v112
v_mul_f32 v113,s85,v113
v_mul_f32 v114,s85,v114
v_mul_f32 v115,s85,v115
CV 32,32,33
CV 33,34,35
CV 34,112,113
CV 35,114,115
v_mul_f32 v36,s85,v36
v_mul_f32 v37,s85,v37
v_mul_f32 v38,s85,v38
v_mul_f32 v39,s85,v39
v_mul_f32 v116,s85,v116
v_mul_f32 v117,s85,v117
v_mul_f32 v118,s85,v118
v_mul_f32 v119,s85,v119
CV 36,36,37
CV 37,38,39
CV 38,116,117
CV 39,118,119
AW 160,20
AW 161,21
AW 162,22
AW 163,23
AW 164,24
AW 165,25
AW 166,26
AW 167,27
AW 168,28
AW 169,29
AW 170,30
AW 171,31
AW 172,32
AW 173,33
AW 174,34
AW 175,35
AW 176,36
AW 177,37
AW 178,38
AW 179,39
Mvvv 176,224,216
WL 2
LN 2,96,63,1024
Zvav 20,240,200
s_nop 0
Ta 252,9,16904
Ta 254,10,17160
Zvav 24,240,204
s_nop 0
v_mul_f32 v40,s85,v40
v_mul_f32 v41,s85,v41
Zvav 28,240,208
s_nop 0
v_mul_f32 v42,s85,v42
v_mul_f32 v43,s85,v43
Zvav 32,240,212
LN 2,96,62,2048
Zvav 36,240,216
v_mul_f32 v120,s85,v120
v_mul_f32 v121,s85,v121
Zvav 100,244,200
s_nop 0
Tv 220,9,17408
Tv 222,10,17664
Zvav 104,244,204
v_mul_f32 v122,s85,v122
v_mul_f32 v123,s85,v123
Zvav 108,244,208
LN 2,96,63,3072
Zvav 112,244,212
CV 40,40,41
CV 41,42,43
CV 42,120,121
CV 43,122,123
v_mul_f32 v44,s85,v44
v_mul_f32 v45,s85,v45
v_mul_f32 v46,s85,v46
v_mul_f32 v47,s85,v47
v_mul_f32 v124,s85,v124
v_mul_f32 v125,s85,v125
v_mul_f32 v126,s85,v126
v_mul_f32 v127,s85,v127
CV 44,44,45
CV 45,46,47
CV 46,124,125
CV 47,126,127
v_mul_f32 v48,s85,v48
v_mul_f32 v49,s85,v49
v_mul_f32 v50,s85,v50
v_mul_f32 v51,s85,v51
v_mul_f32 v128,s85,v128
v_mul_f32 v129,s85,v129
v_mul_f32 v130,s85,v130
v_mul_f32 v131,s85,v131
CV 48,48,49
CV 49,50,51
CV 50,128,129
CV 51,130,131
v_mul_f32 v52,s85,v52
v_mul_f32 v53,s85,v53
v_mul_f32 v54,s85,v54
v_mul_f32 v55,s85,v55
v_mul_f32 v132,s85,v132
v_mul_f32 v133,s85,v133
v_mul_f32 v134,s85,v134
v_mul_f32 v135,s85,v135
CV 52,52,53
CV 53,54,55
CV 54,132,133
CV 55,134,135
v_mul_f32 v56,s85,v56
v_mul_f32 v57,s85,v57
v_mul_f32 v58,s85,v58
v_mul_f32 v59,s85,v59
v_mul_f32 v136,s85,v136
v_mul_f32 v137,s85,v137
v_mul_f32 v138,s85,v138
v_mul_f32 v139,s85,v139
CV 56,56,57
CV 57,58,59
CV 58,136,137
CV 59,138,139
AW 180,40
AW 181,41
AW 182,42
AW 183,43
AW 184,44
AW 185,45
AW 186,46
AW 187,47
AW 188,48
AW 189,49
AW 190,50
AW 191,51
AW 192,52
AW 193,53
AW 194,54
AW 195,55
AW 196,56
AW 197,57
AW 198,58
AW 199,59
Zvav 116,244,216
WL 2
v_mul_f32 v60,s85,v60
v_mul_f32 v61,s85,v61
Zvav 40,248,200
s_nop 0
Tv 224,9,17416
Tv 226,10,17672
Zvav 44,248,204
v_mul_f32 v62,s85,v62
v_mul_f32 v63,s85,v63
Zvav 48,248,208
v_mul_f32 v140,s85,v140
v_mul_f32 v141,s85,v141
Zvav 52,248,212
v_mul_f32 v142,s85,v142
v_mul_f32 v143,s85,v143
Zvav 56,248,216
CV 60,60,61
CV 61,62,63
Zvav 120,252,200
s_nop 0
Ta 240,9,17920
Ta 242,10,18176
Zvav 124,252,204
CV 62,140,141
CV 63,142,143
Zvav 128,252,208
v_mul_f32 v64,s85,v64
v_mul_f32 v65,s85,v65
Zvav 132,252,212
v_mul_f32 v66,s85,v66
v_mul_f32 v67,s85,v67
Zvav 136,252,216
WL 2
Ta 244,9,17928
Ta 246,10,18184
Mvvv 20,220,180
s_nop 0
Rv 200,12,40960
Rv 204,12,43008
Mvvv 24,220,184
Rv 208,12,45056
v_mul_f32 v144,s85,v144
Mvvv 28,220,188
Rv 212,12,47104
v_mul_f32 v145,s85,v145
Mvvv 32,220,192
Rv 216,12,49152
v_mul_f32 v146,s85,v146
Mvvv 36,220,196
v_mul_f32 v147,s85,v147
CV 64,64,65
Mvvv 100,224,180
CV 65,66,67
CV 66,144,145
Mvvv 104,224,184
CV 67,146,147
v_mul_f32 v68,s85,v68
Mvvv 108,224,188
v_mul_f32 v69,s85,v69
v_mul_f32 v70,s85,v70
Mvvv 112,224,192
v_mul_f32 v71,s85,v71
v_mul_f32 v148,s85,v148
Mvvv 116,224,196
s_waitcnt vmcnt(9) lgkmcnt(2)
s_barrier
Mvav 40,240,180
s_add_u32 m0,s76,57344
Ta 248,9,32768
Ta 250,10,33024
Mvav 44,240,184
s_nop 0
Ta 252,9,32776
Ta 254,10,33032
Mvav 48,240,188
s_cmp_le_u32 s89,6
s_cselect_b64 s[28:29],s[70:71],%3
v_subrev_u32 v3,262144,v3
Mvav 52,240,192
LD 3,28,64,0
Mvav 56,240,196
v_mul_f32 v149,s85,v149
v_mul_f32 v150,s85,v150
Mvav 120,244,180
s_nop 0
Tv 220,9,33280
Tv 222,10,33536
Mvav 124,244,184
v_mul_f32 v151,s85,v151
CV 68,68,69
Mvav 128,244,188
LD 3,28,66,2048
Mvav 132,244,192
s_add_u32 m0,s76,58368
v_subrev_u32 v3,262144,v3
CV 69,70,71
Mvav 136,244,196
WL 2
Rv 180,11,40960
Rv 184,11,43008
Mvav 20,248,200
Tv 224,9,33288
CV 70,148,149
Mvav 24,248,204
s_nop 0
Tv 226,10,33544
Rv 188,11,45056
Mvav 28,248,208
LD 3,28,64,0
Mvav 32,248,212
s_nop 0
Rv 192,11,47104
Rv 196,11,49152
Mvav 36,248,216
CV 71,150,151
v_mul_f32 v72,s85,v72
Mvav 100,252,200
s_nop 0
Ta 240,9,33792
Ta 242,10,34048
Mvav 104,252,204
LD 3,28,66,2048
Mvav 108,252,208
v_mul_f32 v73,s85,v73
v_mul_f32 v74,s85,v74
Mvav 112,252,212
v_mul_f32 v75,s85,v75
v_mul_f32 v152,s85,v152
Mvav 116,252,216
WL 4
v_mul_f32 v153,s85,v153
v_mul_f32 v154,s85,v154
Mvvv 40,220,200
s_nop 0
Ta 244,9,33800
Ta 246,10,34056
Mvvv 44,220,204
v_mul_f32 v155,s85,v155
CV 72,72,73
Mvvv 48,220,208
CV 73,74,75
CV 74,152,153
Mvvv 52,220,212
CV 75,154,155
v_mul_f32 v76,s85,v76
Mvvv 56,220,216
v_mul_f32 v77,s85,v77
v_mul_f32 v78,s85,v78
Mvvv 120,224,200
s_nop 0
Ta 248,9,34304
Ta 250,10,34560
Mvvv 124,224,204
v_mul_f32 v79,s85,v79
v_mul_f32 v156,s85,v156
Mvvv 128,224,208
v_mul_f32 v157,s85,v157
v_mul_f32 v158,s85,v158
Mvvv 132,224,212
v_mul_f32 v159,s85,v159
CV 76,76,77
Mvvv 136,224,216
WL 2
Ta 252,9,34312
Ta 254,10,34568
Mvav 20,240,180
CV 77,78,79
CV 78,156,157
Mvav 24,240,184
CV 79,158,159
AW 200,60
Mvav 28,240,188
AW 201,61
AW 202,62
Mvav 32,240,192
AW 203,63
AW 204,64
Mvav 36,240,196
AW 205,65
AW 206,66
Mvav 100,244,180
AW 207,67
AW 208,68
Mvav 104,244,184
AW 209,69
AW 210,70
Mvav 108,244,188
AW 211,71
AW 212,72
Mvav 112,244,192
AW 213,73
AW 214,74
Mvav 116,244,196
s_waitcnt vmcnt(2) lgkmcnt(0)
s_barrier
Mvav 40,248,180
s_nop 0
Rv 220,6,0
Rv 224,7,0
Mvav 44,248,184
s_cmp_le_u32 s89,5
s_cselect_b64 s[24:25],s[68:69],%2
Ra 240,6,1024
Mvav 48,248,188
s_nop 0
Ra 244,7,1024
v_subrev_u32 v0,128,v0
Mvav 52,248,192
s_add_u32 m0,s77,139264
Rv 200,12,20480
AW 215,75
Mvav 56,248,196
LD 0,24,49,2048
Mvav 120,252,180
s_add_u32 m0,s77,143360
Rv 204,12,22528
Rv 208,12,24576
Mvav 124,252,184
s_nop 0
Rv 212,12,26624
AW 216,76
Mvav 128,252,188
s_nop 0
AW 217,77
AW 218,78
AW 219,79
v_mul_f32 v80,s85,v80
v_mul_f32 v81,s85,v81
v_mul_f32 v82,s85,v82
v_mul_f32 v83,s85,v83
v_mul_f32 v160,s85,v160
v_mul_f32 v161,s85,v161
v_mul_f32 v162,s85,v162
v_mul_f32 v163,s85,v163
CV 80,80,81
CV 81,82,83
CV 82,160,161
CV 83,162,163
v_mul_f32 v84,s85,v84
v_mul_f32 v85,s85,v85
v_mul_f32 v86,s85,v86
v_mul_f32 v87,s85,v87
v_mul_f32 v164,s85,v164
v_mul_f32 v165,s85,v165
v_mul_f32 v166,s85,v166
v_mul_f32 v167,s85,v167
CV 84,84,85
CV 85,86,87
CV 86,164,165
CV 87,166,167
v_mul_f32 v88,s85,v88
v_mul_f32 v89,s85,v89
v_mul_f32 v90,s85,v90
v_mul_f32 v91,s85,v91
v_mul_f32 v168,s85,v168
v_mul_f32 v169,s85,v169
v_mul_f32 v170,s85,v170
v_mul_f32 v171,s85,v171
CV 88,88,89
CV 89,90,91
CV 90,168,169
CV 91,170,171
v_mul_f32 v92,s85,v92
v_mul_f32 v93,s85,v93
v_mul_f32 v94,s85,v94
v_mul_f32 v95,s85,v95
v_mul_f32 v172,s85,v172
v_mul_f32 v173,s85,v173
v_mul_f32 v174,s85,v174
v_mul_f32 v175,s85,v175
CV 92,92,93
CV 93,94,95
CV 94,172,173
CV 95,174,175
v_mul_f32 v96,s85,v96
v_mul_f32 v97,s85,v97
v_mul_f32 v98,s85,v98
v_mul_f32 v99,s85,v99
v_mul_f32 v176,s85,v176
v_mul_f32 v177,s85,v177
v_mul_f32 v178,s85,v178
v_mul_f32 v179,s85,v179
CV 96,96,97
CV 97,98,99
CV 98,176,177
CV 99,178,179
AW 220,80
AW 221,81
AW 222,82
AW 223,83
AW 224,84
AW 225,85
AW 226,86
AW 227,87
AW 228,88
AW 229,89
AW 230,90
AW 231,91
AW 232,92
AW 233,93
AW 234,94
AW 235,95
AW 236,96
AW 237,97
AW 238,98
AW 239,99
Mvav 132,252,192
LD 0,24,51,2048
Mvav 136,252,196
WL 5
s_add_u32 m0,s77,147456
Rv 216,12,28672
Zvva 140,220,160
s_nop 0
Rv 180,11,20480
Rv 184,11,22528
Mvva 140,224,180
s_nop 0
Rv 188,11,24576
Mvaa 140,240,200
WL 7
LD 0,24,53,2048
Mvaa 140,244,220
s_add_u32 m0,s77,151552
Rv 192,11,26624
Rv 196,11,28672
Zvva 144,220,164
s_nop 0
Mvva 144,224,184
s_nop 0
Ta 248,9,16384
Ta 250,10,16640
Mvaa 144,240,204
LD 0,24,55,2048
Mvaa 144,244,224
s_add_u32 m0,s77,155648
Zvva 148,220,168
s_nop 0
Mvva 148,224,188
Mvaa 148,240,208
s_nop 0
Ta 252,9,16392
Ta 254,10,16648
Mvaa 148,244,228
LD 0,24,57,2048
Zvva 152,220,172
s_add_u32 m0,s76,73728
s_cmp_eq_u32 s88,2
s_cselect_b32 s86,0x20000,0
Mvva 152,224,192
s_nop 0
v_subrev_u32 v2,s86,v2
v_add_u32 v2,8192,v2
Mvaa 152,240,212
s_nop 0
Mvaa 152,244,232
LN 2,96,62,0
Zvva 156,220,176
s_nop 0
Tv 220,9,16896
Tv 222,10,17152
Mvva 156,224,196
Mvaa 156,240,216
Mvaa 156,244,236
WL 2
Tv 224,9,16904
Tv 226,10,17160
Mvav 20,248,200
LN 2,96,63,1024
Mvav 24,248,204
Mvav 28,248,208
v_max3_f32 v80,v140,v141,v142
v_max3_f32 v84,v144,v145,v146
Mvav 32,248,212
v_max3_f32 v88,v148,v149,v150
v_max3_f32 v92,v152,v153,v154
Mvav 36,248,216
LN 2,96,62,2048
Mvav 100,252,200
Ra 240,6,2048
v_max3_f32 v96,v156,v157,v158
Mvav 104,252,204
Ra 244,7,2048
v_max_f32 v81,v80,v143
Mvav 108,252,208
Ra 248,6,3072
v_max_f32 v80,v80,v143
Mvav 112,252,212
LN 2,96,63,3072
Mvav 116,252,216
s_waitcnt vmcnt(9) lgkmcnt(3)
s_barrier
Mvvv 40,220,200
s_add_u32 m0,s76,57344
Ra 252,7,3072
v_max_f32 v85,v84,v147
Mvvv 44,220,204
s_cmp_le_u32 s89,5
s_cselect_b64 s[28:29],s[70:71],%3
v_subrev_u32 v3,262144,v3
Mvvv 48,220,208
LD 3,28,64,0
Mvvv 52,220,212
s_nop 0
v_max_f32 v84,v84,v147
v_max_f32 v89,v88,v151
Mvvv 56,220,216
s_nop 0
v_max_f32 v88,v88,v151
v_max_f32 v93,v92,v155
Mvvv 120,224,200
s_nop 0
v_max_f32 v92,v92,v155
v_max_f32 v97,v96,v159
Mvvv 124,224,204
LD 3,28,66,2048
Mvvv 128,224,208
s_cmp_le_u32 s89,4
s_cselect_b64 s[24:25],s[68:69],%2
v_subrev_u32 v0,128,v0
Mvvv 132,224,212
s_nop 0
v_max_f32 v96,v96,v159
v_permlane32_swap_b32 v80,v81
v_permlane32_swap_b32 v84,v85
v_permlane32_swap_b32 v88,v89
v_permlane32_swap_b32 v92,v93
v_permlane32_swap_b32 v96,v97
v_max_f32 v82,v80,v81
v_max_f32 v80,v80,v81
v_max_f32 v86,v84,v85
v_max_f32 v84,v84,v85
v_max_f32 v90,v88,v89
v_max_f32 v88,v88,v89
v_max_f32 v94,v92,v93
v_max_f32 v92,v92,v93
v_max_f32 v98,v96,v97
v_max_f32 v96,v96,v97
v_permlane16_swap_b32 v80,v82
v_permlane16_swap_b32 v84,v86
v_permlane16_swap_b32 v88,v90
v_permlane16_swap_b32 v92,v94
v_permlane16_swap_b32 v96,v98
v_min_f32_e64 v81,-v80,-v82
v_min_f32_e64 v85,-v84,-v86
v_min_f32_e64 v89,-v88,-v90
v_min_f32_e64 v93,-v92,-v94
v_min_f32_e64 v97,-v96,-v98
v_min_f32_e64 v83,-v80,-v82
v_min_f32_e64 v87,-v84,-v86
v_min_f32_e64 v91,-v88,-v90
v_min_f32_e64 v95,-v92,-v94
v_min_f32_e64 v99,-v96,-v98
v_min_f32_e64 v80,-v80,-v82
v_min_f32_e64 v84,-v84,-v86
v_min_f32_e64 v88,-v88,-v90
v_min_f32_e64 v92,-v92,-v94
v_min_f32_e64 v96,-v96,-v98
v_mov_b32 v82,v80
v_mov_b32 v86,v84
v_mov_b32 v90,v88
v_mov_b32 v94,v92
v_mov_b32 v98,v96
Mvvv 136,224,216
WL 0
s_add_u32 m0,s77,118784
v_add_f32 v140,v140,v80
Nvaa 160,240,160,80
LD 0,24,49,2048
Mvaa 160,244,180
s_add_u32 m0,s77,122880
v_add_f32 v141,v141,v80
Mvaa 160,248,200
s_nop 0
v_add_f32 v142,v142,v80
v_add_f32 v143,v143,v80
Mvaa 160,252,220
s_nop 0
EX 140
Nvaa 164,240,164,84
LD 0,24,51,2048
Mvaa 164,244,184
s_add_u32 m0,s77,126976
Tv 220,9,17408
Tv 222,10,17664
Mvaa 164,248,204
s_nop 0
EX 141
Mvaa 164,252,224
EX 142
Nvaa 168,240,168,88
LD 0,24,53,2048
Mvaa 168,244,188
s_add_u32 m0,s77,131072
EX 143
Mvaa 168,248,208
s_nop 0
Tv 224,9,17416
Tv 226,10,17672
Mvaa 168,252,228
s_nop 0
CV 140,140,141
CV 141,142,143
Nvaa 172,240,172,92
LD 0,24,55,2048
Mvaa 172,244,192
s_add_u32 m0,s77,135168
v_add_f32 v144,v144,v84
v_add_f32 v145,v145,v84
Mvaa 172,248,212
v_add_f32 v146,v146,v84
v_add_f32 v147,v147,v84
Mvaa 172,252,232
EX 144
Nvaa 176,240,176,96
s_nop 0
Ta 240,9,17920
Ta 242,10,18176
Mvaa 176,244,196
LD 0,24,57,2048
Mvaa 176,248,216
EX 145
Mvaa 176,252,236
WL 2
Ta 244,9,17928
Ta 246,10,18184
Mvvv 20,220,180
EX 146
Mvvv 24,220,184
EX 147
Mvvv 28,220,188
CV 144,144,145
CV 145,146,147
Mvvv 32,220,192
v_add_f32 v148,v148,v88
v_add_f32 v149,v149,v88
Mvvv 36,220,196
v_add_f32 v150,v150,v88
v_add_f32 v151,v151,v88
Mvvv 100,224,180
EX 148
Mvvv 104,224,184
EX 149
Mvvv 108,224,188
EX 150
Mvvv 112,224,192
EX 151
Mvvv 116,224,196
s_waitcnt vmcnt(5) lgkmcnt(0)
s_barrier
Mvav 40,240,180
s_add_u32 m0,s76,58368
Ra 248,6,16384
Ra 252,7,16384
Mvav 44,240,184
s_nop 0
Rv 220,6,17408
v_subrev_u32 v3,262144,v3
Mvav 48,240,188
Rv 224,7,17408
CV 148,148,149
Mvav 52,240,192
LD 3,28,64,0
Mvav 56,240,196
s_nop 0
Rv 200,12,40960
Rv 204,12,43008
Mvav 120,244,180
Rv 208,12,45056
CV 149,150,151
Mvav 124,244,184
Rv 212,12,47104
v_add_f32 v152,v152,v92
Mvav 128,244,188
LD 3,28,66,2048
Mvav 132,244,192
v_add_f32 v153,v153,v92
v_add_f32 v154,v154,v92
v_add_f32 v155,v155,v92
EX 152
EX 153
EX 154
EX 155
CV 152,152,153
CV 153,154,155
v_add_f32 v156,v156,v96
v_add_f32 v157,v157,v96
v_add_f32 v158,v158,v96
v_add_f32 v159,v159,v96
EX 156
EX 157
EX 158
EX 159
CV 156,156,157
CV 157,158,159
EX 160
EX 161
EX 162
EX 163
CV 142,160,161
CV 143,162,163
Mvav 136,244,196
WL 5
s_add_u32 m0,s76,8192
Rv 216,12,49152
Nvaa 160,248,160,80
s_cmp_eq_u32 s88,3
s_cselect_b32 s86,0x20000,0
Mvaa 160,252,180
v_subrev_u32 v1,s86,v1
v_add_u32 v1,8192,v1
Mvva 160,220,200
EX 164
EX 165
EX 166
EX 167
CV 146,164,165
CV 147,166,167
WL 3
Mvva 160,224,220
LN 1,36,58,0
Nvaa 164,248,164,84
s_nop 0
Rv 180,11,40960
Rv 184,11,43008
Mvaa 164,252,184
Ta 240,9,16384
v_readfirstlane_b32 %0,v240
Mvva 164,220,204
EX 168
EX 169
EX 170
EX 171
CV 150,168,169
CV 151,170,171
Mvva 164,224,224
s_nop 0
Ta 242,10,16640
Rv 188,11,45056
Nvaa 168,248,168,88
LN 1,36,59,1024
Mvaa 168,252,188
Rv 192,11,47104
v_readfirstlane_b32 %1,v241
Mvva 168,220,208
EX 172
EX 173
EX 174
EX 175
CV 154,172,173
CV 155,174,175
Mvva 168,224,228
s_nop 0
Ta 244,9,16392
Ta 246,10,16648
Nvaa 172,248,172,92
LN 1,36,60,2048
Mvaa 172,252,192
Rv 196,11,49152
Mvva 172,220,212
EX 176
EX 177
EX 178
EX 179
CV 158,176,177
CV 159,178,179
Mvva 172,224,232
EX 160
Nvaa 176,248,176,96
s_nop 0
Ta 248,9,16896
Ta 250,10,17152
Mvaa 176,252,196
LN 1,36,61,3072
Mvva 176,220,216
EX 161
Mvva 176,224,236
WL 3
Ta 252,9,16904
Ta 254,10,17160
Mvav 20,240,200
EX 162
Mvav 24,240,204
EX 163
Mvav 28,240,208
CV 160,160,161
CV 161,162,163
Mvav 32,240,212
EX 164
Mvav 36,240,216
EX 165
Mvav 100,244,200
EX 166
Mvav 104,244,204
EX 167
Mvav 108,244,208
CV 164,164,165
CV 165,166,167
Mvav 112,244,212
EX 168
Mvav 116,244,216
WL 0
s_barrier
Mvav 40,248,200
s_add_u32 m0,s76,57344
Tv 220,8,49152
Tv 222,8,53248
Mvav 44,248,204
s_cmp_le_u32 s89,4
s_cselect_b64 s[28:29],s[70:71],%3
v_subrev_u32 v3,262144,v3
Mvav 48,248,208
LD 3,28,64,0
Mvav 52,248,212
EX 169
Mvav 56,248,216
EX 170
Mvav 120,252,200
s_nop 0
Tv 224,252,49152
Tv 226,252,53248
Mvav 124,252,204
LD 3,28,66,2048
Mvav 128,252,208
s_cmp_le_u32 s89,3
s_cselect_b64 s[24:25],s[68:69],%2
v_subrev_u32 v0,128,v0
Mvav 132,252,212
s_add_u32 m0,s77,139264
EX 171
Mvav 136,252,216
CV 168,168,169
CV 169,170,171
Zvvv 228,248,140
s_nop 0
Ta 240,8,49664
Ta 242,8,53760
Zvvv 232,248,144
LD 0,24,49,2048
Zvvv 236,248,148
s_add_u32 m0,s77,143360
EX 172
Zvvv 240,248,152
EX 173
Zvvv 244,248,156
WL 2
EX 174
Zavv 0,220,140
s_nop 0
Ta 244,252,49664
Ta 246,252,53760
Zavv 4,220,144
LD 0,24,51,2048
Zavv 8,220,148
s_add_u32 m0,s77,147456
EX 175
Zavv 12,220,152
CV 172,172,173
CV 173,174,175
Zavv 16,220,156
EX 176
Zavv 20,224,140
s_nop 0
Ta 248,8,50176
Ta 250,8,54272
Zavv 24,224,144
LD 0,24,53,2048
Zavv 28,224,148
s_add_u32 m0,s77,151552
EX 177
Zavv 32,224,152
EX 178
Zavv 36,224,156
WL 2
EX 179
Zaav 40,240,140
s_nop 0
Ta 252,252,50176
Ta 254,252,54272
Zaav 44,240,144
LD 0,24,55,2048
Zaav 48,240,148
s_add_u32 m0,s77,155648
CV 176,176,177
CV 177,178,179
Zaav 52,240,152
Zaav 56,240,156
Zaav 60,244,140
s_nop 0
Tv 220,8,50688
Tv 222,8,54784
Zaav 64,244,144
LD 0,24,57,2048
Zaav 68,244,148
Zaav 72,244,152
Zaav 76,244,156
WL 2
Tv 224,252,50688
Tv 226,252,54784
Zaav 80,248,140
Zaav 84,248,144
Zaav 88,248,148
Zaav 92,248,152
Zaav 96,248,156
Zaav 100,252,140
Ra 240,6,18432
Zaav 104,252,144
Ra 244,7,18432
Zaav 108,252,148
Ra 248,6,19456
Zaav 112,252,152
Zaav 116,252,156
s_waitcnt vmcnt(11) lgkmcnt(3)
s_barrier
Zavv 120,220,140
s_add_u32 m0,s76,106496
Ra 252,7,19456
Zavv 124,220,144
s_cmp_eq_u32 s88,3
s_cselect_b32 s86,0x20000,0
v_subrev_u32 v2,s86,v2
Zavv 128,220,148
v_add_u32 v2,8192,v2
Zavv 132,220,152
LN 2,96,62,0
Zavv 136,220,156
Zavv 140,224,140
Zavv 144,224,144
Zavv 148,224,148
LN 2,96,63,1024
Zavv 152,224,152
Zavv 156,224,156
WL 0
Nvaa 140,240,160,80
Mvaa 140,244,180
LN 2,96,62,2048
Mvaa 140,248,200
Mvaa 140,252,220
Nvaa 144,240,164,84
Mvaa 144,244,184
s_nop 0
Tv 220,9,17408
Tv 222,10,17664
Mvaa 144,248,204
LN 2,96,63,3072
Mvaa 144,252,224
EX 140
Nvaa 148,240,168,88
EX 141
Mvaa 148,244,188
EX 142
Mvaa 148,248,208
s_nop 0
Tv 224,9,17416
Tv 226,10,17672
Mvaa 148,252,228
EX 143
Nvaa 152,240,172,92
CV 162,140,141
CV 163,142,143
Mvaa 152,244,192
EX 144
Mvaa 152,248,212
EX 145
Mvaa 152,252,232
EX 146
Nvaa 156,240,176,96
s_nop 0
Ta 240,9,17920
Ta 242,10,18176
Mvaa 156,244,196
EX 147
Mvaa 156,248,216
CV 166,144,145
CV 167,146,147
Mvaa 156,252,236
WL 2
Ta 244,9,17928
Ta 246,10,18184
Mvvv 20,220,180
EX 148
Mvvv 24,220,184
EX 149
Mvvv 28,220,188
EX 150
Mvvv 32,220,192
EX 151
Mvvv 36,220,196
CV 170,148,149
CV 171,150,151
Mvvv 100,224,180
EX 152
Mvvv 104,224,184
EX 153
Mvvv 108,224,188
EX 154
Mvvv 112,224,192
EX 155
Mvvv 116,224,196
s_waitcnt vmcnt(9) lgkmcnt(0)
s_barrier
Mvav 40,240,180
s_add_u32 m0,s76,58368
Ra 248,6,32768
Ra 252,7,32768
Mvav 44,240,184
)ASM"
R"ASM(s_nop 0
Rv 220,6,33792
v_subrev_u32 v3,262144,v3
Mvav 48,240,188
s_nop 0
Rv 224,7,33792
CV 174,152,153
Mvav 52,240,192
LD 3,28,64,0
Mvav 56,240,196
s_nop 0
Rv 200,12,20480
Rv 204,12,22528
Mvav 120,244,180
s_nop 0
Rv 208,12,24576
CV 175,154,155
Mvav 124,244,184
s_nop 0
Rv 212,12,26624
Mvav 128,244,188
LD 3,28,66,2048
Mvav 132,244,192
s_add_u32 m0,s76,24576
s_cmp_eq_u32 s88,4
s_cselect_b32 s86,0x20000,0
Mvav 136,244,196
WL 5
v_subrev_u32 v1,s86,v1
v_add_u32 v1,8192,v1
Nvaa 140,248,160,80
s_nop 0
Rv 216,12,28672
Rv 180,11,20480
Mvaa 140,252,180
LN 1,36,58,0
Mvva 140,220,200
WL 4
Rv 184,11,22528
Rv 188,11,24576
Mvva 140,224,220
s_nop 0
Rv 192,11,26624
Nvaa 144,248,164,84
Rv 196,11,28672
Mvaa 144,252,184
Ta 240,9,16384
Mvva 144,220,204
Ta 242,10,16640
Mvva 144,224,224
LN 1,36,59,1024
Nvaa 148,248,168,88
EX 156
Mvaa 148,252,188
EX 157
Mvva 148,220,208
s_nop 0
Ta 244,9,16392
Ta 246,10,16648
Mvva 148,224,228
LN 1,36,60,2048
Nvaa 152,248,172,92
EX 158
Mvaa 152,252,192
EX 159
Mvva 152,220,212
CV 178,156,157
CV 179,158,159
Mvva 152,224,232
LN 1,36,61,3072
Nvaa 156,248,176,96
s_nop 0
Ta 248,9,16896
Ta 250,10,17152
Mvaa 156,252,196
EX 140
Mvva 156,220,216
EX 141
Mvva 156,224,236
WL 2
Ta 252,9,16904
Ta 254,10,17160
Mvav 20,240,200
EX 142
Mvav 24,240,204
EX 143
Mvav 28,240,208
CV 140,140,141
CV 141,142,143
Mvav 32,240,212
EX 144
Mvav 36,240,216
EX 145
Mvav 100,244,200
EX 146
Mvav 104,244,204
EX 147
Mvav 108,244,208
CV 144,144,145
CV 145,146,147
Mvav 112,244,212
EX 148
Mvav 116,244,216
WL 0
s_barrier
Mvav 40,248,200
s_add_u32 m0,s76,57344
Tv 220,8,32768
Tv 222,8,36864
Mvav 44,248,204
s_cmp_le_u32 s89,3
s_cselect_b64 s[28:29],s[70:71],%3
v_subrev_u32 v3,262144,v3
Mvav 48,248,208
LD 3,28,64,0
Mvav 52,248,212
s_nop 0
EX 149
Mvav 56,248,216
EX 150
Mvav 120,252,200
s_nop 0
Tv 224,252,32768
Tv 226,252,36864
Mvav 124,252,204
LD 3,28,66,2048
Mvav 128,252,208
s_cmp_le_u32 s89,2
s_cselect_b64 s[24:25],s[68:69],%2
v_subrev_u32 v0,128,v0
Mvav 132,252,212
s_add_u32 m0,s77,118784
EX 151
Mvav 136,252,216
s_nop 0
CV 148,148,149
CV 149,150,151
Mvvv 228,248,160
s_nop 0
Ta 240,8,33280
Ta 242,8,37376
Mvvv 232,248,164
LD 0,24,49,2048
Mvvv 236,248,168
s_add_u32 m0,s77,122880
EX 152
Mvvv 240,248,172
s_nop 0
EX 153
Mvvv 244,248,176
WL 2
EX 154
Mavv 0,220,160
s_nop 0
Ta 244,252,33280
Ta 246,252,37376
Mavv 4,220,164
LD 0,24,51,2048
Mavv 8,220,168
s_add_u32 m0,s77,126976
EX 155
Mavv 12,220,172
s_nop 0
CV 152,152,153
CV 153,154,155
Mavv 16,220,176
s_nop 0
EX 156
Mavv 20,224,160
s_nop 0
Ta 248,8,33792
Ta 250,8,37888
Mavv 24,224,164
LD 0,24,53,2048
Mavv 28,224,168
s_add_u32 m0,s77,131072
EX 157
Mavv 32,224,172
EX 158
Mavv 36,224,176
WL 2
EX 159
Maav 40,240,160
s_nop 0
Ta 252,252,33792
Ta 254,252,37888
Maav 44,240,164
LD 0,24,55,2048
Maav 48,240,168
s_add_u32 m0,s77,135168
CV 156,156,157
CV 157,158,159
Maav 52,240,172
Maav 56,240,176
Maav 60,244,160
s_nop 0
Tv 220,8,34304
Tv 222,8,38400
Maav 64,244,164
LD 0,24,57,2048
Maav 68,244,168
Maav 72,244,172
Maav 76,244,176
WL 2
Tv 224,252,34304
Tv 226,252,38400
Maav 80,248,160
Maav 84,248,164
Maav 88,248,168
Maav 92,248,172
Maav 96,248,176
Maav 100,252,160
Ra 240,6,34816
Maav 104,252,164
Ra 244,7,34816
Maav 108,252,168
Ra 248,6,35840
Maav 112,252,172
Maav 116,252,176
s_waitcnt vmcnt(11) lgkmcnt(3)
s_barrier
Mavv 120,220,160
s_add_u32 m0,s76,90112
Ra 252,7,35840
Mavv 124,220,164
s_cmp_eq_u32 s88,4
s_cselect_b32 s86,0x20000,0
v_subrev_u32 v2,s86,v2
Mavv 128,220,168
v_add_u32 v2,8192,v2
Mavv 132,220,172
LN 2,96,62,0
Mavv 136,220,176
Mavv 140,224,160
Mavv 144,224,164
Mavv 148,224,168
LN 2,96,63,1024
Mavv 152,224,172
Mavv 156,224,176
WL 0
Nvaa 160,240,160,80
Mvaa 160,244,180
LN 2,96,62,2048
Mvaa 160,248,200
Mvaa 160,252,220
Nvaa 164,240,164,84
Mvaa 164,244,184
s_nop 0
Tv 220,9,17408
Tv 222,10,17664
Mvaa 164,248,204
LN 2,96,63,3072
Mvaa 164,252,224
EX 160
Nvaa 168,240,168,88
EX 161
Mvaa 168,244,188
EX 162
Mvaa 168,248,208
s_nop 0
Tv 224,9,17416
Tv 226,10,17672
Mvaa 168,252,228
EX 163
Nvaa 172,240,172,92
CV 142,160,161
CV 143,162,163
Mvaa 172,244,192
EX 164
Mvaa 172,248,212
EX 165
Mvaa 172,252,232
EX 166
Nvaa 176,240,176,96
s_nop 0
Ta 240,9,17920
Ta 242,10,18176
Mvaa 176,244,196
EX 167
Mvaa 176,248,216
CV 146,164,165
CV 147,166,167
Mvaa 176,252,236
WL 2
Ta 244,9,17928
Ta 246,10,18184
Mvvv 20,220,180
EX 168
Mvvv 24,220,184
EX 169
Mvvv 28,220,188
EX 170
Mvvv 32,220,192
EX 171
Mvvv 36,220,196
CV 150,168,169
CV 151,170,171
Mvvv 100,224,180
EX 172
Mvvv 104,224,184
EX 173
Mvvv 108,224,188
EX 174
Mvvv 112,224,192
EX 175
Mvvv 116,224,196
s_waitcnt vmcnt(9) lgkmcnt(0)
s_barrier
Mvav 40,240,180
s_add_u32 m0,s76,58368
Ra 248,6,0
Ra 252,7,0
Mvav 44,240,184
Rv 220,6,1024
v_subrev_u32 v3,262144,v3
Mvav 48,240,188
Rv 224,7,1024
CV 154,172,173
Mvav 52,240,192
LD 3,28,64,0
Mvav 56,240,196
s_nop 0
Rv 200,12,40960
Rv 204,12,43008
Mvav 120,244,180
Rv 208,12,45056
CV 155,174,175
Mvav 124,244,184
Rv 212,12,47104
Mvav 128,244,188
LD 3,28,66,2048
Mvav 132,244,192
s_add_u32 m0,s76,40960
s_cmp_eq_u32 s88,5
s_cselect_b32 s86,0x20000,0
Mvav 136,244,196
WL 5
v_subrev_u32 v1,s86,v1
v_add_u32 v1,8192,v1
Nvaa 160,248,160,80
s_nop 0
Rv 216,12,49152
Rv 180,11,40960
Mvaa 160,252,180
LN 1,36,58,0
Mvva 160,220,200
WL 4
Rv 184,11,43008
Rv 188,11,45056
Mvva 160,224,220
Rv 192,11,47104
Nvaa 164,248,164,84
Rv 196,11,49152
Mvaa 164,252,184
Ta 240,9,16384
Mvva 164,220,204
Ta 242,10,16640
Mvva 164,224,224
LN 1,36,59,1024
Nvaa 168,248,168,88
EX 176
Mvaa 168,252,188
EX 177
Mvva 168,220,208
s_nop 0
Ta 244,9,16392
Ta 246,10,16648
Mvva 168,224,228
LN 1,36,60,2048
Nvaa 172,248,172,92
EX 178
Mvaa 172,252,192
EX 179
Mvva 172,220,212
CV 158,176,177
CV 159,178,179
Mvva 172,224,232
LN 1,36,61,3072
Nvaa 176,248,176,96
s_nop 0
Ta 248,9,16896
Ta 250,10,17152
Mvaa 176,252,196
EX 160
Mvva 176,220,216
EX 161
Mvva 176,224,236
WL 2
Ta 252,9,16904
Ta 254,10,17160
Mvav 20,240,200
EX 162
Mvav 24,240,204
EX 163
Mvav 28,240,208
CV 160,160,161
CV 161,162,163
Mvav 32,240,212
EX 164
Mvav 36,240,216
EX 165
Mvav 100,244,200
EX 166
Mvav 104,244,204
EX 167
Mvav 108,244,208
CV 164,164,165
CV 165,166,167
Mvav 112,244,212
EX 168
Mvav 116,244,216
WL 0
s_barrier
Mvav 40,248,200
s_add_u32 m0,s76,57344
Tv 220,8,16384
Tv 222,8,20480
Mvav 44,248,204
s_cmp_le_u32 s89,2
s_cselect_b64 s[28:29],s[70:71],%3
v_subrev_u32 v3,262144,v3
Mvav 48,248,208
LD 3,28,64,0
Mvav 52,248,212
EX 169
Mvav 56,248,216
EX 170
Mvav 120,252,200
s_nop 0
Tv 224,252,16384
Tv 226,252,20480
Mvav 124,252,204
LD 3,28,66,2048
Mvav 128,252,208
s_cmp_le_u32 s89,1
s_cselect_b64 s[24:25],s[68:69],%2
v_subrev_u32 v0,128,v0
Mvav 132,252,212
s_add_u32 m0,s77,139264
EX 171
Mvav 136,252,216
CV 168,168,169
CV 169,170,171
Mvvv 228,248,140
s_nop 0
Ta 240,8,16896
Ta 242,8,20992
Mvvv 232,248,144
LD 0,24,49,2048
Mvvv 236,248,148
s_add_u32 m0,s77,143360
EX 172
Mvvv 240,248,152
EX 173
Mvvv 244,248,156
WL 2
EX 174
Mavv 0,220,140
s_nop 0
Ta 244,252,16896
Ta 246,252,20992
Mavv 4,220,144
LD 0,24,51,2048
Mavv 8,220,148
s_add_u32 m0,s77,147456
EX 175
Mavv 12,220,152
CV 172,172,173
CV 173,174,175
Mavv 16,220,156
EX 176
Mavv 20,224,140
s_nop 0
Ta 248,8,17408
Ta 250,8,21504
Mavv 24,224,144
LD 0,24,53,2048
Mavv 28,224,148
s_add_u32 m0,s77,151552
EX 177
Mavv 32,224,152
EX 178
Mavv 36,224,156
WL 2
EX 179
Maav 40,240,140
s_nop 0
Ta 252,252,17408
Ta 254,252,21504
Maav 44,240,144
LD 0,24,55,2048
Maav 48,240,148
s_add_u32 m0,s77,155648
CV 176,176,177
CV 177,178,179
Maav 52,240,152
Maav 56,240,156
Maav 60,244,140
s_nop 0
Tv 220,8,17920
Tv 222,8,22016
Maav 64,244,144
LD 0,24,57,2048
Maav 68,244,148
Maav 72,244,152
Maav 76,244,156
WL 2
Tv 224,252,17920
Tv 226,252,22016
Maav 80,248,140
Maav 84,248,144
Maav 88,248,148
Maav 92,248,152
Maav 96,248,156
Maav 100,252,140
Ra 240,6,2048
Maav 104,252,144
Ra 244,7,2048
Maav 108,252,148
Ra 248,6,3072
Maav 112,252,152
Maav 116,252,156
s_waitcnt vmcnt(11) lgkmcnt(3)
s_barrier
Mavv 120,220,140
s_add_u32 m0,s76,73728
Ra 252,7,3072
Mavv 124,220,144
s_cmp_eq_u32 s88,5
s_cselect_b32 s86,0x20000,0
v_subrev_u32 v2,s86,v2
Mavv 128,220,148
v_add_u32 v2,8192,v2
Mavv 132,220,152
LN 2,96,62,0
Mavv 136,220,156
Mavv 140,224,140
Mavv 144,224,144
Mavv 148,224,148
LN 2,96,63,1024
Mavv 152,224,152
Mavv 156,224,156
WL 0
Nvaa 140,240,160,80
Mvaa 140,244,180
LN 2,96,62,2048
Mvaa 140,248,200
Mvaa 140,252,220
Nvaa 144,240,164,84
Mvaa 144,244,184
s_nop 0
Tv 220,9,17408
Tv 222,10,17664
Mvaa 144,248,204
LN 2,96,63,3072
Mvaa 144,252,224
EX 140
Nvaa 148,240,168,88
EX 141
Mvaa 148,244,188
EX 142
Mvaa 148,248,208
s_nop 0
Tv 224,9,17416
Tv 226,10,17672
Mvaa 148,252,228
EX 143
Nvaa 152,240,172,92
CV 162,140,141
CV 163,142,143
Mvaa 152,244,192
EX 144
Mvaa 152,248,212
EX 145
Mvaa 152,252,232
EX 146
Nvaa 156,240,176,96
s_nop 0
Ta 240,9,17920
Ta 242,10,18176
Mvaa 156,244,196
EX 147
Mvaa 156,248,216
CV 166,144,145
CV 167,146,147
Mvaa 156,252,236
WL 2
Ta 244,9,17928
Ta 246,10,18184
Mvvv 20,220,180
EX 148
Mvvv 24,220,184
EX 149
Mvvv 28,220,188
EX 150
Mvvv 32,220,192
EX 151
Mvvv 36,220,196
CV 170,148,149
CV 171,150,151
Mvvv 100,224,180
EX 152
Mvvv 104,224,184
EX 153
Mvvv 108,224,188
EX 154
Mvvv 112,224,192
EX 155
Mvvv 116,224,196
s_waitcnt vmcnt(9) lgkmcnt(0)
s_barrier
Mvav 40,240,180
s_add_u32 m0,s76,58368
Ra 248,6,16384
Ra 252,7,16384
Mvav 44,240,184
s_nop 0
Rv 220,6,17408
v_subrev_u32 v3,262144,v3
Mvav 48,240,188
s_nop 0
Rv 224,7,17408
CV 174,152,153
Mvav 52,240,192
LD 3,28,64,0
Mvav 56,240,196
s_nop 0
Rv 200,12,20480
Rv 204,12,22528
Mvav 120,244,180
s_nop 0
Rv 208,12,24576
CV 175,154,155
Mvav 124,244,184
Rv 212,12,26624
Mvav 128,244,188
LD 3,28,66,2048
Mvav 132,244,192
s_add_u32 m0,s76,8192
s_cmp_eq_u32 s88,6
s_cselect_b32 s86,0x20000,0
Mvav 136,244,196
WL 5
v_subrev_u32 v1,s86,v1
v_add_u32 v1,8192,v1
Nvaa 140,248,160,80
s_nop 0
Rv 216,12,28672
Rv 180,11,20480
Mvaa 140,252,180
LN 1,36,58,0
Mvva 140,220,200
WL 4
Rv 184,11,22528
Rv 188,11,24576
Mvva 140,224,220
Rv 192,11,26624
Nvaa 144,248,164,84
Rv 196,11,28672
Mvaa 144,252,184
Ta 240,9,16384
Mvva 144,220,204
Ta 242,10,16640
Mvva 144,224,224
LN 1,36,59,1024
Nvaa 148,248,168,88
EX 156
Mvaa 148,252,188
EX 157
Mvva 148,220,208
s_nop 0
Ta 244,9,16392
Ta 246,10,16648
Mvva 148,224,228
LN 1,36,60,2048
Nvaa 152,248,172,92
EX 158
Mvaa 152,252,192
EX 159
Mvva 152,220,212
CV 178,156,157
CV 179,158,159
Mvva 152,224,232
LN 1,36,61,3072
Nvaa 156,248,176,96
s_nop 0
Ta 248,9,16896
Ta 250,10,17152
Mvaa 156,252,196
EX 140
Mvva 156,220,216
EX 141
Mvva 156,224,236
WL 2
Ta 252,9,16904
Ta 254,10,17160
Mvav 20,240,200
EX 142
Mvav 24,240,204
EX 143
Mvav 28,240,208
CV 140,140,141
CV 141,142,143
Mvav 32,240,212
EX 144
Mvav 36,240,216
EX 145
Mvav 100,244,200
EX 146
Mvav 104,244,204
EX 147
Mvav 108,244,208
CV 144,144,145
CV 145,146,147
Mvav 112,244,212
EX 148
Mvav 116,244,216
WL 0
s_barrier
Mvav 40,248,200
s_add_u32 m0,s76,57344
Tv 220,8,49152
Tv 222,8,53248
Mvav 44,248,204
s_cmp_le_u32 s89,1
s_cselect_b64 s[28:29],s[70:71],%3
v_subrev_u32 v3,262144,v3
Mvav 48,248,208
LD 3,28,64,0
Mvav 52,248,212
s_nop 0
EX 149
Mvav 56,248,216
EX 150
Mvav 120,252,200
s_nop 0
Tv 224,252,49152
Tv 226,252,53248
Mvav 124,252,204
LD 3,28,66,2048
Mvav 128,252,208
s_cmp_le_u32 s89,0
s_cselect_b64 s[24:25],s[68:69],%2
v_subrev_u32 v0,128,v0
Mvav 132,252,212
s_add_u32 m0,s77,118784
EX 151
Mvav 136,252,216
s_nop 0
CV 148,148,149
CV 149,150,151
Mvvv 228,248,160
s_nop 0
Ta 240,8,49664
Ta 242,8,53760
Mvvv 232,248,164
LD 0,24,49,2048
Mvvv 236,248,168
s_add_u32 m0,s77,122880
EX 152
Mvvv 240,248,172
s_nop 0
EX 153
Mvvv 244,248,176
WL 2
EX 154
Mavv 0,220,160
s_nop 0
Ta 244,252,49664
Ta 246,252,53760
Mavv 4,220,164
LD 0,24,51,2048
Mavv 8,220,168
s_add_u32 m0,s77,126976
EX 155
Mavv 12,220,172
s_nop 0
CV 152,152,153
CV 153,154,155
Mavv 16,220,176
s_nop 0
EX 156
Mavv 20,224,160
s_nop 0
Ta 248,8,50176
Ta 250,8,54272
Mavv 24,224,164
LD 0,24,53,2048
Mavv 28,224,168
s_add_u32 m0,s77,131072
EX 157
Mavv 32,224,172
EX 158
Mavv 36,224,176
WL 2
EX 159
Maav 40,240,160
s_nop 0
Ta 252,252,50176
Ta 254,252,54272
Maav 44,240,164
LD 0,24,55,2048
Maav 48,240,168
s_add_u32 m0,s77,135168
CV 156,156,157
CV 157,158,159
Maav 52,240,172
Maav 56,240,176
Maav 60,244,160
s_nop 0
Tv 220,8,50688
Tv 222,8,54784
Maav 64,244,164
LD 0,24,57,2048
Maav 68,244,168
Maav 72,244,172
Maav 76,244,176
WL 2
Tv 224,252,50688
Tv 226,252,54784
Maav 80,248,160
Maav 84,248,164
Maav 88,248,168
Maav 92,248,172
Maav 96,248,176
Maav 100,252,160
Ra 240,6,18432
Maav 104,252,164
Ra 244,7,18432
Maav 108,252,168
Ra 248,6,19456
Maav 112,252,172
Maav 116,252,176
s_waitcnt vmcnt(11) lgkmcnt(3)
s_barrier
Mavv 120,220,160
s_add_u32 m0,s76,106496
Ra 252,7,19456
Mavv 124,220,164
s_cmp_eq_u32 s88,6
s_cselect_b32 s86,0x20000,0
v_subrev_u32 v2,s86,v2
Mavv 128,220,168
v_add_u32 v2,8192,v2
Mavv 132,220,172
LN 2,96,62,0
Mavv 136,220,176
Mavv 140,224,160
Mavv 144,224,164
Mavv 148,224,168
LN 2,96,63,1024
Mavv 152,224,172
Mavv 156,224,176
WL 0
Nvaa 160,240,160,80
Mvaa 160,244,180
LN 2,96,62,2048
Mvaa 160,248,200
Mvaa 160,252,220
Nvaa 164,240,164,84
Mvaa 164,244,184
s_nop 0
Tv 220,9,17408
Tv 222,10,17664
Mvaa 164,248,204
LN 2,96,63,3072
Mvaa 164,252,224
EX 160
Nvaa 168,240,168,88
EX 161
Mvaa 168,244,188
EX 162
Mvaa 168,248,208
s_nop 0
Tv 224,9,17416
Tv 226,10,17672
Mvaa 168,252,228
EX 163
Nvaa 172,240,172,92
CV 142,160,161
CV 143,162,163
Mvaa 172,244,192
EX 164
Mvaa 172,248,212
EX 165
Mvaa 172,252,232
EX 166
Nvaa 176,240,176,96
s_nop 0
Ta 240,9,17920
Ta 242,10,18176
Mvaa 176,244,196
EX 167
Mvaa 176,248,216
CV 146,164,165
CV 147,166,167
Mvaa 176,252,236
WL 2
Ta 244,9,17928
Ta 246,10,18184
Mvvv 20,220,180
EX 168
Mvvv 24,220,184
EX 169
Mvvv 28,220,188
EX 170
Mvvv 32,220,192
EX 171
Mvvv 36,220,196
CV 150,168,169
CV 151,170,171
Mvvv 100,224,180
EX 172
Mvvv 104,224,184
EX 173
Mvvv 108,224,188
EX 174
Mvvv 112,224,192
EX 175
Mvvv 116,224,196
s_waitcnt vmcnt(9) lgkmcnt(0)
s_barrier
Mvav 40,240,180
s_add_u32 m0,s76,58368
Ra 248,6,32768
Ra 252,7,32768
Mvav 44,240,184
Rv 220,6,33792
v_subrev_u32 v3,262144,v3
Mvav 48,240,188
Rv 224,7,33792
CV 154,172,173
Mvav 52,240,192
LD 3,28,64,0
Mvav 56,240,196
s_nop 0
Rv 200,12,40960
Rv 204,12,43008
Mvav 120,244,180
Rv 208,12,45056
CV 155,174,175
Mvav 124,244,184
Rv 212,12,47104
Mvav 128,244,188
LD 3,28,66,2048
Mvav 132,244,192
s_add_u32 m0,s76,24576
s_cmp_eq_u32 s88,7
s_cselect_b32 s86,0x20000,0
Mvav 136,244,196
WL 5
v_subrev_u32 v1,s86,v1
v_add_u32 v1,8192,v1
Nvaa 160,248,160,80
Rv 216,12,49152
Mvaa 160,252,180
LN 1,36,58,0
Mvva 160,220,200
WL 3
EX 176
Mvva 160,224,220
EX 177
Nvaa 164,248,164,84
EX 178
Mvaa 164,252,184
s_nop 0
Ta 240,9,16384
Ta 242,10,16640
Mvva 164,220,204
LN 1,36,59,1024
Mvva 164,224,224
EX 179
Nvaa 168,248,168,88
CV 158,176,177
CV 159,178,179
Mvaa 168,252,188
EX 160
Mvva 168,220,208
s_nop 0
Ta 244,9,16392
Ta 246,10,16648
Mvva 168,224,228
LN 1,36,60,2048
Nvaa 172,248,172,92
EX 161
Mvaa 172,252,192
EX 162
Mvva 172,220,212
EX 163
Mvva 172,224,232
LN 1,36,61,3072
Nvaa 176,248,176,96
s_nop 0
Ta 248,9,16896
Ta 250,10,17152
Mvaa 176,252,196
CV 160,160,161
CV 161,162,163
Mvva 176,220,216
EX 164
Mvva 176,224,236
WL 2
Ta 252,9,16904
Ta 254,10,17160
Mvav 20,240,200
EX 165
Mvav 24,240,204
EX 166
Mvav 28,240,208
EX 167
Mvav 32,240,212
CV 164,164,165
CV 165,166,167
Mvav 36,240,216
EX 168
Mvav 100,244,200
EX 169
Mvav 104,244,204
EX 170
Mvav 108,244,208
EX 171
Mvav 112,244,212
CV 168,168,169
CV 169,170,171
Mvav 116,244,216
WL 0
s_barrier
Mvav 40,248,200
s_add_u32 m0,s76,57344
Tv 220,8,32768
Tv 222,8,36864
Mvav 44,248,204
s_cmp_le_u32 s89,0
s_cselect_b64 s[28:29],s[70:71],%3
v_subrev_u32 v3,262144,v3
Mvav 48,248,208
LD 3,28,64,0
Mvav 52,248,212
EX 172
Mvav 56,248,216
EX 173
Mvav 120,252,200
s_nop 0
Tv 224,252,32768
Tv 226,252,36864
Mvav 124,252,204
LD 3,28,66,2048
Mvav 128,252,208
EX 174
Mvav 132,252,212
EX 175
Mvav 136,252,216
CV 172,172,173
CV 173,174,175
Mvvv 228,248,140
s_nop 0
Ta 240,8,33280
Ta 242,8,37376
Mvvv 232,248,144
EX 176
Mvvv 236,248,148
EX 177
Mvvv 240,248,152
EX 178
Mvvv 244,248,156
WL 2
EX 179
Mavv 0,220,140
s_nop 0
Ta 244,252,33280
Ta 246,252,37376
Mavv 4,220,144
CV 176,176,177
CV 177,178,179
Mavv 8,220,148
Mavv 12,220,152
Mavv 16,220,156
Mavv 20,224,140
s_nop 0
Ta 248,8,33792
Ta 250,8,37888
Mavv 24,224,144
Mavv 28,224,148
Mavv 32,224,152
Mavv 36,224,156
WL 2
Maav 40,240,140
s_nop 0
Ta 252,252,33792
Ta 254,252,37888
Maav 44,240,144
Maav 48,240,148
Maav 52,240,152
Maav 56,240,156
Maav 60,244,140
s_nop 0
Tv 220,8,34304
Tv 222,8,38400
Maav 64,244,144
Maav 68,244,148
Maav 72,244,152
Maav 76,244,156
WL 2
Tv 224,252,34304
Tv 226,252,38400
Maav 80,248,140
s_nop 0
Rv 180,11,40960
Rv 184,11,43008
Maav 84,248,144
Rv 188,11,45056
Maav 88,248,148
Rv 192,11,47104
Maav 92,248,152
Rv 196,11,49152
Maav 96,248,156
Maav 100,252,140
Ra 240,6,34816
Maav 104,252,144
Ra 244,7,34816
Maav 108,252,148
Ra 248,6,35840
Maav 112,252,152
Maav 116,252,156
s_waitcnt vmcnt(6) lgkmcnt(5)
s_barrier
Mavv 120,220,140
s_add_u32 m0,s76,90112
Ra 252,7,35840
Mavv 124,220,144
s_cmp_eq_u32 s88,7
s_cselect_b32 s86,0x20000,0
v_subrev_u32 v2,s86,v2
Mavv 128,220,148
v_add_u32 v2,8192,v2
Mavv 132,220,152
LN 2,96,62,0
Mavv 136,220,156
Mavv 140,224,140
Mavv 144,224,144
Mavv 148,224,148
LN 2,96,63,1024
Mavv 152,224,152
Mavv 156,224,156
WL 0
Nvaa 140,240,160,80
Mvaa 140,244,180
LN 2,96,62,2048
Mvaa 140,248,200
Mvaa 140,252,220
Nvaa 144,240,164,84
Mvaa 144,244,184
s_nop 0
Tv 220,9,17408
Tv 222,10,17664
Mvaa 144,248,204
LN 2,96,63,3072
Mvaa 144,252,224
EX 140
Nvaa 148,240,168,88
EX 141
Mvaa 148,244,188
EX 142
Mvaa 148,248,208
s_nop 0
Tv 224,9,17416
Tv 226,10,17672
Mvaa 148,252,228
EX 143
Nvaa 152,240,172,92
CV 162,140,141
CV 163,142,143
Mvaa 152,244,192
EX 144
Mvaa 152,248,212
EX 145
Mvaa 152,252,232
EX 146
Nvaa 156,240,176,96
s_nop 0
Ta 240,9,17920
Ta 242,10,18176
Mvaa 156,244,196
EX 147
Mvaa 156,248,216
CV 166,144,145
CV 167,146,147
Mvaa 156,252,236
WL 2
Ta 244,9,17928
Ta 246,10,18184
Mvvv 20,220,180
EX 148
Mvvv 24,220,184
EX 149
Mvvv 28,220,188
EX 150
Mvvv 32,220,192
EX 151
Mvvv 36,220,196
CV 170,148,149
CV 171,150,151
Mvvv 100,224,180
EX 152
Mvvv 104,224,184
EX 153
Mvvv 108,224,188
EX 154
Mvvv 112,224,192
EX 155
Mvvv 116,224,196
s_waitcnt vmcnt(4) lgkmcnt(0)
s_barrier
Mvav 40,240,180
s_add_u32 m0,s76,58368
Ra 248,6,0
Ra 252,7,0
Mvav 44,240,184
s_nop 0
Rv 220,6,1024
v_subrev_u32 v3,262144,v3
Mvav 48,240,188
s_nop 0
Rv 224,7,1024
CV 174,152,153
Mvav 52,240,192
LD 3,28,64,0
Mvav 56,240,196
s_nop 0
Rv 200,12,20480
Rv 204,12,22528
Mvav 120,244,180
s_nop 0
Rv 208,12,24576
CV 175,154,155
Mvav 124,244,184
s_nop 0
Rv 212,12,26624
Mvav 128,244,188
LD 3,28,66,2048
Mvav 132,244,192
s_add_u32 m0,s76,40960
s_cmp_eq_u32 s88,8
s_cselect_b32 s86,0x20000,0
Mvav 136,244,196
WL 5
v_subrev_u32 v1,s86,v1
v_add_u32 v1,8192,v1
Nvaa 140,248,160,80
s_nop 0
Rv 216,12,28672
Mvaa 140,252,180
LN 1,36,58,0
Mvva 140,220,200
WL 3
EX 156
Mvva 140,224,220
EX 157
Nvaa 144,248,164,84
EX 158
Mvaa 144,252,184
s_nop 0
Ta 240,9,16384
Ta 242,10,16640
Mvva 144,220,204
LN 1,36,59,1024
Mvva 144,224,224
EX 159
Nvaa 148,248,168,88
CV 178,156,157
CV 179,158,159
Mvaa 148,252,188
EX 140
Mvva 148,220,208
s_nop 0
Ta 244,9,16392
Ta 246,10,16648
Mvva 148,224,228
LN 1,36,60,2048
Nvaa 152,248,172,92
EX 141
Mvaa 152,252,192
EX 142
Mvva 152,220,212
EX 143
Mvva 152,224,232
LN 1,36,61,3072
Nvaa 156,248,176,96
s_nop 0
Ta 248,9,16896
Ta 250,10,17152
Mvaa 156,252,196
CV 140,140,141
CV 141,142,143
Mvva 156,220,216
EX 144
Mvva 156,224,236
WL 2
Ta 252,9,16904
Ta 254,10,17160
Mvav 20,240,200
EX 145
Mvav 24,240,204
EX 146
Mvav 28,240,208
EX 147
Mvav 32,240,212
CV 144,144,145
CV 145,146,147
Mvav 36,240,216
EX 148
Mvav 100,244,200
EX 149
Mvav 104,244,204
EX 150
Mvav 108,244,208
EX 151
Mvav 112,244,212
CV 148,148,149
CV 149,150,151
Mvav 116,244,216
WL 0
s_barrier
Mvav 40,248,200
s_add_u32 m0,s76,57344
Tv 220,8,16384
Tv 222,8,20480
Mvav 44,248,204
s_nop 0
v_add_u32 v3,128,v3
Mvav 48,248,208
LD 3,28,64,0
Mvav 52,248,212
s_nop 0
EX 152
Mvav 56,248,216
EX 153
Mvav 120,252,200
s_nop 0
Tv 224,252,16384
Tv 226,252,20480
Mvav 124,252,204
LD 3,28,66,2048
Mvav 128,252,208
s_nop 0
EX 154
Mvav 132,252,212
EX 155
Mvav 136,252,216
s_nop 0
CV 152,152,153
CV 153,154,155
Mvvv 228,248,160
s_nop 0
Ta 240,8,16896
Ta 242,8,20992
Mvvv 232,248,164
s_nop 0
EX 156
Mvvv 236,248,168
EX 157
Mvvv 240,248,172
EX 158
Mvvv 244,248,176
WL 2
EX 159
Mavv 0,220,160
s_nop 0
Ta 244,252,16896
Ta 246,252,20992
Mavv 4,220,164
CV 156,156,157
CV 157,158,159
Mavv 8,220,168
Mavv 12,220,172
Mavv 16,220,176
Mavv 20,224,160
s_nop 0
Ta 248,8,17408
Ta 250,8,21504
Mavv 24,224,164
Mavv 28,224,168
Mavv 32,224,172
Mavv 36,224,176
WL 2
Maav 40,240,160
s_nop 0
Ta 252,252,17408
Ta 254,252,21504
Maav 44,240,164
Maav 48,240,168
Maav 52,240,172
Maav 56,240,176
Maav 60,244,160
s_nop 0
Tv 220,8,17920
Tv 222,8,22016
Maav 64,244,164
Maav 68,244,168
Maav 72,244,172
Maav 76,244,176
WL 2
Tv 224,252,17920
Tv 226,252,22016
Maav 80,248,160
s_nop 0
Rv 180,11,20480
Rv 184,11,22528
Maav 84,248,164
Rv 188,11,24576
Maav 88,248,168
Rv 192,11,26624
Maav 92,248,172
Rv 196,11,28672
Maav 96,248,176
Maav 100,252,160
Ra 240,6,2048
Maav 104,252,164
Ra 244,7,2048
Maav 108,252,168
Ra 248,6,3072
Maav 112,252,172
Maav 116,252,176
s_waitcnt vmcnt(6) lgkmcnt(5)
s_barrier
Mavv 120,220,160
s_add_u32 m0,s76,73728
Ra 252,7,3072
Mavv 124,220,164
s_cmp_eq_u32 s88,8
s_cselect_b32 s86,0x20000,0
v_subrev_u32 v2,s86,v2
Mavv 128,220,168
v_add_u32 v2,8192,v2
Mavv 132,220,172
LN 2,96,62,0
Mavv 136,220,176
Mavv 140,224,160
Mavv 144,224,164
Mavv 148,224,168
LN 2,96,63,1024
Mavv 152,224,172
Mavv 156,224,176
WL 0
Nvaa 160,240,160,80
Mvaa 160,244,180
LN 2,96,62,2048
Mvaa 160,248,200
Mvaa 160,252,220
Nvaa 164,240,164,84
Mvaa 164,244,184
s_nop 0
Tv 220,9,17408
Tv 222,10,17664
Mvaa 164,248,204
LN 2,96,63,3072
Mvaa 164,252,224
EX 160
Nvaa 168,240,168,88
EX 161
Mvaa 168,244,188
EX 162
Mvaa 168,248,208
s_nop 0
Tv 224,9,17416
Tv 226,10,17672
Mvaa 168,252,228
EX 163
Nvaa 172,240,172,92
CV 142,160,161
CV 143,162,163
Mvaa 172,244,192
EX 164
Mvaa 172,248,212
EX 165
Mvaa 172,252,232
EX 166
Nvaa 176,240,176,96
s_nop 0
Ta 240,9,17920
Ta 242,10,18176
Mvaa 176,244,196
EX 167
Mvaa 176,248,216
CV 146,164,165
CV 147,166,167
Mvaa 176,252,236
WL 2
Ta 244,9,17928
Ta 246,10,18184
Mvvv 20,220,180
EX 168
Mvvv 24,220,184
EX 169
Mvvv 28,220,188
EX 170
Mvvv 32,220,192
EX 171
Mvvv 36,220,196
CV 150,168,169
CV 151,170,171
Mvvv 100,224,180
EX 172
Mvvv 104,224,184
EX 173
Mvvv 108,224,188
EX 174
Mvvv 112,224,192
EX 175
Mvvv 116,224,196
WL 0
s_barrier
Mvav 40,240,180
s_add_u32 m0,s76,58368
Ra 248,6,16384
Ra 252,7,16384
Mvav 44,240,184
Rv 220,6,17408
v_add_u32 v3,262144,v3
Mvav 48,240,188
Rv 224,7,17408
CV 154,172,173
Mvav 52,240,192
LD 3,28,64,0
Mvav 56,240,196
CV 155,174,175
v_mul_f32 v20,s85,v20
Mvav 120,244,180
EX 176
Mvav 124,244,184
EX 177
Mvav 128,244,188
LD 3,28,66,2048
Mvav 132,244,192
s_add_u32 m0,s76,8192
s_cmp_eq_u32 s88,9
s_cselect_b32 s86,0x20000,0
Mvav 136,244,196
WL 1
v_subrev_u32 v1,s86,v1
v_add_u32 v1,8192,v1
Nvaa 160,248,160,80
EX 178
Mvaa 160,252,180
LN 1,36,58,0
Mvva 160,220,200
WL 0
EX 179
Mvva 160,224,220
CV 158,176,177
CV 159,178,179
Nvaa 164,248,164,84
v_mul_f32 v21,s85,v21
v_mul_f32 v22,s85,v22
Mvaa 164,252,184
LN 1,36,59,1024
Mvva 164,220,204
v_mul_f32 v23,s85,v23
v_mul_f32 v100,s85,v100
Mvva 164,224,224
v_mul_f32 v101,s85,v101
v_mul_f32 v102,s85,v102
Nvaa 168,248,168,88
v_mul_f32 v103,s85,v103
CV 20,20,21
Mvaa 168,252,188
s_barrier
Mvva 168,220,208
s_nop 0
Ta 240,8,49152
Ta 242,8,53248
Mvva 168,224,228
LN 1,36,60,2048
Nvaa 172,248,172,92
CV 21,22,23
CV 22,100,101
Mvaa 172,252,192
CV 23,102,103
v_mul_f32 v24,s85,v24
Mvva 172,220,212
v_mul_f32 v25,s85,v25
v_mul_f32 v26,s85,v26
Mvva 172,224,232
s_nop 0
Ta 244,252,49152
Ta 246,252,53248
Nvaa 176,248,176,96
LN 1,36,61,3072
Mvaa 176,252,196
v_mul_f32 v27,s85,v27
v_mul_f32 v104,s85,v104
Mvva 176,220,216
v_mul_f32 v105,s85,v105
v_mul_f32 v106,s85,v106
Mvva 176,224,236
v_mul_f32 v107,s85,v107
CV 24,24,25
Mvvv 228,248,140
s_nop 0
Ta 248,8,49664
Ta 250,8,53760
Mvvv 232,248,144
CV 25,26,27
CV 26,104,105
Mvvv 236,248,148
CV 27,106,107
v_mul_f32 v28,s85,v28
Mvvv 240,248,152
v_mul_f32 v29,s85,v29
v_mul_f32 v30,s85,v30
Mvvv 244,248,156
WL 2
v_mul_f32 v31,s85,v31
v_mul_f32 v108,s85,v108
Maav 0,240,140
s_nop 0
Ta 252,252,49664
Ta 254,252,53760
Maav 4,240,144
v_mul_f32 v109,s85,v109
v_mul_f32 v110,s85,v110
Maav 8,240,148
v_mul_f32 v111,s85,v111
CV 28,28,29
Maav 12,240,152
CV 29,30,31
CV 30,108,109
Maav 16,240,156
CV 31,110,111
v_mul_f32 v32,s85,v32
Maav 20,244,140
s_nop 0
Tv 220,8,50176
Tv 222,8,54272
Maav 24,244,144
v_mul_f32 v33,s85,v33
v_mul_f32 v34,s85,v34
Maav 28,244,148
v_mul_f32 v35,s85,v35
v_mul_f32 v112,s85,v112
Maav 32,244,152
v_mul_f32 v113,s85,v113
v_mul_f32 v114,s85,v114
Maav 36,244,156
WL 2
v_mul_f32 v115,s85,v115
CV 32,32,33
Maav 40,248,140
s_nop 0
Tv 224,252,50176
Tv 226,252,54272
Maav 44,248,144
CV 33,34,35
CV 34,112,113
Maav 48,248,148
CV 35,114,115
v_mul_f32 v36,s85,v36
Maav 52,248,152
v_mul_f32 v37,s85,v37
v_mul_f32 v38,s85,v38
Maav 56,248,156
v_mul_f32 v39,s85,v39
v_mul_f32 v116,s85,v116
Maav 60,252,140
s_nop 0
Ta 240,8,50688
Ta 242,8,54784
Maav 64,252,144
v_mul_f32 v117,s85,v117
v_mul_f32 v118,s85,v118
Maav 68,252,148
v_mul_f32 v119,s85,v119
CV 36,36,37
Maav 72,252,152
CV 37,38,39
CV 38,116,117
Maav 76,252,156
WL 2
Ta 244,252,50688
Ta 246,252,54784
Mavv 80,220,140
CV 39,118,119
v_mul_f32 v40,s85,v40
Mavv 84,220,144
v_mul_f32 v41,s85,v41
v_mul_f32 v42,s85,v42
Mavv 88,220,148
v_mul_f32 v43,s85,v43
v_mul_f32 v120,s85,v120
Mavv 92,220,152
v_mul_f32 v121,s85,v121
v_mul_f32 v122,s85,v122
Mavv 96,220,156
v_mul_f32 v123,s85,v123
CV 40,40,41
Mavv 100,224,140
Ra 248,6,18432
CV 41,42,43
Mavv 104,224,144
Ra 252,7,18432
CV 42,120,121
Mavv 108,224,148
Rv 220,6,19456
CV 43,122,123
Mavv 112,224,152
v_mul_f32 v44,s85,v44
v_mul_f32 v45,s85,v45
Mavv 116,224,156
s_waitcnt vmcnt(10) lgkmcnt(3)
s_barrier
Maav 120,240,140
s_add_u32 m0,s76,106496
Rv 224,7,19456
v_mul_f32 v46,s85,v46
Maav 124,240,144
s_cmp_eq_u32 s88,9
s_cselect_b32 s86,0x20000,0
v_subrev_u32 v2,s86,v2
Maav 128,240,148
v_add_u32 v2,8192,v2
v_mul_f32 v47,s85,v47
Maav 132,240,152
LN 2,96,62,0
Maav 136,240,156
v_mul_f32 v124,s85,v124
v_mul_f32 v125,s85,v125
Maav 140,244,140
v_mul_f32 v126,s85,v126
v_mul_f32 v127,s85,v127
Maav 144,244,144
CV 44,44,45
CV 45,46,47
Maav 148,244,148
LN 2,96,63,1024
Maav 152,244,152
CV 46,124,125
CV 47,126,127
Maav 156,244,156
WL 0
v_mul_f32 v48,s85,v48
v_mul_f32 v49,s85,v49
Nvaa 140,248,160,80
v_mul_f32 v50,s85,v50
v_mul_f32 v51,s85,v51
Mvaa 140,252,180
LN 2,96,62,2048
Mvva 140,220,200
v_mul_f32 v128,s85,v128
v_mul_f32 v129,s85,v129
Mvva 140,224,220
v_mul_f32 v130,s85,v130
v_mul_f32 v131,s85,v131
Nvaa 144,248,164,84
CV 48,48,49
CV 49,50,51
Mvaa 144,252,184
s_nop 0
Ta 240,9,16384
Ta 242,10,16640
Mvva 144,220,204
LN 2,96,63,3072
Mvva 144,224,224
CV 50,128,129
CV 51,130,131
Nvaa 148,248,168,88
v_mul_f32 v52,s85,v52
v_mul_f32 v53,s85,v53
Mvaa 148,252,188
v_mul_f32 v54,s85,v54
v_mul_f32 v55,s85,v55
Mvva 148,220,208
s_nop 0
Ta 244,9,16896
Ta 246,10,17152
Mvva 148,224,228
v_mul_f32 v132,s85,v132
v_mul_f32 v133,s85,v133
Nvaa 152,248,172,92
v_mul_f32 v134,s85,v134
v_mul_f32 v135,s85,v135
Mvaa 152,252,192
CV 52,52,53
CV 53,54,55
Mvva 152,220,212
CV 54,132,133
CV 55,134,135
Mvva 152,224,232
v_mul_f32 v56,s85,v56
v_mul_f32 v57,s85,v57
Nvaa 156,248,176,96
s_nop 0
Ta 248,9,16392
Ta 250,10,16648
Mvaa 156,252,196
v_mul_f32 v58,s85,v58
v_mul_f32 v59,s85,v59
Mvva 156,220,216
v_mul_f32 v136,s85,v136
v_mul_f32 v137,s85,v137
Mvva 156,224,236
WL 2
Ta 252,9,16904
Ta 254,10,17160
Zvav 60,240,180
v_mul_f32 v138,s85,v138
v_mul_f32 v139,s85,v139
Zvav 64,240,184
CV 56,56,57
CV 57,58,59
Zvav 68,240,188
CV 58,136,137
CV 59,138,139
Zvav 72,240,192
EX 160
Zvav 76,240,196
EX 161
Zvav 200,244,180
EX 162
Zvav 204,244,184
EX 163
Zvav 208,244,188
CV 160,160,161
CV 161,162,163
Zvav 212,244,192
EX 164
Zvav 216,244,196
s_waitcnt vmcnt(8) lgkmcnt(0)
s_barrier
Zvav 100,248,180
s_add_u32 m0,s76,57344
Rv 220,6,32768
Rv 224,7,32768
Zvav 104,248,184
s_cmp_le_u32 s89,1
s_cselect_b64 s[28:29],s[70:71],%3
Ra 240,6,33792
Zvav 108,248,188
s_nop 0
Ra 244,7,33792
v_add_u32 v3,262144,v3
Zvav 112,248,192
LD 3,28,64,0
Zvav 116,248,196
s_nop 0
EX 165
Zvav 120,252,180
EX 166
Zvav 124,252,184
s_nop 0
EX 167
Zvav 128,252,188
LD 3,28,66,2048
Zvav 132,252,192
s_nop 0
CV 164,164,165
CV 165,166,167
EX 168
EX 169
EX 170
EX 171
CV 168,168,169
CV 169,170,171
EX 172
EX 173
EX 174
EX 175
CV 172,172,173
CV 173,174,175
EX 176
EX 177
EX 178
EX 179
CV 176,176,177
CV 177,178,179
EX 140
EX 141
EX 142
EX 143
CV 162,140,141
CV 163,142,143
Zvav 136,252,196
WL 1
s_add_u32 m0,s76,24576
Rv 180,12,20480
Nvva 140,220,160,80
s_cmp_eq_u32 s88,10
s_cselect_b32 s86,0x20000,0
Mvva 140,224,180
s_nop 0
v_subrev_u32 v1,s86,v1
v_add_u32 v1,8192,v1
Mvaa 140,240,200
EX 144
EX 145
EX 146
EX 147
CV 166,144,145
CV 167,146,147
WL 1
Mvaa 140,244,220
LN 1,36,58,0
Nvva 144,220,164,84
s_nop 0
Rv 184,12,22528
Rv 188,12,24576
Mvva 144,224,184
Ta 248,9,17408
Mvaa 144,240,204
EX 148
EX 149
EX 150
EX 151
CV 170,148,149
CV 171,150,151
Mvaa 144,244,224
s_nop 0
Ta 250,10,17664
Rv 192,12,26624
Nvva 148,220,168,88
LN 1,36,59,1024
Mvva 148,224,188
Rv 196,12,28672
Mvaa 148,240,208
EX 152
EX 153
EX 154
EX 155
CV 174,152,153
CV 175,154,155
Mvaa 148,244,228
s_nop 0
Ta 252,9,17920
Ta 254,10,18176
Nvva 152,220,172,92
LN 1,36,60,2048
Mvva 152,224,192
EX 156
Mvaa 152,240,212
EX 157
EX 158
EX 159
CV 178,156,157
CV 179,158,159
Mvaa 152,244,232
EX 140
Nvva 156,220,176,96
s_nop 0
Tv 220,9,17416
Tv 222,10,17672
Mvva 156,224,196
LN 1,36,61,3072
Mvaa 156,240,216
EX 141
Mvaa 156,244,236
WL 2
Tv 224,9,17928
Tv 226,10,18184
Mvav 60,248,180
EX 142
Mvav 64,248,184
EX 143
Mvav 68,248,188
CV 140,140,141
CV 141,142,143
Mvav 72,248,192
EX 144
Mvav 76,248,196
EX 145
Mvav 200,252,180
EX 146
Mvav 204,252,184
EX 147
Mvav 208,252,188
CV 144,144,145
CV 145,146,147
Mvav 212,252,192
EX 148
Mvav 216,252,196
WL 0
s_barrier
Mvvv 100,220,180
s_add_u32 m0,s76,58368
Ta 240,8,32768
Ta 242,8,36864
Mvvv 104,220,184
s_nop 0
v_add_u32 v3,262144,v3
Mvvv 108,220,188
LD 3,28,64,0
Mvvv 112,220,192
s_nop 0
EX 149
Mvvv 116,220,196
EX 150
Mvvv 120,224,180
s_nop 0
Ta 244,252,32768
Ta 246,252,36864
Mvvv 124,224,184
LD 3,28,66,2048
Mvvv 128,224,188
s_cmp_le_u32 s89,2
s_cselect_b64 s[24:25],s[68:69],%2
v_add_u32 v0,256,v0
Mvvv 132,224,192
s_add_u32 m0,s77,118784
EX 151
Mvvv 136,224,196
s_nop 0
CV 148,148,149
CV 149,150,151
Mvvv 228,248,160
s_nop 0
Ta 248,8,33280
Ta 250,8,37376
Mvvv 232,248,164
LD 0,24,49,2048
Mvvv 236,248,168
s_add_u32 m0,s77,122880
EX 152
Mvvv 240,248,172
s_nop 0
)ASM"
R"ASM(EX 153
Mvvv 244,248,176
WL 2
EX 154
Maav 0,240,160
s_nop 0
Ta 252,252,33280
Ta 254,252,37376
Maav 4,240,164
LD 0,24,51,2048
Maav 8,240,168
s_add_u32 m0,s77,126976
EX 155
Maav 12,240,172
s_nop 0
CV 152,152,153
CV 153,154,155
Maav 16,240,176
EX 156
Maav 20,244,160
s_nop 0
Tv 220,8,33792
Tv 222,8,37888
Maav 24,244,164
LD 0,24,53,2048
Maav 28,244,168
s_add_u32 m0,s77,131072
EX 157
Maav 32,244,172
EX 158
Maav 36,244,176
WL 2
EX 159
Maav 40,248,160
s_nop 0
Tv 224,252,33792
Tv 226,252,37888
Maav 44,248,164
LD 0,24,55,2048
Maav 48,248,168
s_add_u32 m0,s77,135168
CV 156,156,157
CV 157,158,159
Maav 52,248,172
Maav 56,248,176
Maav 60,252,160
s_nop 0
Ta 240,8,34304
Ta 242,8,38400
Maav 64,252,164
LD 0,24,57,2048
Maav 68,252,168
Maav 72,252,172
Maav 76,252,176
WL 2
Ta 244,252,34304
Ta 246,252,38400
Mavv 80,220,160
s_nop 0
Rv 180,11,40960
Rv 184,11,43008
Mavv 84,220,164
Rv 188,11,45056
Mavv 88,220,168
Rv 192,11,47104
Mavv 92,220,172
Rv 196,11,49152
Mavv 96,220,176
Mavv 100,224,160
Ra 248,6,34816
Mavv 104,224,164
Ra 252,7,34816
Mavv 108,224,168
Rv 220,6,35840
Mavv 112,224,172
Mavv 116,224,176
s_waitcnt vmcnt(11) lgkmcnt(5)
s_barrier
Maav 120,240,160
s_add_u32 m0,s76,90112
Rv 224,7,35840
Maav 124,240,164
s_cmp_eq_u32 s88,10
s_cselect_b32 s86,0x20000,0
v_subrev_u32 v2,s86,v2
Maav 128,240,168
v_add_u32 v2,8192,v2
Maav 132,240,172
LN 2,96,62,0
Maav 136,240,176
Maav 140,244,160
Maav 144,244,164
Maav 148,244,168
LN 2,96,63,1024
Maav 152,244,172
Maav 156,244,176
WL 0
Nvaa 160,248,160,80
Mvaa 160,252,180
LN 2,96,62,2048
Mvva 160,220,200
Mvva 160,224,220
Nvaa 164,248,164,84
Mvaa 164,252,184
s_nop 0
Ta 240,9,16384
Ta 242,10,16640
Mvva 164,220,204
LN 2,96,63,3072
Mvva 164,224,224
EX 160
Nvaa 168,248,168,88
EX 161
Mvaa 168,252,188
EX 162
Mvva 168,220,208
s_nop 0
Ta 244,9,16896
Ta 246,10,17152
Mvva 168,224,228
EX 163
Nvaa 172,248,172,92
CV 142,160,161
CV 143,162,163
Mvaa 172,252,192
EX 164
Mvva 172,220,212
EX 165
Mvva 172,224,232
EX 166
Nvaa 176,248,176,96
s_nop 0
Ta 248,9,16392
Ta 250,10,16648
Mvaa 176,252,196
EX 167
Mvva 176,220,216
CV 146,164,165
CV 147,166,167
Mvva 176,224,236
WL 2
Ta 252,9,16904
Ta 254,10,17160
Mvav 60,240,180
EX 168
Mvav 64,240,184
EX 169
Mvav 68,240,188
EX 170
Mvav 72,240,192
EX 171
Mvav 76,240,196
CV 150,168,169
CV 151,170,171
Mvav 200,244,180
EX 172
Mvav 204,244,184
EX 173
Mvav 208,244,188
EX 174
Mvav 212,244,192
EX 175
Mvav 216,244,196
s_waitcnt vmcnt(9) lgkmcnt(0)
s_barrier
Mvav 100,248,180
s_add_u32 m0,s76,57344
Rv 220,6,0
Rv 224,7,0
Mvav 104,248,184
s_cmp_le_u32 s89,2
s_cselect_b64 s[28:29],s[70:71],%3
Ra 240,6,1024
Mvav 108,248,188
Ra 244,7,1024
v_add_u32 v3,262144,v3
Mvav 112,248,192
LD 3,28,64,0
Mvav 116,248,196
CV 154,172,173
CV 155,174,175
Mvav 120,252,180
EX 176
Mvav 124,252,184
EX 177
Mvav 128,252,188
LD 3,28,66,2048
Mvav 132,252,192
s_add_u32 m0,s76,40960
s_cmp_eq_u32 s88,11
s_cselect_b32 s86,0x20000,0
Mvav 136,252,196
WL 1
v_subrev_u32 v1,s86,v1
v_add_u32 v1,8192,v1
Nvva 160,220,160,80
s_nop 0
Rv 180,12,40960
Rv 184,12,43008
Mvva 160,224,180
LN 1,36,58,0
Mvaa 160,240,200
WL 2
Rv 188,12,45056
Rv 192,12,47104
Mvaa 160,244,220
Rv 196,12,49152
Nvva 164,220,164,84
EX 178
Mvva 164,224,184
s_nop 0
Ta 248,9,17408
Ta 250,10,17664
Mvaa 164,240,204
LN 1,36,59,1024
Mvaa 164,244,224
EX 179
Nvva 168,220,168,88
CV 158,176,177
CV 159,178,179
Mvva 168,224,188
EX 160
Mvaa 168,240,208
s_nop 0
Ta 252,9,17920
Ta 254,10,18176
Mvaa 168,244,228
LN 1,36,60,2048
Nvva 172,220,172,92
EX 161
Mvva 172,224,192
EX 162
Mvaa 172,240,212
EX 163
Mvaa 172,244,232
LN 1,36,61,3072
Nvva 176,220,176,96
s_nop 0
Tv 220,9,17416
Tv 222,10,17672
Mvva 176,224,196
CV 160,160,161
CV 161,162,163
Mvaa 176,240,216
EX 164
Mvaa 176,244,236
WL 2
Tv 224,9,17928
Tv 226,10,18184
Mvav 60,248,180
EX 165
Mvav 64,248,184
EX 166
Mvav 68,248,188
EX 167
Mvav 72,248,192
CV 164,164,165
CV 165,166,167
Mvav 76,248,196
EX 168
Mvav 200,252,180
EX 169
Mvav 204,252,184
EX 170
Mvav 208,252,188
EX 171
Mvav 212,252,192
CV 168,168,169
CV 169,170,171
Mvav 216,252,196
WL 0
s_barrier
Mvvv 100,220,180
s_add_u32 m0,s76,58368
Ta 240,8,16384
Ta 242,8,20480
Mvvv 104,220,184
v_add_u32 v3,262144,v3
Mvvv 108,220,188
LD 3,28,64,0
Mvvv 112,220,192
EX 172
Mvvv 116,220,196
EX 173
Mvvv 120,224,180
s_nop 0
Ta 244,252,16384
Ta 246,252,20480
Mvvv 124,224,184
LD 3,28,66,2048
Mvvv 128,224,188
s_cmp_le_u32 s89,3
s_cselect_b64 s[24:25],s[68:69],%2
v_add_u32 v0,128,v0
Mvvv 132,224,192
s_add_u32 m0,s77,139264
EX 174
Mvvv 136,224,196
EX 175
Mvvv 228,248,140
s_nop 0
Ta 248,8,16896
Ta 250,8,20992
Mvvv 232,248,144
LD 0,24,49,2048
Mvvv 236,248,148
s_add_u32 m0,s77,143360
CV 172,172,173
CV 173,174,175
Mvvv 240,248,152
EX 176
Mvvv 244,248,156
WL 2
EX 177
Maav 0,240,140
s_nop 0
Ta 252,252,16896
Ta 254,252,20992
Maav 4,240,144
LD 0,24,51,2048
Maav 8,240,148
s_add_u32 m0,s77,147456
EX 178
Maav 12,240,152
EX 179
Maav 16,240,156
CV 176,176,177
CV 177,178,179
Maav 20,244,140
s_nop 0
Tv 220,8,17408
Tv 222,8,21504
Maav 24,244,144
LD 0,24,53,2048
Maav 28,244,148
s_add_u32 m0,s77,151552
Maav 32,244,152
Maav 36,244,156
WL 2
Maav 40,248,140
s_nop 0
Tv 224,252,17408
Tv 226,252,21504
Maav 44,248,144
LD 0,24,55,2048
Maav 48,248,148
s_add_u32 m0,s77,155648
Maav 52,248,152
Maav 56,248,156
Maav 60,252,140
s_nop 0
Ta 240,8,17920
Ta 242,8,22016
Maav 64,252,144
LD 0,24,57,2048
Maav 68,252,148
Maav 72,252,152
Maav 76,252,156
WL 2
Ta 244,252,17920
Ta 246,252,22016
Mavv 80,220,140
Mavv 84,220,144
Mavv 88,220,148
Mavv 92,220,152
Mavv 96,220,156
Mavv 100,224,140
Ra 248,6,2048
Mavv 104,224,144
Ra 252,7,2048
Mavv 108,224,148
Rv 220,6,3072
Mavv 112,224,152
Mavv 116,224,156
s_waitcnt vmcnt(11) lgkmcnt(3)
s_barrier
Maav 120,240,140
s_add_u32 m0,s76,73728
Rv 224,7,3072
Rv 180,11,20480
Maav 124,240,144
s_cmp_eq_u32 s88,11
s_cselect_b32 s86,0x20000,0
v_subrev_u32 v2,s86,v2
Maav 128,240,148
s_nop 0
Rv 184,11,22528
v_add_u32 v2,8192,v2
Maav 132,240,152
LN 2,96,62,0
Maav 136,240,156
s_nop 0
Rv 188,11,24576
Rv 192,11,26624
Maav 140,244,140
s_nop 0
Rv 196,11,28672
Maav 144,244,144
Maav 148,244,148
LN 2,96,63,1024
Maav 152,244,152
s_nop 0
Maav 156,244,156
WL 4
Nvaa 140,248,160,80
s_nop 0
Mvaa 140,252,180
LN 2,96,62,2048
Mvva 140,220,200
s_nop 0
Mvva 140,224,220
Nvaa 144,248,164,84
Mvaa 144,252,184
s_nop 0
Ta 240,9,16384
Ta 242,10,16640
Mvva 144,220,204
LN 2,96,63,3072
Mvva 144,224,224
s_nop 0
EX 140
Nvaa 148,248,168,88
EX 141
Mvaa 148,252,188
s_nop 0
EX 142
Mvva 148,220,208
s_nop 0
Ta 244,9,16896
Ta 246,10,17152
Mvva 148,224,228
s_nop 0
EX 143
Nvaa 152,248,172,92
CV 162,140,141
CV 163,142,143
Mvaa 152,252,192
EX 144
Mvva 152,220,212
EX 145
Mvva 152,224,232
EX 146
Nvaa 156,248,176,96
s_nop 0
Ta 248,9,16392
Ta 250,10,16648
Mvaa 156,252,196
EX 147
Mvva 156,220,216
CV 166,144,145
CV 167,146,147
Mvva 156,224,236
WL 2
Ta 252,9,16904
Ta 254,10,17160
Mvav 60,240,180
EX 148
Mvav 64,240,184
EX 149
Mvav 68,240,188
EX 150
Mvav 72,240,192
EX 151
Mvav 76,240,196
CV 170,148,149
CV 171,150,151
Mvav 200,244,180
EX 152
Mvav 204,244,184
EX 153
Mvav 208,244,188
EX 154
Mvav 212,244,192
EX 155
Mvav 216,244,196
s_waitcnt vmcnt(9) lgkmcnt(0)
s_barrier
Mvav 100,248,180
s_add_u32 m0,s76,57344
Rv 220,6,16384
Rv 224,7,16384
Mvav 104,248,184
s_cmp_le_u32 s89,3
s_cselect_b64 s[28:29],s[70:71],%3
Ra 240,6,17408
Mvav 108,248,188
Ra 244,7,17408
v_add_u32 v3,262144,v3
Mvav 112,248,192
LD 3,28,64,0
Mvav 116,248,196
CV 174,152,153
CV 175,154,155
Mvav 120,252,180
EX 156
Mvav 124,252,184
EX 157
Mvav 128,252,188
LD 3,28,66,2048
Mvav 132,252,192
s_add_u32 m0,s76,8192
s_cmp_eq_u32 s88,12
s_cselect_b32 s86,0x20000,0
Mvav 136,252,196
WL 1
v_subrev_u32 v1,s86,v1
v_add_u32 v1,8192,v1
Nvva 140,220,160,80
s_nop 0
Rv 180,12,20480
Rv 184,12,22528
Mvva 140,224,180
LN 1,36,58,0
Mvaa 140,240,200
WL 2
Rv 188,12,24576
Rv 192,12,26624
Mvaa 140,244,220
Rv 196,12,28672
Nvva 144,220,164,84
EX 158
Mvva 144,224,184
s_nop 0
Ta 248,9,17408
Ta 250,10,17664
Mvaa 144,240,204
LN 1,36,59,1024
Mvaa 144,244,224
EX 159
Nvva 148,220,168,88
CV 178,156,157
CV 179,158,159
Mvva 148,224,188
EX 140
Mvaa 148,240,208
s_nop 0
Ta 252,9,17920
Ta 254,10,18176
Mvaa 148,244,228
LN 1,36,60,2048
Nvva 152,220,172,92
EX 141
Mvva 152,224,192
EX 142
Mvaa 152,240,212
EX 143
Mvaa 152,244,232
LN 1,36,61,3072
Nvva 156,220,176,96
s_nop 0
Tv 220,9,17416
Tv 222,10,17672
Mvva 156,224,196
CV 140,140,141
CV 141,142,143
Mvaa 156,240,216
EX 144
Mvaa 156,244,236
WL 2
Tv 224,9,17928
Tv 226,10,18184
Mvav 60,248,180
EX 145
Mvav 64,248,184
EX 146
Mvav 68,248,188
EX 147
Mvav 72,248,192
CV 144,144,145
CV 145,146,147
Mvav 76,248,196
EX 148
Mvav 200,252,180
EX 149
Mvav 204,252,184
EX 150
Mvav 208,252,188
EX 151
Mvav 212,252,192
CV 148,148,149
CV 149,150,151
Mvav 216,252,196
WL 0
s_barrier
Mvvv 100,220,180
s_add_u32 m0,s76,58368
Ta 240,8,49152
Ta 242,8,53248
Mvvv 104,220,184
s_nop 0
v_add_u32 v3,262144,v3
Mvvv 108,220,188
LD 3,28,64,0
Mvvv 112,220,192
s_nop 0
EX 152
Mvvv 116,220,196
EX 153
Mvvv 120,224,180
s_nop 0
Ta 244,252,49152
Ta 246,252,53248
Mvvv 124,224,184
LD 3,28,66,2048
Mvvv 128,224,188
s_cmp_le_u32 s89,4
s_cselect_b64 s[24:25],s[68:69],%2
v_add_u32 v0,128,v0
Mvvv 132,224,192
s_add_u32 m0,s77,118784
EX 154
Mvvv 136,224,196
s_nop 0
EX 155
Mvvv 228,248,160
s_nop 0
Ta 248,8,49664
Ta 250,8,53760
Mvvv 232,248,164
LD 0,24,49,2048
Mvvv 236,248,168
s_add_u32 m0,s77,122880
CV 152,152,153
CV 153,154,155
Mvvv 240,248,172
s_nop 0
EX 156
Mvvv 244,248,176
WL 2
EX 157
Maav 0,240,160
s_nop 0
Ta 252,252,49664
Ta 254,252,53760
Maav 4,240,164
LD 0,24,51,2048
Maav 8,240,168
s_add_u32 m0,s77,126976
EX 158
Maav 12,240,172
s_nop 0
EX 159
Maav 16,240,176
CV 156,156,157
CV 157,158,159
Maav 20,244,160
s_nop 0
Tv 220,8,50176
Tv 222,8,54272
Maav 24,244,164
LD 0,24,53,2048
Maav 28,244,168
s_add_u32 m0,s77,131072
Maav 32,244,172
Maav 36,244,176
WL 2
Maav 40,248,160
s_nop 0
Tv 224,252,50176
Tv 226,252,54272
Maav 44,248,164
LD 0,24,55,2048
Maav 48,248,168
s_add_u32 m0,s77,135168
Maav 52,248,172
Maav 56,248,176
Maav 60,252,160
s_nop 0
Ta 240,8,50688
Ta 242,8,54784
Maav 64,252,164
LD 0,24,57,2048
Maav 68,252,168
Maav 72,252,172
Maav 76,252,176
WL 2
Ta 244,252,50688
Ta 246,252,54784
Mavv 80,220,160
Mavv 84,220,164
Mavv 88,220,168
Mavv 92,220,172
Mavv 96,220,176
Mavv 100,224,160
Ra 248,6,18432
Mavv 104,224,164
Ra 252,7,18432
Mavv 108,224,168
Rv 220,6,19456
Mavv 112,224,172
Mavv 116,224,176
s_waitcnt vmcnt(11) lgkmcnt(3)
s_barrier
Maav 120,240,160
s_add_u32 m0,s76,106496
Rv 224,7,19456
Rv 180,11,40960
Maav 124,240,164
s_cmp_eq_u32 s88,12
s_cselect_b32 s86,0x20000,0
v_subrev_u32 v2,s86,v2
Maav 128,240,168
Rv 184,11,43008
v_add_u32 v2,8192,v2
Maav 132,240,172
LN 2,96,62,0
Maav 136,240,176
s_nop 0
Rv 188,11,45056
Rv 192,11,47104
Maav 140,244,160
Rv 196,11,49152
Maav 144,244,164
Maav 148,244,168
LN 2,96,63,1024
Maav 152,244,172
Maav 156,244,176
WL 4
Nvaa 160,248,160,80
Mvaa 160,252,180
LN 2,96,62,2048
Mvva 160,220,200
Mvva 160,224,220
Nvaa 164,248,164,84
Mvaa 164,252,184
s_nop 0
Ta 240,9,16384
Ta 242,10,16640
Mvva 164,220,204
LN 2,96,63,3072
Mvva 164,224,224
EX 160
Nvaa 168,248,168,88
EX 161
Mvaa 168,252,188
EX 162
Mvva 168,220,208
s_nop 0
Ta 244,9,16896
Ta 246,10,17152
Mvva 168,224,228
EX 163
Nvaa 172,248,172,92
CV 142,160,161
CV 143,162,163
Mvaa 172,252,192
EX 164
Mvva 172,220,212
EX 165
Mvva 172,224,232
EX 166
Nvaa 176,248,176,96
s_nop 0
Ta 248,9,16392
Ta 250,10,16648
Mvaa 176,252,196
EX 167
Mvva 176,220,216
CV 146,164,165
CV 147,166,167
Mvva 176,224,236
WL 2
Ta 252,9,16904
Ta 254,10,17160
Mvav 60,240,180
EX 168
Mvav 64,240,184
EX 169
Mvav 68,240,188
EX 170
Mvav 72,240,192
EX 171
Mvav 76,240,196
CV 150,168,169
CV 151,170,171
Mvav 200,244,180
EX 172
Mvav 204,244,184
EX 173
Mvav 208,244,188
EX 174
Mvav 212,244,192
EX 175
Mvav 216,244,196
s_waitcnt vmcnt(9) lgkmcnt(0)
s_barrier
Mvav 100,248,180
s_add_u32 m0,s76,57344
Rv 220,6,32768
Rv 224,7,32768
Mvav 104,248,184
s_cmp_le_u32 s89,4
s_cselect_b64 s[28:29],s[70:71],%3
Ra 240,6,33792
Mvav 108,248,188
Ra 244,7,33792
v_add_u32 v3,262144,v3
Mvav 112,248,192
LD 3,28,64,0
Mvav 116,248,196
CV 154,172,173
CV 155,174,175
Mvav 120,252,180
EX 176
Mvav 124,252,184
EX 177
Mvav 128,252,188
LD 3,28,66,2048
Mvav 132,252,192
s_add_u32 m0,s76,24576
s_cmp_eq_u32 s88,13
s_cselect_b32 s86,0x20000,0
Mvav 136,252,196
WL 1
v_subrev_u32 v1,s86,v1
v_add_u32 v1,8192,v1
Nvva 160,220,160,80
s_nop 0
Rv 180,12,40960
Rv 184,12,43008
Mvva 160,224,180
LN 1,36,58,0
Mvaa 160,240,200
WL 2
Rv 188,12,45056
Rv 192,12,47104
Mvaa 160,244,220
Rv 196,12,49152
Nvva 164,220,164,84
EX 178
Mvva 164,224,184
s_nop 0
Ta 248,9,17408
Ta 250,10,17664
Mvaa 164,240,204
LN 1,36,59,1024
Mvaa 164,244,224
EX 179
Nvva 168,220,168,88
CV 158,176,177
CV 159,178,179
Mvva 168,224,188
EX 160
Mvaa 168,240,208
s_nop 0
Ta 252,9,17920
Ta 254,10,18176
Mvaa 168,244,228
LN 1,36,60,2048
Nvva 172,220,172,92
EX 161
Mvva 172,224,192
EX 162
Mvaa 172,240,212
EX 163
Mvaa 172,244,232
LN 1,36,61,3072
Nvva 176,220,176,96
s_nop 0
Tv 220,9,17416
Tv 222,10,17672
Mvva 176,224,196
CV 160,160,161
CV 161,162,163
Mvaa 176,240,216
EX 164
Mvaa 176,244,236
WL 2
Tv 224,9,17928
Tv 226,10,18184
Mvav 60,248,180
EX 165
Mvav 64,248,184
EX 166
Mvav 68,248,188
EX 167
Mvav 72,248,192
CV 164,164,165
CV 165,166,167
Mvav 76,248,196
EX 168
Mvav 200,252,180
EX 169
Mvav 204,252,184
EX 170
Mvav 208,252,188
EX 171
Mvav 212,252,192
CV 168,168,169
CV 169,170,171
Mvav 216,252,196
WL 0
s_barrier
Mvvv 100,220,180
s_add_u32 m0,s76,58368
Ta 240,8,32768
Ta 242,8,36864
Mvvv 104,220,184
v_add_u32 v3,262144,v3
Mvvv 108,220,188
LD 3,28,64,0
Mvvv 112,220,192
EX 172
Mvvv 116,220,196
EX 173
Mvvv 120,224,180
s_nop 0
Ta 244,252,32768
Ta 246,252,36864
Mvvv 124,224,184
LD 3,28,66,2048
Mvvv 128,224,188
s_cmp_le_u32 s89,5
s_cselect_b64 s[24:25],s[68:69],%2
v_add_u32 v0,128,v0
Mvvv 132,224,192
s_add_u32 m0,s77,139264
EX 174
Mvvv 136,224,196
EX 175
Mvvv 228,248,140
s_nop 0
Ta 248,8,33280
Ta 250,8,37376
Mvvv 232,248,144
LD 0,24,49,2048
Mvvv 236,248,148
s_add_u32 m0,s77,143360
CV 172,172,173
CV 173,174,175
Mvvv 240,248,152
EX 176
Mvvv 244,248,156
WL 2
EX 177
Maav 0,240,140
s_nop 0
Ta 252,252,33280
Ta 254,252,37376
Maav 4,240,144
LD 0,24,51,2048
Maav 8,240,148
s_add_u32 m0,s77,147456
EX 178
Maav 12,240,152
EX 179
Maav 16,240,156
CV 176,176,177
CV 177,178,179
Maav 20,244,140
s_nop 0
Tv 220,8,33792
Tv 222,8,37888
Maav 24,244,144
LD 0,24,53,2048
Maav 28,244,148
s_add_u32 m0,s77,151552
Maav 32,244,152
Maav 36,244,156
WL 2
Maav 40,248,140
s_nop 0
Tv 224,252,33792
Tv 226,252,37888
Maav 44,248,144
LD 0,24,55,2048
Maav 48,248,148
s_add_u32 m0,s77,155648
Maav 52,248,152
Maav 56,248,156
Maav 60,252,140
s_nop 0
Ta 240,8,34304
Ta 242,8,38400
Maav 64,252,144
LD 0,24,57,2048
Maav 68,252,148
Maav 72,252,152
Maav 76,252,156
WL 2
Ta 244,252,34304
Ta 246,252,38400
Mavv 80,220,140
Mavv 84,220,144
Mavv 88,220,148
Mavv 92,220,152
Mavv 96,220,156
Mavv 100,224,140
Ra 248,6,34816
Mavv 104,224,144
Ra 252,7,34816
Mavv 108,224,148
Rv 220,6,35840
Mavv 112,224,152
Mavv 116,224,156
s_waitcnt vmcnt(11) lgkmcnt(3)
s_barrier
Maav 120,240,140
s_add_u32 m0,s76,90112
Rv 224,7,35840
Rv 180,11,20480
Maav 124,240,144
s_cmp_eq_u32 s88,13
s_cselect_b32 s86,0x20000,0
v_subrev_u32 v2,s86,v2
Maav 128,240,148
s_nop 0
Rv 184,11,22528
v_add_u32 v2,8192,v2
Maav 132,240,152
LN 2,96,62,0
Maav 136,240,156
s_nop 0
Rv 188,11,24576
Rv 192,11,26624
Maav 140,244,140
s_nop 0
Rv 196,11,28672
Maav 144,244,144
Maav 148,244,148
LN 2,96,63,1024
Maav 152,244,152
s_nop 0
Maav 156,244,156
WL 4
Nvaa 140,248,160,80
s_nop 0
Mvaa 140,252,180
LN 2,96,62,2048
Mvva 140,220,200
s_nop 0
Mvva 140,224,220
Nvaa 144,248,164,84
Mvaa 144,252,184
s_nop 0
Ta 240,9,16384
Ta 242,10,16640
Mvva 144,220,204
LN 2,96,63,3072
Mvva 144,224,224
s_nop 0
EX 140
Nvaa 148,248,168,88
EX 141
Mvaa 148,252,188
s_nop 0
EX 142
Mvva 148,220,208
s_nop 0
Ta 244,9,16896
Ta 246,10,17152
Mvva 148,224,228
s_nop 0
EX 143
Nvaa 152,248,172,92
CV 162,140,141
CV 163,142,143
Mvaa 152,252,192
EX 144
Mvva 152,220,212
EX 145
Mvva 152,224,232
EX 146
Nvaa 156,248,176,96
s_nop 0
Ta 248,9,16392
Ta 250,10,16648
Mvaa 156,252,196
EX 147
Mvva 156,220,216
CV 166,144,145
CV 167,146,147
Mvva 156,224,236
WL 2
Ta 252,9,16904
Ta 254,10,17160
Mvav 60,240,180
EX 148
Mvav 64,240,184
EX 149
Mvav 68,240,188
EX 150
Mvav 72,240,192
EX 151
Mvav 76,240,196
CV 170,148,149
CV 171,150,151
Mvav 200,244,180
EX 152
Mvav 204,244,184
EX 153
Mvav 208,244,188
EX 154
Mvav 212,244,192
EX 155
Mvav 216,244,196
s_waitcnt vmcnt(9) lgkmcnt(0)
s_barrier
Mvav 100,248,180
s_add_u32 m0,s76,57344
Rv 220,6,0
Rv 224,7,0
Mvav 104,248,184
s_cmp_le_u32 s89,5
s_cselect_b64 s[28:29],s[70:71],%3
Ra 240,6,1024
Mvav 108,248,188
Ra 244,7,1024
v_add_u32 v3,262144,v3
Mvav 112,248,192
LD 3,28,64,0
Mvav 116,248,196
CV 174,152,153
CV 175,154,155
Mvav 120,252,180
EX 156
Mvav 124,252,184
EX 157
Mvav 128,252,188
LD 3,28,66,2048
Mvav 132,252,192
s_add_u32 m0,s76,40960
s_cmp_eq_u32 s88,14
s_cselect_b32 s86,0x20000,0
Mvav 136,252,196
WL 1
v_subrev_u32 v1,s86,v1
v_add_u32 v1,8192,v1
Nvva 140,220,160,80
s_nop 0
Rv 180,12,20480
Rv 184,12,22528
Mvva 140,224,180
LN 1,36,58,0
Mvaa 140,240,200
WL 2
Rv 188,12,24576
Rv 192,12,26624
Mvaa 140,244,220
Rv 196,12,28672
Nvva 144,220,164,84
EX 158
Mvva 144,224,184
s_nop 0
Ta 248,9,17408
Ta 250,10,17664
Mvaa 144,240,204
LN 1,36,59,1024
Mvaa 144,244,224
EX 159
Nvva 148,220,168,88
CV 178,156,157
CV 179,158,159
Mvva 148,224,188
EX 140
Mvaa 148,240,208
s_nop 0
Ta 252,9,17920
Ta 254,10,18176
Mvaa 148,244,228
LN 1,36,60,2048
Nvva 152,220,172,92
EX 141
Mvva 152,224,192
EX 142
Mvaa 152,240,212
EX 143
Mvaa 152,244,232
LN 1,36,61,3072
Nvva 156,220,176,96
s_nop 0
Tv 220,9,17416
Tv 222,10,17672
Mvva 156,224,196
CV 140,140,141
CV 141,142,143
Mvaa 156,240,216
EX 144
Mvaa 156,244,236
WL 2
Tv 224,9,17928
Tv 226,10,18184
Mvav 60,248,180
EX 145
Mvav 64,248,184
EX 146
Mvav 68,248,188
EX 147
Mvav 72,248,192
CV 144,144,145
CV 145,146,147
Mvav 76,248,196
EX 148
Mvav 200,252,180
EX 149
Mvav 204,252,184
EX 150
Mvav 208,252,188
EX 151
Mvav 212,252,192
CV 148,148,149
CV 149,150,151
Mvav 216,252,196
WL 0
s_barrier
Mvvv 100,220,180
s_add_u32 m0,s76,58368
Ta 240,8,16384
Ta 242,8,20480
Mvvv 104,220,184
s_nop 0
v_add_u32 v3,262144,v3
Mvvv 108,220,188
LD 3,28,64,0
Mvvv 112,220,192
s_nop 0
EX 152
Mvvv 116,220,196
EX 153
Mvvv 120,224,180
s_nop 0
Ta 244,252,16384
Ta 246,252,20480
Mvvv 124,224,184
LD 3,28,66,2048
Mvvv 128,224,188
s_cmp_le_u32 s89,6
s_cselect_b64 s[24:25],s[68:69],%2
v_add_u32 v0,128,v0
Mvvv 132,224,192
s_add_u32 m0,s77,118784
EX 154
Mvvv 136,224,196
s_nop 0
EX 155
Mvvv 228,248,160
s_nop 0
Ta 248,8,16896
Ta 250,8,20992
Mvvv 232,248,164
LD 0,24,49,2048
Mvvv 236,248,168
s_add_u32 m0,s77,122880
CV 152,152,153
CV 153,154,155
Mvvv 240,248,172
s_nop 0
EX 156
Mvvv 244,248,176
WL 2
EX 157
Maav 0,240,160
s_nop 0
Ta 252,252,16896
Ta 254,252,20992
Maav 4,240,164
LD 0,24,51,2048
Maav 8,240,168
s_add_u32 m0,s77,126976
EX 158
Maav 12,240,172
s_nop 0
EX 159
Maav 16,240,176
CV 156,156,157
CV 157,158,159
Maav 20,244,160
s_nop 0
Tv 220,8,17408
Tv 222,8,21504
Maav 24,244,164
LD 0,24,53,2048
Maav 28,244,168
s_add_u32 m0,s77,131072
Maav 32,244,172
Maav 36,244,176
WL 2
Maav 40,248,160
s_nop 0
Tv 224,252,17408
Tv 226,252,21504
Maav 44,248,164
LD 0,24,55,2048
Maav 48,248,168
s_add_u32 m0,s77,135168
Maav 52,248,172
Maav 56,248,176
Maav 60,252,160
s_nop 0
Ta 240,8,17920
Ta 242,8,22016
Maav 64,252,164
LD 0,24,57,2048
Maav 68,252,168
Maav 72,252,172
Maav 76,252,176
WL 2
Ta 244,252,17920
Ta 246,252,22016
Mavv 80,220,160
Mavv 84,220,164
Mavv 88,220,168
Mavv 92,220,172
Mavv 96,220,176
Mavv 100,224,160
Ra 248,6,2048
Mavv 104,224,164
Ra 252,7,2048
Mavv 108,224,168
Rv 220,6,3072
Mavv 112,224,172
Mavv 116,224,176
s_waitcnt vmcnt(11) lgkmcnt(3)
s_barrier
Maav 120,240,160
s_add_u32 m0,s76,73728
Rv 224,7,3072
Rv 180,11,40960
Maav 124,240,164
s_cmp_eq_u32 s88,14
s_cselect_b32 s86,0x20000,0
v_subrev_u32 v2,s86,v2
Maav 128,240,168
Rv 184,11,43008
v_add_u32 v2,8192,v2
Maav 132,240,172
LN 2,96,62,0
Maav 136,240,176
s_nop 0
Rv 188,11,45056
Rv 192,11,47104
Maav 140,244,160
Rv 196,11,49152
Maav 144,244,164
Maav 148,244,168
LN 2,96,63,1024
Maav 152,244,172
Maav 156,244,176
WL 4
Nvaa 160,248,160,80
Mvaa 160,252,180
LN 2,96,62,2048
Mvva 160,220,200
Mvva 160,224,220
Nvaa 164,248,164,84
Mvaa 164,252,184
s_nop 0
Ta 240,9,16384
Ta 242,10,16640
Mvva 164,220,204
LN 2,96,63,3072
Mvva 164,224,224
EX 160
Nvaa 168,248,168,88
EX 161
Mvaa 168,252,188
EX 162
Mvva 168,220,208
s_nop 0
Ta 244,9,16896
Ta 246,10,17152
Mvva 168,224,228
EX 163
Nvaa 172,248,172,92
CV 142,160,161
CV 143,162,163
Mvaa 172,252,192
EX 164
Mvva 172,220,212
EX 165
Mvva 172,224,232
EX 166
Nvaa 176,248,176,96
s_nop 0
Ta 248,9,16392
Ta 250,10,16648
Mvaa 176,252,196
EX 167
Mvva 176,220,216
CV 146,164,165
CV 147,166,167
Mvva 176,224,236
WL 2
Ta 252,9,16904
Ta 254,10,17160
Mvav 60,240,180
EX 168
Mvav 64,240,184
EX 169
Mvav 68,240,188
EX 170
Mvav 72,240,192
EX 171
Mvav 76,240,196
CV 150,168,169
CV 151,170,171
Mvav 200,244,180
EX 172
Mvav 204,244,184
EX 173
Mvav 208,244,188
EX 174
Mvav 212,244,192
EX 175
Mvav 216,244,196
s_waitcnt vmcnt(9) lgkmcnt(0)
s_barrier
Mvav 100,248,180
s_add_u32 m0,s76,57344
Rv 220,6,16384
Rv 224,7,16384
Mvav 104,248,184
s_cmp_le_u32 s89,6
s_cselect_b64 s[28:29],s[70:71],%3
Ra 240,6,17408
Mvav 108,248,188
Ra 244,7,17408
v_add_u32 v3,262144,v3
Mvav 112,248,192
LD 3,28,64,0
Mvav 116,248,196
CV 154,172,173
CV 155,174,175
Mvav 120,252,180
EX 176
Mvav 124,252,184
EX 177
Mvav 128,252,188
LD 3,28,66,2048
Mvav 132,252,192
s_add_u32 m0,s76,8192
s_cmp_eq_u32 s88,15
s_cselect_b32 s86,0x20000,0
Mvav 136,252,196
WL 1
v_subrev_u32 v1,s86,v1
v_add_u32 v1,8192,v1
Nvva 160,220,160,80
s_nop 0
Rv 180,12,40960
Rv 184,12,43008
Mvva 160,224,180
LN 1,36,58,0
Mvaa 160,240,200
WL 2
Rv 188,12,45056
Rv 192,12,47104
Mvaa 160,244,220
Rv 196,12,49152
Nvva 164,220,164,84
EX 178
Mvva 164,224,184
s_nop 0
Ta 248,9,17408
Ta 250,10,17664
Mvaa 164,240,204
LN 1,36,59,1024
Mvaa 164,244,224
EX 179
Nvva 168,220,168,88
CV 158,176,177
CV 159,178,179
Mvva 168,224,188
EX 160
Mvaa 168,240,208
s_nop 0
Ta 252,9,17920
Ta 254,10,18176
Mvaa 168,244,228
LN 1,36,60,2048
Nvva 172,220,172,92
EX 161
Mvva 172,224,192
EX 162
Mvaa 172,240,212
EX 163
Mvaa 172,244,232
LN 1,36,61,3072
Nvva 176,220,176,96
s_nop 0
Tv 220,9,17416
Tv 222,10,17672
Mvva 176,224,196
CV 160,160,161
CV 161,162,163
Mvaa 176,240,216
EX 164
Mvaa 176,244,236
WL 2
Tv 224,9,17928
Tv 226,10,18184
Mvav 60,248,180
EX 165
Mvav 64,248,184
EX 166
Mvav 68,248,188
EX 167
Mvav 72,248,192
CV 164,164,165
CV 165,166,167
Mvav 76,248,196
EX 168
Mvav 200,252,180
EX 169
Mvav 204,252,184
EX 170
Mvav 208,252,188
EX 171
Mvav 212,252,192
CV 168,168,169
CV 169,170,171
Mvav 216,252,196
WL 0
s_barrier
Mvvv 100,220,180
s_add_u32 m0,s76,58368
Ta 240,8,49152
Ta 242,8,53248
Mvvv 104,220,184
v_add_u32 v3,262144,v3
Mvvv 108,220,188
LD 3,28,64,0
Mvvv 112,220,192
EX 172
Mvvv 116,220,196
EX 173
Mvvv 120,224,180
s_nop 0
Ta 244,252,49152
Ta 246,252,53248
Mvvv 124,224,184
LD 3,28,66,2048
Mvvv 128,224,188
s_cmp_le_u32 s89,7
s_cselect_b64 s[24:25],s[68:69],%2
v_add_u32 v0,128,v0
Mvvv 132,224,192
s_add_u32 m0,s77,139264
EX 174
Mvvv 136,224,196
EX 175
Mvvv 228,248,140
s_nop 0
Ta 248,8,49664
Ta 250,8,53760
Mvvv 232,248,144
LD 0,24,49,2048
Mvvv 236,248,148
s_add_u32 m0,s77,143360
CV 172,172,173
CV 173,174,175
Mvvv 240,248,152
EX 176
Mvvv 244,248,156
WL 2
EX 177
Maav 0,240,140
s_nop 0
Ta 252,252,49664
Ta 254,252,53760
Maav 4,240,144
LD 0,24,51,2048
Maav 8,240,148
s_add_u32 m0,s77,147456
EX 178
Maav 12,240,152
EX 179
Maav 16,240,156
CV 176,176,177
CV 177,178,179
Maav 20,244,140
s_nop 0
Tv 220,8,50176
Tv 222,8,54272
Maav 24,244,144
LD 0,24,53,2048
Maav 28,244,148
s_add_u32 m0,s77,151552
Maav 32,244,152
Maav 36,244,156
WL 2
Maav 40,248,140
s_nop 0
Tv 224,252,50176
Tv 226,252,54272
Maav 44,248,144
LD 0,24,55,2048
Maav 48,248,148
s_add_u32 m0,s77,155648
Maav 52,248,152
Maav 56,248,156
Maav 60,252,140
s_nop 0
Ta 240,8,50688
Ta 242,8,54784
Maav 64,252,144
LD 0,24,57,2048
Maav 68,252,148
Maav 72,252,152
Maav 76,252,156
WL 2
Ta 244,252,50688
Ta 246,252,54784
Mavv 80,220,140
Mavv 84,220,144
Mavv 88,220,148
Mavv 92,220,152
Mavv 96,220,156
Mavv 100,224,140
Ra 248,6,18432
Mavv 104,224,144
Ra 252,7,18432
Mavv 108,224,148
Rv 220,6,19456
Mavv 112,224,152
Mavv 116,224,156
s_waitcnt vmcnt(11) lgkmcnt(3)
s_barrier
Maav 120,240,140
s_add_u32 m0,s76,106496
Rv 224,7,19456
Rv 180,11,20480
Maav 124,240,144
s_cmp_eq_u32 s88,15
s_cselect_b32 s86,0x20000,0
v_subrev_u32 v2,s86,v2
Maav 128,240,148
s_nop 0
Rv 184,11,22528
v_add_u32 v2,8192,v2
Maav 132,240,152
LN 2,96,62,0
Maav 136,240,156
s_nop 0
Rv 188,11,24576
Rv 192,11,26624
Maav 140,244,140
s_nop 0
Rv 196,11,28672
Maav 144,244,144
Maav 148,244,148
LN 2,96,63,1024
Maav 152,244,152
s_nop 0
Maav 156,244,156
WL 4
Nvaa 140,248,160,80
s_nop 0
Mvaa 140,252,180
LN 2,96,62,2048
Mvva 140,220,200
s_nop 0
Mvva 140,224,220
Nvaa 144,248,164,84
Mvaa 144,252,184
s_nop 0
Ta 240,9,16384
Ta 242,10,16640
Mvva 144,220,204
LN 2,96,63,3072
Mvva 144,224,224
s_nop 0
EX 140
Nvaa 148,248,168,88
EX 141
Mvaa 148,252,188
s_nop 0
EX 142
Mvva 148,220,208
s_nop 0
Ta 244,9,16896
Ta 246,10,17152
Mvva 148,224,228
s_nop 0
EX 143
Nvaa 152,248,172,92
CV 162,140,141
CV 163,142,143
Mvaa 152,252,192
EX 144
Mvva 152,220,212
EX 145
Mvva 152,224,232
EX 146
Nvaa 156,248,176,96
s_nop 0
Ta 248,9,16392
Ta 250,10,16648
Mvaa 156,252,196
EX 147
Mvva 156,220,216
CV 166,144,145
CV 167,146,147
Mvva 156,224,236
WL 2
Ta 252,9,16904
Ta 254,10,17160
Mvav 60,240,180
EX 148
Mvav 64,240,184
EX 149
Mvav 68,240,188
EX 150
Mvav 72,240,192
EX 151
Mvav 76,240,196
CV 170,148,149
CV 171,150,151
Mvav 200,244,180
EX 152
Mvav 204,244,184
EX 153
Mvav 208,244,188
EX 154
Mvav 212,244,192
EX 155
Mvav 216,244,196
s_waitcnt vmcnt(9) lgkmcnt(0)
s_barrier
Mvav 100,248,180
s_add_u32 m0,s76,57344
Rv 220,6,32768
Rv 224,7,32768
Mvav 104,248,184
s_cmp_le_u32 s89,7
s_cselect_b64 s[28:29],s[70:71],%3
Ra 240,6,33792
Mvav 108,248,188
Ra 244,7,33792
v_add_u32 v3,262144,v3
Mvav 112,248,192
LD 3,28,64,0
Mvav 116,248,196
CV 174,152,153
CV 175,154,155
Mvav 120,252,180
EX 156
Mvav 124,252,184
EX 157
Mvav 128,252,188
LD 3,28,66,2048
Mvav 132,252,192
s_add_u32 m0,s76,24576
s_cmp_le_u32 s88,15
s_cselect_b32 s86,0x20000,0
Mvav 136,252,196
WL 1
v_add_u32 v1,s86,v1
v_add_u32 v1,139264,v1
Nvva 140,220,160,80
s_nop 0
Rv 180,12,20480
Rv 184,12,22528
Mvva 140,224,180
LN 1,36,58,0
Mvaa 140,240,200
WL 2
Rv 188,12,24576
Rv 192,12,26624
Mvaa 140,244,220
Rv 196,12,28672
Nvva 144,220,164,84
EX 158
Mvva 144,224,184
s_nop 0
Ta 248,9,17408
Ta 250,10,17664
Mvaa 144,240,204
LN 1,36,59,1024
Mvaa 144,244,224
EX 159
Nvva 148,220,168,88
CV 178,156,157
CV 179,158,159
Mvva 148,224,188
EX 140
Mvaa 148,240,208
s_nop 0
Ta 252,9,17920
Ta 254,10,18176
Mvaa 148,244,228
LN 1,36,60,2048
Nvva 152,220,172,92
EX 141
Mvva 152,224,192
EX 142
Mvaa 152,240,212
EX 143
Mvaa 152,244,232
LN 1,36,61,3072
Nvva 156,220,176,96
s_nop 0
Tv 220,9,17416
Tv 222,10,17672
Mvva 156,224,196
CV 140,140,141
CV 141,142,143
Mvaa 156,240,216
EX 144
Mvaa 156,244,236
WL 2
Tv 224,9,17928
Tv 226,10,18184
Mvav 60,248,180
EX 145
Mvav 64,248,184
EX 146
Mvav 68,248,188
EX 147
Mvav 72,248,192
CV 144,144,145
CV 145,146,147
Mvav 76,248,196
EX 148
Mvav 200,252,180
EX 149
Mvav 204,252,184
EX 150
Mvav 208,252,188
EX 151
Mvav 212,252,192
CV 148,148,149
CV 149,150,151
Mvav 216,252,196
WL 0
s_barrier
Mvvv 100,220,180
s_add_u32 m0,s76,58368
Ta 240,8,32768
Ta 242,8,36864
Mvvv 104,220,184
s_nop 0
v_add_u32 v3,262144,v3
Mvvv 108,220,188
LD 3,28,64,0
Mvvv 112,220,192
s_nop 0
EX 152
Mvvv 116,220,196
EX 153
Mvvv 120,224,180
s_nop 0
Ta 244,252,32768
Ta 246,252,36864
Mvvv 124,224,184
LD 3,28,66,2048
Mvvv 128,224,188
s_cmp_le_u32 s89,8
s_cselect_b64 s[24:25],s[68:69],%2
v_add_u32 v0,128,v0
Mvvv 132,224,192
s_add_u32 m0,s77,118784
EX 154
Mvvv 136,224,196
s_nop 0
EX 155
Mvvv 228,248,160
s_nop 0
Ta 248,8,33280
Ta 250,8,37376
Mvvv 232,248,164
LD 0,24,49,2048
Mvvv 236,248,168
s_add_u32 m0,s77,122880
CV 152,152,153
CV 153,154,155
Mvvv 240,248,172
s_nop 0
EX 156
Mvvv 244,248,176
WL 2
EX 157
Maav 0,240,160
s_nop 0
Ta 252,252,33280
Ta 254,252,37376
Maav 4,240,164
LD 0,24,51,2048
Maav 8,240,168
s_add_u32 m0,s77,126976
EX 158
Maav 12,240,172
s_nop 0
EX 159
Maav 16,240,176
CV 156,156,157
CV 157,158,159
Maav 20,244,160
s_nop 0
Tv 220,8,33792
Tv 222,8,37888
Maav 24,244,164
LD 0,24,53,2048
Maav 28,244,168
s_add_u32 m0,s77,131072
Maav 32,244,172
Maav 36,244,176
WL 2
Maav 40,248,160
s_nop 0
Tv 224,252,33792
Tv 226,252,37888
Maav 44,248,164
LD 0,24,55,2048
Maav 48,248,168
s_add_u32 m0,s77,135168
Maav 52,248,172
Maav 56,248,176
Maav 60,252,160
s_nop 0
Ta 240,8,34304
Ta 242,8,38400
Maav 64,252,164
LD 0,24,57,2048
Maav 68,252,168
Maav 72,252,172
Maav 76,252,176
WL 2
Ta 244,252,34304
Ta 246,252,38400
Mavv 80,220,160
Mavv 84,220,164
Mavv 88,220,168
Mavv 92,220,172
Mavv 96,220,176
Mavv 100,224,160
Ra 248,6,34816
Mavv 104,224,164
Ra 252,7,34816
Mavv 108,224,168
Rv 220,6,35840
Mavv 112,224,172
Mavv 116,224,176
s_waitcnt vmcnt(11) lgkmcnt(3)
s_barrier
Maav 120,240,160
s_add_u32 m0,s76,90112
Rv 224,7,35840
Rv 180,11,40960
Maav 124,240,164
s_cmp_le_u32 s88,15
s_cselect_b32 s86,0x20000,0
v_add_u32 v2,s86,v2
Maav 128,240,168
Rv 184,11,43008
v_add_u32 v2,139264,v2
Maav 132,240,172
LN 2,96,62,0
Maav 136,240,176
s_nop 0
Rv 188,11,45056
Rv 192,11,47104
Maav 140,244,160
Rv 196,11,49152
Maav 144,244,164
Maav 148,244,168
LN 2,96,63,1024
Maav 152,244,172
Maav 156,244,176
WL 4
Nvaa 160,248,160,80
Mvaa 160,252,180
LN 2,96,62,2048
Mvva 160,220,200
Mvva 160,224,220
Nvaa 164,248,164,84
Mvaa 164,252,184
s_nop 0
Ta 240,9,16384
Ta 242,10,16640
Mvva 164,220,204
LN 2,96,63,3072
Mvva 164,224,224
EX 160
Nvaa 168,248,168,88
EX 161
Mvaa 168,252,188
EX 162
Mvva 168,220,208
s_nop 0
Ta 244,9,16896
Ta 246,10,17152
Mvva 168,224,228
EX 163
Nvaa 172,248,172,92
CV 142,160,161
CV 143,162,163
Mvaa 172,252,192
EX 164
Mvva 172,220,212
EX 165
Mvva 172,224,232
EX 166
Nvaa 176,248,176,96
s_nop 0
Ta 248,9,16392
Ta 250,10,16648
Mvaa 176,252,196
EX 167
Mvva 176,220,216
CV 146,164,165
CV 147,166,167
Mvva 176,224,236
WL 2
Ta 252,9,16904
Ta 254,10,17160
Mvav 60,240,180
EX 168
Mvav 64,240,184
EX 169
Mvav 68,240,188
EX 170
Mvav 72,240,192
EX 171
Mvav 76,240,196
CV 150,168,169
CV 151,170,171
Mvav 200,244,180
EX 172
Mvav 204,244,184
EX 173
Mvav 208,244,188
EX 174
Mvav 212,244,192
EX 175
)ASM"
R"ASM(Mvav 216,244,196
s_waitcnt vmcnt(9) lgkmcnt(0)
s_barrier
Mvav 100,248,180
s_add_u32 m0,s76,57344
Rv 220,6,0
Rv 224,7,0
Mvav 104,248,184
s_cmp_le_u32 s89,8
s_cselect_b64 s[28:29],s[70:71],%3
Ra 240,6,1024
Mvav 108,248,188
Ra 244,7,1024
v_add_u32 v3,262144,v3
Mvav 112,248,192
LD 3,28,64,0
Mvav 116,248,196
CV 154,172,173
CV 155,174,175
Mvav 120,252,180
EX 176
Mvav 124,252,184
EX 177
Mvav 128,252,188
LD 3,28,66,2048
Mvav 132,252,192
s_add_u32 m0,s76,40960
s_cmp_eq_u32 s88,1
s_cselect_b32 s86,0x20000,0
Mvav 136,252,196
WL 1
v_subrev_u32 v1,s86,v1
v_add_u32 v1,8192,v1
Nvva 160,220,160,80
s_nop 0
Rv 180,12,40960
Rv 184,12,43008
Mvva 160,224,180
LN 1,36,58,0
Mvaa 160,240,200
WL 2
Rv 188,12,45056
Rv 192,12,47104
Mvaa 160,244,220
Rv 196,12,49152
Nvva 164,220,164,84
EX 178
Mvva 164,224,184
s_nop 0
Ta 248,9,17408
Ta 250,10,17664
Mvaa 164,240,204
LN 1,36,59,1024
Mvaa 164,244,224
EX 179
Nvva 168,220,168,88
CV 158,176,177
CV 159,178,179
Mvva 168,224,188
EX 160
Mvaa 168,240,208
s_nop 0
Ta 252,9,17920
Ta 254,10,18176
Mvaa 168,244,228
LN 1,36,60,2048
Nvva 172,220,172,92
EX 161
Mvva 172,224,192
EX 162
Mvaa 172,240,212
EX 163
Mvaa 172,244,232
LN 1,36,61,3072
Nvva 176,220,176,96
s_nop 0
Tv 220,9,17416
Tv 222,10,17672
Mvva 176,224,196
CV 160,160,161
CV 161,162,163
Mvaa 176,240,216
EX 164
Mvaa 176,244,236
WL 2
Tv 224,9,17928
Tv 226,10,18184
Mvav 60,248,180
EX 165
Mvav 64,248,184
EX 166
Mvav 68,248,188
EX 167
Mvav 72,248,192
CV 164,164,165
CV 165,166,167
Mvav 76,248,196
EX 168
Mvav 200,252,180
EX 169
Mvav 204,252,184
EX 170
Mvav 208,252,188
EX 171
Mvav 212,252,192
CV 168,168,169
CV 169,170,171
Mvav 216,252,196
WL 0
s_barrier
Mvvv 100,220,180
s_add_u32 m0,s76,58368
Ta 240,8,16384
Ta 242,8,20480
Mvvv 104,220,184
v_add_u32 v3,262144,v3
Mvvv 108,220,188
LD 3,28,64,0
Mvvv 112,220,192
EX 172
Mvvv 116,220,196
EX 173
Mvvv 120,224,180
s_nop 0
Ta 244,252,16384
Ta 246,252,20480
Mvvv 124,224,184
LD 3,28,66,2048
Mvvv 128,224,188
EX 174
Mvvv 132,224,192
EX 175
Mvvv 136,224,196
CV 172,172,173
CV 173,174,175
Mvvv 228,248,140
s_nop 0
Ta 248,8,16896
Ta 250,8,20992
Mvvv 232,248,144
EX 176
Mvvv 236,248,148
EX 177
Mvvv 240,248,152
EX 178
Mvvv 244,248,156
WL 2
EX 179
Maav 0,240,140
s_nop 0
Ta 252,252,16896
Ta 254,252,20992
Maav 4,240,144
CV 176,176,177
CV 177,178,179
Maav 8,240,148
Maav 12,240,152
Maav 16,240,156
Maav 20,244,140
s_nop 0
Tv 220,8,17408
Tv 222,8,21504
Maav 24,244,144
Maav 28,244,148
Maav 32,244,152
Maav 36,244,156
WL 2
Maav 40,248,140
s_nop 0
Tv 224,252,17408
Tv 226,252,21504
Maav 44,248,144
Maav 48,248,148
Maav 52,248,152
Maav 56,248,156
Maav 60,252,140
s_nop 0
Ta 240,8,17920
Ta 242,8,22016
Maav 64,252,144
Maav 68,252,148
Maav 72,252,152
Maav 76,252,156
WL 2
Ta 244,252,17920
Ta 246,252,22016
Mavv 80,220,140
Mavv 84,220,144
Mavv 88,220,148
Mavv 92,220,152
Mavv 96,220,156
Mavv 100,224,140
Ra 248,6,2048
Mavv 104,224,144
Ra 252,7,2048
Mavv 108,224,148
Rv 220,6,3072
Mavv 112,224,152
Mavv 116,224,156
s_waitcnt vmcnt(6) lgkmcnt(3)
s_barrier
Maav 120,240,140
s_add_u32 m0,s76,73728
Rv 224,7,3072
Rv 180,11,20480
Maav 124,240,144
s_cmp_eq_u32 s88,1
s_cselect_b32 s86,0x20000,0
v_subrev_u32 v2,s86,v2
Maav 128,240,148
s_nop 0
Rv 184,11,22528
v_add_u32 v2,8192,v2
Maav 132,240,152
LN 2,96,62,0
Maav 136,240,156
s_nop 0
Rv 188,11,24576
Rv 192,11,26624
Maav 140,244,140
s_nop 0
Rv 196,11,28672
Maav 144,244,144
Maav 148,244,148
LN 2,96,63,1024
Maav 152,244,152
s_nop 0
Maav 156,244,156
WL 4
Nvaa 140,248,160,80
s_nop 0
Mvaa 140,252,180
LN 2,96,62,2048
Mvva 140,220,200
s_nop 0
Mvva 140,224,220
Nvaa 144,248,164,84
Mvaa 144,252,184
s_nop 0
Ta 240,9,16384
Ta 242,10,16640
Mvva 144,220,204
LN 2,96,63,3072
Mvva 144,224,224
s_nop 0
EX 140
Nvaa 148,248,168,88
EX 141
Mvaa 148,252,188
s_nop 0
EX 142
Mvva 148,220,208
s_nop 0
Ta 244,9,16896
Ta 246,10,17152
Mvva 148,224,228
s_nop 0
EX 143
Nvaa 152,248,172,92
CV 162,140,141
CV 163,142,143
Mvaa 152,252,192
EX 144
Mvva 152,220,212
EX 145
Mvva 152,224,232
EX 146
Nvaa 156,248,176,96
s_nop 0
Ta 248,9,16392
Ta 250,10,16648
Mvaa 156,252,196
EX 147
Mvva 156,220,216
CV 166,144,145
CV 167,146,147
Mvva 156,224,236
WL 2
Ta 252,9,16904
Ta 254,10,17160
Mvav 60,240,180
EX 148
Mvav 64,240,184
EX 149
Mvav 68,240,188
EX 150
Mvav 72,240,192
EX 151
Mvav 76,240,196
CV 170,148,149
CV 171,150,151
Mvav 200,244,180
EX 152
Mvav 204,244,184
EX 153
Mvav 208,244,188
EX 154
Mvav 212,244,192
EX 155
Mvav 216,244,196
s_waitcnt vmcnt(4) lgkmcnt(0)
s_barrier
Mvav 100,248,180
s_add_u32 m0,s76,8192
Tv 220,9,17408
Tv 222,10,17664
Mvav 104,248,184
s_nop 0
Tv 224,9,17920
Tv 226,10,18176
Mvav 108,248,188
s_cmp_eq_u32 s88,2
s_cselect_b32 s86,0x20000,0
v_subrev_u32 v1,s86,v1
Mvav 112,248,192
v_add_u32 v1,8192,v1
CV 174,152,153
Mvav 116,248,196
LN 1,36,58,0
Mvav 120,252,180
s_nop 0
Ta 240,9,17416
Ta 242,10,17672
Mvav 124,252,184
CV 175,154,155
Mvav 128,252,188
EX 156
Mvav 132,252,192
LN 1,36,59,1024
Mvav 136,252,196
s_nop 0
Rv 180,12,20480
Rv 184,12,22528
Rv 188,12,24576
Rv 192,12,26624
Rv 196,12,28672
WL 4
Mvvv 60,220,180
WL 3
EX 157
Mvvv 64,220,184
WL 2
EX 158
Mvvv 68,220,188
WL 1
Ta 244,9,17928
Mvvv 72,220,192
WL 1
Ta 246,10,18184
Mvvv 76,220,196
LN 1,36,60,2048
Mvvv 200,224,180
EX 159
Mvvv 204,224,184
CV 178,156,157
CV 179,158,159
Mvvv 208,224,188
Mvvv 212,224,192
LN 1,36,61,3072
Mvvv 216,224,196
WL 0
s_barrier
Mvav 100,240,180
s_add_u32 m0,s76,57344
Ta 248,8,49152
Ta 250,8,53248
Mvav 104,240,184
s_cmp_eq_u32 s88,2
s_cselect_b32 s86,0x20000,0
v_subrev_u32 v2,s86,v2
Mvav 108,240,188
s_nop 0
v_add_u32 v2,8192,v2
Mvav 112,240,192
LN 2,96,62,0
Mvav 116,240,196
s_nop 0
v_mul_f32 v60,s85,v60
v_mul_f32 v61,s85,v61
Mvav 120,244,180
s_nop 0
Ta 252,252,49152
Ta 254,252,53248
Mvav 124,244,184
v_mul_f32 v62,s85,v62
v_mul_f32 v63,s85,v63
Mvav 128,244,188
LN 2,96,63,1024
Mvav 132,244,192
v_mul_f32 v100,s85,v100
v_mul_f32 v101,s85,v101
Mvav 136,244,196
v_mul_f32 v102,s85,v102
v_mul_f32 v103,s85,v103
Mvvv 228,248,160
s_nop 0
Tv 220,8,49664
Tv 222,8,53760
Mvvv 232,248,164
LN 2,96,62,2048
Mvvv 236,248,168
CV 60,60,61
CV 61,62,63
Mvvv 240,248,172
CV 62,100,101
CV 63,102,103
Mvvv 244,248,176
WL 2
v_mul_f32 v64,s85,v64
v_mul_f32 v65,s85,v65
Maav 0,248,160
s_nop 0
Tv 224,252,49664
Tv 226,252,53760
Maav 4,248,164
LN 2,96,63,3072
Maav 8,248,168
v_mul_f32 v66,s85,v66
v_mul_f32 v67,s85,v67
Maav 12,248,172
v_mul_f32 v104,s85,v104
v_mul_f32 v105,s85,v105
Maav 16,248,176
v_mul_f32 v106,s85,v106
v_mul_f32 v107,s85,v107
Maav 20,252,160
s_nop 0
Ta 240,8,50176
Ta 242,8,54272
Maav 24,252,164
CV 64,64,65
CV 65,66,67
Maav 28,252,168
CV 66,104,105
CV 67,106,107
Maav 32,252,172
v_mul_f32 v68,s85,v68
v_mul_f32 v69,s85,v69
Maav 36,252,176
WL 2
v_mul_f32 v70,s85,v70
v_mul_f32 v71,s85,v71
Mavv 40,220,160
s_nop 0
Ta 244,252,50176
Ta 246,252,54272
Mavv 44,220,164
v_mul_f32 v108,s85,v108
v_mul_f32 v109,s85,v109
Mavv 48,220,168
v_mul_f32 v110,s85,v110
v_mul_f32 v111,s85,v111
Mavv 52,220,172
CV 68,68,69
CV 69,70,71
Mavv 56,220,176
CV 70,108,109
CV 71,110,111
Mavv 60,224,160
s_nop 0
Ta 248,8,50688
Ta 250,8,54784
Mavv 64,224,164
v_mul_f32 v72,s85,v72
v_mul_f32 v73,s85,v73
Mavv 68,224,168
v_mul_f32 v74,s85,v74
v_mul_f32 v75,s85,v75
Mavv 72,224,172
v_mul_f32 v112,s85,v112
v_mul_f32 v113,s85,v113
Mavv 76,224,176
WL 2
Ta 252,252,50688
Ta 254,252,54784
Maav 80,240,160
v_mul_f32 v114,s85,v114
v_mul_f32 v115,s85,v115
Maav 84,240,164
CV 72,72,73
CV 73,74,75
Maav 88,240,168
CV 74,112,113
CV 75,114,115
Maav 92,240,172
v_mul_f32 v76,s85,v76
v_mul_f32 v77,s85,v77
Maav 96,240,176
v_mul_f32 v78,s85,v78
v_mul_f32 v79,s85,v79
Maav 100,244,160
v_mul_f32 v116,s85,v116
v_mul_f32 v117,s85,v117
Maav 104,244,164
v_mul_f32 v118,s85,v118
v_mul_f32 v119,s85,v119
Maav 108,244,168
CV 76,76,77
CV 77,78,79
Maav 112,244,172
CV 78,116,117
CV 79,118,119
Maav 116,244,176
WL 0
s_barrier
Maav 120,248,160
s_add_u32 m0,s76,106496
Rv 220,6,16384
Rv 224,7,16384
Maav 124,248,164
s_cmp_eq_u32 s88,3
s_cselect_b32 s86,0x20000,0
v_subrev_u32 v2,s86,v2
Maav 128,248,168
s_nop 0
v_add_u32 v2,8192,v2
v_mul_f32 v200,s85,v200
Maav 132,248,172
LN 2,96,62,0
Maav 136,248,176
s_nop 0
v_mul_f32 v201,s85,v201
v_mul_f32 v202,s85,v202
Maav 140,252,160
Ra 240,6,17408
v_mul_f32 v203,s85,v203
Maav 144,252,164
v_mul_f32 v120,s85,v120
v_mul_f32 v121,s85,v121
Maav 148,252,168
LN 2,96,63,1024
Maav 152,252,172
v_mul_f32 v122,s85,v122
v_mul_f32 v123,s85,v123
Maav 156,252,176
WL 1
CV 200,200,201
CV 201,202,203
Zvvv 140,220,20
Ra 244,7,17408
CV 202,120,121
Zvvv 144,220,24
LN 2,96,62,2048
Zvvv 148,220,28
CV 203,122,123
v_mul_f32 v204,s85,v204
Zvvv 152,220,32
v_mul_f32 v205,s85,v205
v_mul_f32 v206,s85,v206
Zvvv 156,220,36
v_mul_f32 v207,s85,v207
v_mul_f32 v124,s85,v124
Mvvv 140,224,40
Ra 248,6,18432
v_mul_f32 v125,s85,v125
Mvvv 144,224,44
LN 2,96,63,3072
Mvvv 148,224,48
v_mul_f32 v126,s85,v126
v_mul_f32 v127,s85,v127
Mvvv 152,224,52
CV 204,204,205
CV 205,206,207
Mvvv 156,224,56
WL 1
CV 206,124,125
CV 207,126,127
Mvav 140,240,60
Ra 252,7,18432
v_mul_f32 v208,s85,v208
Mvav 144,240,64
v_mul_f32 v209,s85,v209
v_mul_f32 v210,s85,v210
Mvav 148,240,68
v_mul_f32 v211,s85,v211
v_mul_f32 v128,s85,v128
Mvav 152,240,72
v_mul_f32 v129,s85,v129
v_mul_f32 v130,s85,v130
v_mul_f32 v131,s85,v131
CV 208,208,209
CV 209,210,211
CV 210,128,129
CV 211,130,131
v_mul_f32 v212,s85,v212
v_mul_f32 v213,s85,v213
v_mul_f32 v214,s85,v214
v_mul_f32 v215,s85,v215
v_mul_f32 v132,s85,v132
v_mul_f32 v133,s85,v133
v_mul_f32 v134,s85,v134
v_mul_f32 v135,s85,v135
CV 212,212,213
CV 213,214,215
CV 214,132,133
CV 215,134,135
v_mul_f32 v216,s85,v216
v_mul_f32 v217,s85,v217
v_mul_f32 v218,s85,v218
v_mul_f32 v219,s85,v219
v_mul_f32 v136,s85,v136
v_mul_f32 v137,s85,v137
v_mul_f32 v138,s85,v138
v_mul_f32 v139,s85,v139
CV 216,216,217
CV 217,218,219
CV 218,136,137
CV 219,138,139
Mvav 156,240,76
v_mul_f32 v229,0x4d800000,v229
Mvav 140,244,200
Rv 220,6,19456
Mvav 144,244,204
v_rcp_f32 v228,v228
Mvav 148,244,208
v_mul_f32 v233,0x4d800000,v233
Mvav 152,244,212
v_rcp_f32 v232,v232
Mvav 156,244,216
WL 1
Rv 224,7,19456
v_mul_f32 v237,0x4d800000,v237
Zvav 100,248,20
v_rcp_f32 v236,v236
Zvav 104,248,24
v_mul_f32 v241,0x4d800000,v241
Zvav 108,248,28
v_max3_f32 v80,v140,v141,v142
v_max3_f32 v84,v144,v145,v146
Zvav 112,248,32
v_max3_f32 v88,v148,v149,v150
v_max3_f32 v92,v152,v153,v154
Zvav 116,248,36
v_max3_f32 v96,v156,v157,v158
v_max_f32 v81,v80,v143
Mvav 100,252,40
v_max_f32 v80,v80,v143
v_max_f32 v85,v84,v147
Mvav 104,252,44
v_max_f32 v84,v84,v147
v_max_f32 v89,v88,v151
Mvav 108,252,48
v_max_f32 v88,v88,v151
v_max_f32 v93,v92,v155
Mvav 112,252,52
v_max_f32 v92,v92,v155
v_max_f32 v97,v96,v159
Mvav 116,252,56
WL 0
s_barrier
Mvvv 100,220,60
s_add_u32 m0,s76,24576
Ra 240,6,32768
Ra 244,7,32768
Mvvv 104,220,64
s_cmp_eq_u32 s88,3
s_cselect_b32 s86,0x20000,0
v_subrev_u32 v1,s86,v1
Mvvv 108,220,68
s_nop 0
v_add_u32 v1,8192,v1
v_max_f32 v96,v96,v159
Mvvv 112,220,72
LN 1,36,58,0
Mvvv 116,220,76
s_nop 0
v_permlane32_swap_b32 v80,v81
Mvvv 100,224,200
Ra 248,6,33792
Mvvv 104,224,204
s_nop 0
v_permlane32_swap_b32 v84,v85
Mvvv 108,224,208
LN 1,36,59,1024
Mvvv 112,224,212
s_nop 0
v_permlane32_swap_b32 v88,v89
v_permlane32_swap_b32 v92,v93
v_permlane32_swap_b32 v96,v97
v_max_f32 v82,v80,v81
v_max_f32 v80,v80,v81
v_max_f32 v86,v84,v85
v_max_f32 v84,v84,v85
v_max_f32 v90,v88,v89
v_max_f32 v88,v88,v89
v_max_f32 v94,v92,v93
v_max_f32 v92,v92,v93
v_max_f32 v98,v96,v97
v_max_f32 v96,v96,v97
v_permlane16_swap_b32 v80,v82
v_permlane16_swap_b32 v84,v86
v_permlane16_swap_b32 v88,v90
v_permlane16_swap_b32 v92,v94
v_permlane16_swap_b32 v96,v98
v_min_f32_e64 v81,-v80,-v82
v_min_f32_e64 v85,-v84,-v86
v_min_f32_e64 v89,-v88,-v90
v_min_f32_e64 v93,-v92,-v94
v_min_f32_e64 v97,-v96,-v98
v_min_f32_e64 v83,-v80,-v82
v_min_f32_e64 v87,-v84,-v86
v_min_f32_e64 v91,-v88,-v90
v_min_f32_e64 v95,-v92,-v94
v_min_f32_e64 v99,-v96,-v98
v_min_f32_e64 v80,-v80,-v82
v_min_f32_e64 v84,-v84,-v86
v_min_f32_e64 v88,-v88,-v90
v_min_f32_e64 v92,-v92,-v94
v_min_f32_e64 v96,-v96,-v98
v_mov_b32 v82,v80
v_mov_b32 v86,v84
v_mov_b32 v90,v88
v_mov_b32 v94,v92
v_mov_b32 v98,v96
Mvvv 116,224,216
WL 1
v_add_f32 v140,v140,v80
v_add_f32 v141,v141,v80
Nvav 160,240,20,80
s_nop 0
Ra 252,7,33792
v_add_f32 v142,v142,v80
Nvav 164,240,24,84
LN 1,36,60,2048
Nvav 168,240,28,88
s_nop 0
v_add_f32 v143,v143,v80
Nvav 172,240,32,92
EX 140
Nvav 176,240,36,96
s_nop 0
EX 141
Mvav 160,244,40
Rv 220,6,34816
Mvav 164,244,44
LN 1,36,61,3072
Mvav 168,244,48
s_nop 0
EX 142
Mvav 172,244,52
EX 143
Mvav 176,244,56
WL 1
CV 140,140,141
CV 141,142,143
Mvav 160,248,60
s_nop 0
Rv 224,7,34816
v_add_f32 v144,v144,v84
Mvav 164,248,64
s_nop 0
v_add_f32 v145,v145,v84
v_add_f32 v146,v146,v84
Mvav 168,248,68
v_add_f32 v147,v147,v84
Mvav 172,248,72
EX 144
Mvav 176,248,76
EX 145
Mvav 160,252,200
Ra 240,6,35840
Mvav 164,252,204
EX 146
Mvav 168,252,208
EX 147
Mvav 172,252,212
CV 144,144,145
CV 145,146,147
v_add_f32 v148,v148,v88
v_add_f32 v149,v149,v88
v_add_f32 v150,v150,v88
v_add_f32 v151,v151,v88
EX 148
EX 149
EX 150
EX 151
CV 148,148,149
CV 149,150,151
v_add_f32 v152,v152,v92
v_add_f32 v153,v153,v92
v_add_f32 v154,v154,v92
v_add_f32 v155,v155,v92
EX 152
EX 153
EX 154
EX 155
CV 152,152,153
CV 153,154,155
v_add_f32 v156,v156,v96
v_add_f32 v157,v157,v96
v_add_f32 v158,v158,v96
v_add_f32 v159,v159,v96
EX 156
EX 157
EX 158
EX 159
CV 156,156,157
CV 157,158,159
v_add_f32 v100,v100,v80
v_add_f32 v101,v101,v80
v_add_f32 v102,v102,v80
v_add_f32 v103,v103,v80
EX 100
EX 101
EX 102
EX 103
CV 142,100,101
CV 143,102,103
Mvav 176,252,216
v_add_f32 v104,v104,v84
v_add_f32 v105,v105,v84
v_add_f32 v106,v106,v84
v_add_f32 v107,v107,v84
EX 104
EX 105
EX 106
EX 107
CV 146,104,105
CV 147,106,107
WL 1
Nvvv 100,220,20,80
v_add_f32 v108,v108,v88
v_add_f32 v109,v109,v88
v_add_f32 v110,v110,v88
v_add_f32 v111,v111,v88
EX 108
EX 109
EX 110
EX 111
CV 150,108,109
CV 151,110,111
Nvvv 104,220,24,84
v_add_f32 v112,v112,v92
v_add_f32 v113,v113,v92
v_add_f32 v114,v114,v92
v_add_f32 v115,v115,v92
EX 112
EX 113
EX 114
EX 115
CV 154,112,113
CV 155,114,115
Nvvv 108,220,28,88
v_add_f32 v116,v116,v96
v_add_f32 v117,v117,v96
v_add_f32 v118,v118,v96
v_add_f32 v119,v119,v96
EX 116
EX 117
EX 118
EX 119
CV 158,116,117
CV 159,118,119
Nvvv 112,220,32,92
Ra 244,7,35840
Nvvv 116,220,36,96
v_rcp_f32 v240,v240
Mvvv 100,224,40
v_mul_f32 v245,0x4d800000,v245
Mvvv 104,224,44
v_rcp_f32 v244,v244
Mvvv 108,224,48
v_sub_f32 v229,v229,v229
v_sub_f32 v233,v233,v233
Mvvv 112,224,52
v_sub_f32 v237,v237,v237
v_sub_f32 v241,v241,v241
Mvvv 116,224,56
WL 1
v_sub_f32 v245,v245,v245
v_add_f32 v0,v228,v229
Mvav 100,240,60
s_nop 0
Ta 248,8,32768
Ta 250,8,36864
Mvav 104,240,64
v_add_f32 v3,v232,v233
v_add_f32 v9,v236,v237
Mvav 108,240,68
v_add_f32 v10,v240,v241
v_add_f32 v11,v244,v245
Mvav 112,240,72
AR 120,0
AR 12,1
Mvav 116,240,76
WL 2
v_mul_f32 v120,v0,v120
v_mul_f32 v12,v0,v12
Mvav 100,244,200
s_nop 0
Ta 252,252,32768
Ta 254,252,36864
Mvav 104,244,204
CV 120,120,12
AR 121,2
Mvav 108,244,208
AR 253,3
v_mul_f32 v121,v0,v121
Mvav 112,244,212
v_mul_f32 v253,v0,v253
CV 121,121,253
Mvav 116,244,216
AR 122,20
AR 12,21
Zvvv 228,248,140
s_nop 0
Tv 220,8,33280
Tv 222,8,37376
Zvvv 232,248,144
v_mul_f32 v122,v0,v122
v_mul_f32 v12,v0,v12
Zvvv 236,248,148
CV 122,122,12
AR 123,22
Zvvv 240,248,152
AR 253,23
v_mul_f32 v123,v0,v123
Zvvv 244,248,156
WL 2
v_mul_f32 v253,v0,v253
CV 123,123,253
Zaav 160,248,140
s_nop 0
Tv 224,252,33280
Tv 226,252,37376
Zaav 164,248,144
AR 124,4
AR 12,5
Zaav 168,248,148
v_mul_f32 v124,v3,v124
v_mul_f32 v12,v3,v12
Zaav 172,248,152
CV 124,124,12
AR 125,6
Zaav 176,248,156
AR 253,7
v_mul_f32 v125,v3,v125
Zaav 180,252,140
s_nop 0
Ta 240,8,33792
Ta 242,8,37888
Zaav 184,252,144
v_mul_f32 v253,v3,v253
CV 125,125,253
Zaav 188,252,148
AR 126,24
AR 12,25
Zaav 192,252,152
v_mul_f32 v126,v3,v126
v_mul_f32 v12,v3,v12
Zaav 196,252,156
WL 2
CV 126,126,12
AR 127,26
Zavv 200,220,140
s_nop 0
Ta 244,252,33792
Ta 246,252,37888
Zavv 204,220,144
AR 253,27
v_mul_f32 v127,v3,v127
Zavv 208,220,148
v_mul_f32 v253,v3,v253
CV 127,127,253
Zavv 212,220,152
AR 128,8
AR 12,9
Zavv 216,220,156
v_mul_f32 v128,v9,v128
v_mul_f32 v12,v9,v12
Zavv 220,224,140
s_nop 0
Ta 248,8,34304
Ta 250,8,38400
Zavv 224,224,144
CV 128,128,12
AR 129,10
Zavv 228,224,148
AR 253,11
v_mul_f32 v129,v9,v129
Zavv 232,224,152
v_mul_f32 v253,v9,v253
CV 129,129,253
Zavv 236,224,156
WL 2
Ta 252,252,34304
Ta 254,252,38400
Zaav 0,240,140
AR 130,28
AR 12,29
v_mul_f32 v130,v9,v130
v_mul_f32 v12,v9,v12
CV 130,130,12
AR 131,30
AR 253,31
v_mul_f32 v131,v9,v131
v_mul_f32 v253,v9,v253
CV 131,131,253
Zaav 4,240,144
AR 132,12
AR 12,13
v_mul_f32 v132,v10,v132
v_mul_f32 v12,v10,v12
CV 132,132,12
AR 133,14
AR 253,15
v_mul_f32 v133,v10,v133
v_mul_f32 v253,v10,v253
CV 133,133,253
AR 134,32
AR 12,33
v_mul_f32 v134,v10,v134
v_mul_f32 v12,v10,v12
CV 134,134,12
AR 135,34
AR 253,35
v_mul_f32 v135,v10,v135
v_mul_f32 v253,v10,v253
CV 135,135,253
Zaav 8,240,148
AR 136,16
AR 12,17
v_mul_f32 v136,v11,v136
v_mul_f32 v12,v11,v12
CV 136,136,12
AR 137,18
AR 253,19
v_mul_f32 v137,v11,v137
v_mul_f32 v253,v11,v253
CV 137,137,253
AR 138,36
AR 12,37
v_mul_f32 v138,v11,v138
v_mul_f32 v12,v11,v12
CV 138,138,12
AR 139,38
AR 253,39
v_mul_f32 v139,v11,v139
v_mul_f32 v253,v11,v253
CV 139,139,253
Zaav 12,240,152
AR 180,40
AR 12,41
Zaav 16,240,156
v_mul_f32 v180,v0,v180
v_mul_f32 v12,v0,v12
Zaav 20,244,140
CV 180,180,12
AR 181,42
Zaav 24,244,144
AR 253,43
v_mul_f32 v181,v0,v181
Zaav 28,244,148
v_mul_f32 v253,v0,v253
CV 181,181,253
Zaav 32,244,152
AR 182,60
AR 12,61
v_mul_f32 v182,v0,v182
v_mul_f32 v12,v0,v12
CV 182,182,12
AR 183,62
AR 253,63
v_mul_f32 v183,v0,v183
v_mul_f32 v253,v0,v253
CV 183,183,253
Zaav 36,244,156
s_waitcnt vmcnt(12) lgkmcnt(0)
s_barrier
AR 184,44
AR 12,45
v_mul_f32 v184,v3,v184
v_mul_f32 v12,v3,v12
CV 184,184,12
AR 185,46
AR 253,47
v_mul_f32 v185,v3,v185
v_mul_f32 v253,v3,v253
CV 185,185,253
AR 186,64
AR 12,65
v_mul_f32 v186,v3,v186
v_mul_f32 v12,v3,v12
CV 186,186,12
AR 187,66
AR 253,67
v_mul_f32 v187,v3,v187
v_mul_f32 v253,v3,v253
CV 187,187,253
Zaav 40,248,140
AR 188,48
AR 12,49
v_mul_f32 v188,v9,v188
v_mul_f32 v12,v9,v12
CV 188,188,12
AR 189,50
AR 253,51
v_mul_f32 v189,v9,v189
v_mul_f32 v253,v9,v253
CV 189,189,253
AR 190,68
AR 12,69
v_mul_f32 v190,v9,v190
v_mul_f32 v12,v9,v12
CV 190,190,12
AR 191,70
AR 253,71
v_mul_f32 v191,v9,v191
v_mul_f32 v253,v9,v253
CV 191,191,253
Zaav 44,248,144
AR 192,52
AR 12,53
v_mul_f32 v192,v10,v192
v_mul_f32 v12,v10,v12
CV 192,192,12
AR 193,54
AR 253,55
v_mul_f32 v193,v10,v193
v_mul_f32 v253,v10,v253
CV 193,193,253
AR 194,72
AR 12,73
v_mul_f32 v194,v10,v194
v_mul_f32 v12,v10,v12
CV 194,194,12
AR 195,74
AR 253,75
v_mul_f32 v195,v10,v195
v_mul_f32 v253,v10,v253
CV 195,195,253
Zaav 48,248,148
AR 196,56
AR 12,57
v_mul_f32 v196,v11,v196
v_mul_f32 v12,v11,v12
CV 196,196,12
AR 197,58
AR 253,59
v_mul_f32 v197,v11,v197
v_mul_f32 v253,v11,v253
CV 197,197,253
AR 198,76
AR 12,77
v_mul_f32 v198,v11,v198
v_mul_f32 v12,v11,v12
CV 198,198,12
AR 199,78
AR 253,79
v_mul_f32 v199,v11,v199
v_mul_f32 v253,v11,v253
CV 199,199,253
Zaav 52,248,152
s_add_u32 m0,s76,40960
Rv 220,6,0
Rv 224,7,0
Zaav 56,248,156
Ra 240,6,1024
AR 254,80
Zaav 60,252,140
s_cmp_eq_u32 s88,4
s_cselect_b32 s86,0x20000,0
Ra 244,7,1024
Zaav 64,252,144
v_subrev_u32 v1,s86,v1
v_add_u32 v1,8192,v1
Zaav 68,252,148
LN 1,36,58,0
Zaav 72,252,152
EX 160
Zaav 76,252,156
WL 3
EX 161
Nvvv 140,220,20,80
WL 2
EX 162
Mvvv 140,224,40
WL 1
LN 1,36,59,1024
Mvav 140,240,60
WL 0
EX 163
Mvav 140,244,200
CV 160,160,161
CV 161,162,163
Nvvv 144,220,24,84
EX 164
Mvvv 144,224,44
LN 1,36,60,2048
Mvav 144,240,64
EX 165
Mvav 144,244,204
EX 166
Nvvv 148,220,28,88
EX 167
Mvvv 148,224,48
LN 1,36,61,3072
Mvav 148,240,68
s_add_u32 m0,s76,90112
Ta 248,8,16384
Ta 250,8,20480
Mvav 148,244,208
s_cmp_eq_u32 s88,4
s_cselect_b32 s86,0x20000,0
v_subrev_u32 v2,s86,v2
Nvvv 152,220,32,92
v_add_u32 v2,8192,v2
CV 164,164,165
Mvvv 152,224,52
LN 2,96,62,0
Mvav 152,240,72
CV 165,166,167
AR 253,81
Mvav 152,244,212
s_nop 0
Ta 252,252,16384
Ta 254,252,20480
Nvvv 156,220,36,96
EX 168
Mvvv 156,224,56
EX 169
EX 170
EX 171
CV 168,168,169
CV 169,170,171
EX 172
EX 173
EX 174
EX 175
CV 172,172,173
CV 173,174,175
EX 176
EX 177
EX 178
EX 179
CV 176,176,177
CV 177,178,179
EX 100
EX 101
EX 102
EX 103
CV 162,100,101
CV 163,102,103
Mvav 156,240,76
EX 104
EX 105
EX 106
EX 107
CV 166,104,105
CV 167,106,107
Mvav 156,244,216
EX 108
EX 109
EX 110
EX 111
CV 170,108,109
CV 171,110,111
Mvvv 228,248,160
EX 112
EX 113
EX 114
EX 115
CV 174,112,113
CV 175,114,115
Mvvv 232,248,164
EX 116
EX 117
EX 118
EX 119
CV 178,116,117
CV 179,118,119
Mvvv 236,248,168
s_nop 0
Tv 220,8,16896
Tv 222,8,20992
Mvvv 240,248,172
LN 2,96,63,1024
Mvvv 244,248,176
WL 2
EX 140
Maav 160,248,160
s_nop 0
Tv 224,252,16896
Tv 226,252,20992
Maav 164,248,164
EX 141
Maav 168,248,168
LN 2,96,62,2048
Maav 172,248,172
EX 142
Maav 176,248,176
EX 143
Maav 180,252,160
Ra 240,6,2048
CV 140,140,141
Maav 184,252,164
Ra 244,7,2048
CV 141,142,143
Maav 188,252,168
Ra 248,6,3072
v_mul_f32 v254,v0,v254
Maav 192,252,172
LN 2,96,63,3072
Maav 196,252,176
WL 3
Ra 252,7,3072
v_mul_f32 v253,v0,v253
Mavv 200,220,160
EX 144
Mavv 204,220,164
EX 145
Mavv 208,220,168
EX 146
Mavv 212,220,172
EX 147
Mavv 216,220,176
CV 144,144,145
CV 145,146,147
Mavv 220,224,160
EX 148
Mavv 224,224,164
EX 149
Mavv 228,224,168
EX 150
Mavv 232,224,172
EX 151
Mavv 236,224,176
WL 0
CV 148,148,149
CV 149,150,151
Nvav 100,240,20,80
EX 152
Mvav 100,244,40
EX 153
Mvav 100,248,60
EX 154
Mvav 100,252,200
EX 155
Nvav 104,240,24,84
CV 152,152,153
CV 153,154,155
Mvav 104,244,44
s_nop 0
Tv 220,8,17408
Tv 222,8,21504
Mvav 104,248,64
EX 156
Mvav 104,252,204
EX 157
Nvav 108,240,28,88
EX 158
Mvav 108,244,48
EX 159
Mvav 108,248,68
s_nop 0
Tv 224,252,17408
Tv 226,252,21504
Mvav 108,252,208
CV 156,156,157
CV 157,158,159
Nvav 112,240,32,92
EX 100
Mvav 112,244,52
EX 101
Mvav 112,248,72
EX 102
Mvav 112,252,212
EX 103
Nvav 116,240,36,96
s_nop 0
Ta 240,8,17920
Ta 242,8,22016
Mvav 116,244,56
CV 142,100,101
CV 143,102,103
Mvav 116,248,76
EX 104
Mvav 116,252,216
WL 2
Ta 244,252,17920
Ta 246,252,22016
Mavv 0,220,160
EX 105
Mavv 4,220,164
EX 106
Mavv 8,220,168
EX 107
Mavv 12,220,172
CV 146,104,105
CV 147,106,107
Mavv 16,220,176
EX 108
Mavv 20,224,160
EX 109
Mavv 24,224,164
EX 110
Mavv 28,224,168
EX 111
Mavv 32,224,172
CV 150,108,109
CV 151,110,111
Mavv 36,224,176
s_waitcnt vmcnt(8) lgkmcnt(0)
s_barrier
Maav 40,240,160
s_add_u32 m0,s76,8192
Ra 248,6,16384
Ra 252,7,16384
Maav 44,240,164
s_cmp_eq_u32 s88,5
s_cselect_b32 s86,0x20000,0
Rv 220,6,17408
Maav 48,240,168
s_nop 0
Rv 224,7,17408
v_subrev_u32 v1,s86,v1
Maav 52,240,172
v_add_u32 v1,8192,v1
CV 254,254,253
Maav 56,240,176
LN 1,36,58,0
Maav 60,244,160
EX 112
Maav 64,244,164
EX 113
Maav 68,244,168
EX 114
Maav 72,244,172
LN 1,36,59,1024
Maav 76,244,176
WL 1
EX 115
Nvav 160,248,20,80
CV 154,112,113
CV 155,114,115
Mvav 160,252,40
EX 116
Mvvv 160,220,60
WL 0
LN 1,36,60,2048
Mvvv 160,224,200
EX 117
Nvav 164,248,24,84
EX 118
Mvav 164,252,44
EX 119
Mvvv 164,220,64
LN 1,36,61,3072
Mvvv 164,224,204
s_add_u32 m0,s76,73728
s_cmp_eq_u32 s88,5
s_cselect_b32 s86,0x20000,0
Nvav 168,248,28,88
v_subrev_u32 v2,s86,v2
v_add_u32 v2,8192,v2
Mvav 168,252,48
CV 158,116,117
CV 159,118,119
Mvvv 168,220,68
s_nop 0
Ta 240,8,0
Ta 242,8,4096
Mvvv 168,224,208
LN 2,96,62,0
Nvav 172,248,32,92
EX 160
Mvav 172,252,52
EX 161
Mvvv 172,220,72
EX 162
Mvvv 172,224,212
s_nop 0
Ta 244,252,0
Ta 246,252,4096
Nvav 176,248,36,96
LN 2,96,63,1024
Mvav 176,252,56
EX 163
Mvvv 176,220,76
CV 160,160,161
CV 161,162,163
Mvvv 176,224,216
EX 164
Mvvv 228,248,140
s_nop 0
Ta 248,8,512
Ta 250,8,4608
Mvvv 232,248,144
LN 2,96,62,2048
Mvvv 236,248,148
EX 165
Mvvv 240,248,152
EX 166
Mvvv 244,248,156
WL 2
EX 167
Maav 160,240,140
s_nop 0
Ta 252,252,512
Ta 254,252,4608
Maav 164,240,144
LN 2,96,63,3072
Maav 168,240,148
CV 164,164,165
CV 165,166,167
Maav 172,240,152
EX 168
Maav 176,240,156
EX 169
Maav 180,244,140
Rv 220,6,18432
AW 80,254
Maav 184,244,144
Rv 224,7,18432
AR 12,82
Maav 188,244,148
Ra 240,6,19456
AR 253,83
Maav 192,244,152
EX 170
Maav 196,244,156
WL 3
Ra 244,7,19456
v_mul_f32 v12,v0,v12
Maav 200,248,140
EX 171
Maav 204,248,144
CV 168,168,169
CV 169,170,171
Maav 208,248,148
EX 172
Maav 212,248,152
EX 173
Maav 216,248,156
EX 174
Maav 220,252,140
EX 175
Maav 224,252,144
CV 172,172,173
CV 173,174,175
Maav 228,252,148
EX 176
Maav 232,252,152
EX 177
Maav 236,252,156
WL 0
EX 178
Nvvv 100,220,20,80
EX 179
Mvvv 100,224,40
CV 176,176,177
CV 177,178,179
Mvav 100,240,60
ds_write_b128 v15,v[120:123] offset:53248
Mvav 100,244,200
v_mul_f32 v253,v0,v253
CV 12,12,253
Nvvv 104,220,24,84
AW 81,12
AR 254,100
Mvvv 104,224,44
AR 253,101
v_mul_f32 v254,v0,v254
Mvav 104,240,64
v_mul_f32 v253,v0,v253
CV 254,254,253
Mvav 104,244,204
Ta 248,8,1024
AW 82,254
Nvvv 108,220,28,88
Ta 250,8,5120
AR 12,102
Mvvv 108,224,48
EX 100
Mvav 108,240,68
s_nop 0
Ta 252,252,1024
Ta 254,252,5120
Mvav 108,244,208
EX 101
Nvvv 112,220,32,92
EX 102
Mvvv 112,224,52
EX 103
Mvav 112,240,72
CV 162,100,101
CV 163,102,103
Mvav 112,244,212
EX 104
Nvvv 116,220,36,96
s_nop 0
Tv 220,8,1536
Tv 222,8,5632
Mvvv 116,224,56
EX 105
Mvav 116,240,76
EX 106
Mvav 116,244,216
WL 2
Tv 224,252,1536
Tv 226,252,5632
Maav 0,248,140
EX 107
Maav 4,248,144
CV 166,104,105
CV 167,106,107
Maav 8,248,148
EX 108
Maav 12,248,152
EX 109
Maav 16,248,156
EX 110
Maav 20,252,140
EX 111
Maav 24,252,144
CV 170,108,109
CV 171,110,111
Maav 28,252,148
EX 112
Maav 32,252,152
EX 113
Maav 36,252,156
s_waitcnt vmcnt(12) lgkmcnt(0)
s_barrier
Mavv 40,220,140
s_add_u32 m0,s76,24576
Ra 240,6,32768
Ra 244,7,32768
Mavv 44,220,144
s_cmp_eq_u32 s88,6
s_cselect_b32 s86,0x20000,0
Ra 248,6,33792
Mavv 48,220,148
s_nop 0
Ra 252,7,33792
v_subrev_u32 v1,s86,v1
Mavv 52,220,152
v_add_u32 v1,8192,v1
AR 253,103
Mavv 56,220,156
LN 1,36,58,0
Mavv 60,224,140
EX 114
Mavv 64,224,144
EX 115
Mavv 68,224,148
CV 174,112,113
CV 175,114,115
Mavv 72,224,152
LN 1,36,59,1024
Mavv 76,224,156
WL 1
EX 116
Nvav 140,240,20,80
EX 117
Mvav 140,244,40
EX 118
Mvav 140,248,60
WL 0
LN 1,36,60,2048
Mvav 140,252,200
EX 119
Nvav 144,240,24,84
CV 178,116,117
CV 179,118,119
Mvav 144,244,44
ds_write_b128 v15,v[124:127] offset:54272
Mvav 144,248,64
LN 1,36,61,3072
Mvav 144,252,204
s_add_u32 m0,s76,57344
s_cmp_eq_u32 s88,6
s_cselect_b32 s86,0x20000,0
Nvav 148,240,28,88
v_subrev_u32 v2,s86,v2
v_add_u32 v2,8192,v2
Mvav 148,244,48
EX 140
Mvav 148,248,68
Tv 220,8,49152
v_mul_f32 v12,v0,v12
Mvav 148,252,208
Tv 222,8,53248
v_mul_f32 v253,v0,v253
Nvav 152,240,32,92
LN 2,96,62,0
Mvav 152,244,52
EX 141
Mvav 152,248,72
EX 142
Mvav 152,252,212
s_nop 0
Tv 224,252,49152
Tv 226,252,53248
Nvav 156,240,36,96
LN 2,96,63,1024
Mvav 156,244,56
EX 143
Mvav 156,248,76
CV 140,140,141
CV 141,142,143
Mvav 156,252,216
EX 144
Mvvv 228,248,160
s_nop 0
Ta 240,8,49664
Ta 242,8,53760
Mvvv 232,248,164
LN 2,96,62,2048
Mvvv 236,248,168
EX 145
Mvvv 240,248,172
EX 146
Mvvv 244,248,176
WL 2
EX 147
Mavv 160,220,160
s_nop 0
Ta 244,252,49664
Ta 246,252,53760
Mavv 164,220,164
LN 2,96,63,3072
Mavv 168,220,168
CV 144,144,145
CV 145,146,147
Mavv 172,220,172
EX 148
Mavv 176,220,176
EX 149
Mavv 180,224,160
Ra 248,6,34816
CV 12,12,253
Mavv 184,224,164
Ra 252,7,34816
AW 83,12
Mavv 188,224,168
Rv 220,6,35840
AR 254,84
Mavv 192,224,172
EX 150
Mavv 196,224,176
WL 3
Rv 224,7,35840
AR 253,85
Maav 200,240,160
EX 151
Maav 204,240,164
CV 148,148,149
CV 149,150,151
Maav 208,240,168
EX 152
Maav 212,240,172
EX 153
Maav 216,240,176
EX 154
Maav 220,244,160
EX 155
Maav 224,244,164
CV 152,152,153
CV 153,154,155
Maav 228,244,168
EX 156
Maav 232,244,172
EX 157
Maav 236,244,176
WL 0
EX 158
Nvav 100,248,20,80
EX 159
Mvav 100,252,40
CV 156,156,157
CV 157,158,159
Mvvv 100,220,60
ds_write_b128 v15,v[128:131] offset:55296
Mvvv 100,224,200
v_mul_f32 v254,v3,v254
v_mul_f32 v253,v3,v253
Nvav 104,248,24,84
CV 254,254,253
AW 84,254
Mvav 104,252,44
AR 12,86
AR 253,87
Mvvv 104,220,64
v_mul_f32 v12,v3,v12
v_mul_f32 v253,v3,v253
Mvvv 104,224,204
Ta 240,8,50176
CV 12,12,253
Nvav 108,248,28,88
Ta 242,8,54272
AW 85,12
Mvav 108,252,48
EX 100
Mvvv 108,220,68
s_nop 0
Ta 244,252,50176
Ta 246,252,54272
Mvvv 108,224,208
EX 101
Nvav 112,248,32,92
EX 102
Mvav 112,252,52
EX 103
Mvvv 112,220,72
CV 142,100,101
CV 143,102,103
Mvvv 112,224,212
EX 104
Nvav 116,248,36,96
s_nop 0
Ta 248,8,50688
Ta 250,8,54784
Mvav 116,252,56
EX 105
Mvvv 116,220,76
EX 106
Mvvv 116,224,216
WL 2
Ta 252,252,50688
Ta 254,252,54784
Maav 0,240,160
EX 107
Maav 4,240,164
CV 146,104,105
CV 147,106,107
Maav 8,240,168
EX 108
Maav 12,240,172
EX 109
Maav 16,240,176
EX 110
Maav 20,244,160
EX 111
Maav 24,244,164
CV 150,108,109
CV 151,110,111
Maav 28,244,168
EX 112
Maav 32,244,172
EX 113
Maav 36,244,176
s_waitcnt vmcnt(12) lgkmcnt(0)
s_barrier
Maav 40,248,160
s_add_u32 m0,s76,40960
Rv 220,6,0
Rv 224,7,0
Maav 44,248,164
s_cmp_eq_u32 s88,7
s_cselect_b32 s86,0x20000,0
Ra 240,6,1024
Maav 48,248,168
Ra 244,7,1024
v_subrev_u32 v1,s86,v1
Maav 52,248,172
v_add_u32 v1,8192,v1
AR 254,104
Maav 56,248,176
LN 1,36,58,0
Maav 60,252,160
EX 114
Maav 64,252,164
EX 115
Maav 68,252,168
CV 154,112,113
CV 155,114,115
Maav 72,252,172
LN 1,36,59,1024
Maav 76,252,176
WL 1
EX 116
Nvvv 160,220,20,80
EX 117
Mvvv 160,224,40
EX 118
Mvav 160,240,60
WL 0
LN 1,36,60,2048
Mvav 160,244,200
EX 119
Nvvv 164,220,24,84
CV 158,116,117
CV 159,118,119
Mvvv 164,224,44
ds_write_b128 v15,v[132:135] offset:56320
Mvav 164,240,64
LN 1,36,61,3072
Mvav 164,244,204
s_add_u32 m0,s76,106496
s_cmp_eq_u32 s88,7
s_cselect_b32 s86,0x20000,0
Nvvv 168,220,28,88
v_subrev_u32 v2,s86,v2
v_add_u32 v2,8192,v2
Mvvv 168,224,48
EX 160
Mvav 168,240,68
Ta 248,8,32768
AR 253,105
Mvav 168,244,208
Ta 250,8,36864
v_mul_f32 v254,v3,v254
Nvvv 172,220,32,92
LN 2,96,62,0
Mvvv 172,224,52
EX 161
Mvav 172,240,72
EX 162
Mvav 172,244,212
s_nop 0
Ta 252,252,32768
Ta 254,252,36864
Nvvv 176,220,36,96
LN 2,96,63,1024
Mvvv 176,224,56
EX 163
Mvav 176,240,76
CV 160,160,161
CV 161,162,163
Mvav 176,244,216
EX 164
Mvvv 228,248,140
s_nop 0
Tv 220,8,33280
Tv 222,8,37376
Mvvv 232,248,144
LN 2,96,62,2048
Mvvv 236,248,148
EX 165
Mvvv 240,248,152
EX 166
Mvvv 244,248,156
WL 2
EX 167
Maav 160,248,140
s_nop 0
Tv 224,252,33280
Tv 226,252,37376
Maav 164,248,144
LN 2,96,63,3072
Maav 168,248,148
s_mul_i32 s86,%8,73728
s_add_u32 s68,s86,0
s_add_u32 s69,s86,8192
Maav 172,248,152
s_add_u32 s70,s86,16384
s_add_u32 s71,s86,24576
s_add_u32 s72,s86,36864
Maav 176,248,156
s_add_u32 s73,s86,45056
s_add_u32 s74,s86,53248
Maav 180,252,140
s_add_u32 s75,s86,61440
s_mov_b64 s[28:29],%6
Ra 240,6,2048
Maav 184,252,144
s_sub_u32 s28,s28,1152
s_subb_u32 s29,s29,0
Ra 244,7,2048
Maav 188,252,148
Ra 248,6,3072
CV 164,164,165
Maav 192,252,152
CV 165,166,167
v_mul_f32 v253,v3,v253
Maav 196,252,156
WL 3
Ra 252,7,3072
CV 254,254,253
Mavv 200,220,140
EX 168
Mavv 204,220,144
EX 169
Mavv 208,220,148
EX 170
Mavv 212,220,152
EX 171
Mavv 216,220,156
CV 168,168,169
CV 169,170,171
Mavv 220,224,140
EX 172
Mavv 224,224,144
EX 173
Mavv 228,224,148
EX 174
Mavv 232,224,152
EX 175
Mavv 236,224,156
WL 0
CV 172,172,173
CV 173,174,175
Nvav 100,240,20,80
EX 176
Mvav 100,244,40
EX 177
Mvav 100,248,60
EX 178
Mvav 100,252,200
EX 179
Nvav 104,240,24,84
CV 176,176,177
CV 177,178,179
Mvav 104,244,44
s_nop 0
Tv 220,8,33792
Tv 222,8,37888
Mvav 104,248,64
ds_write_b128 v15,v[136:139] offset:57344
Mvav 104,252,204
EX 100
Nvav 108,240,28,88
EX 101
Mvav 108,244,48
EX 102
)ASM"
R"ASM(Mvav 108,248,68
EX 103
Mvav 108,252,208
Tv 224,252,33792
CV 162,100,101
Nvav 112,240,32,92
Tv 226,252,37888
CV 163,102,103
Mvav 112,244,52
EX 104
Mvav 112,248,72
EX 105
Mvav 112,252,212
EX 106
Nvav 116,240,36,96
s_nop 0
Ta 240,8,34304
Ta 242,8,38400
Mvav 116,244,56
EX 107
Mvav 116,248,76
CV 166,104,105
CV 167,106,107
Mvav 116,252,216
WL 3
Ta 244,252,34304
Ta 246,252,38400
Mavv 0,220,140
EX 108
Mavv 4,220,144
EX 109
Mavv 8,220,148
EX 110
Mavv 12,220,152
EX 111
Mavv 16,220,156
WL 2
CV 170,108,109
CV 171,110,111
Mavv 20,224,140
EX 112
Mavv 24,224,144
EX 113
Mavv 28,224,148
EX 114
Mavv 32,224,152
EX 115
Mavv 36,224,156
s_waitcnt vmcnt(12) lgkmcnt(0)
s_barrier
Maav 40,240,140
s_add_u32 m0,s76,8192
Ra 248,6,16384
Ra 252,7,16384
Maav 44,240,144
s_cmp_eq_u32 s88,8
s_cselect_b32 s86,0x20000,0
Rv 220,6,17408
Maav 48,240,148
s_nop 0
Rv 224,7,17408
v_subrev_u32 v1,s86,v1
Maav 52,240,152
s_nop 0
v_add_u32 v1,8192,v1
CV 174,112,113
Maav 56,240,156
LN 1,36,58,0
Maav 60,244,140
s_nop 0
CV 175,114,115
AW 86,254
Maav 64,244,144
s_nop 0
EX 116
Maav 68,244,148
EX 117
Maav 72,244,152
LN 1,36,59,1024
Maav 76,244,156
WL 1
EX 118
Nvav 140,248,20,80
EX 119
Mvav 140,252,40
CV 178,116,117
CV 179,118,119
Mvvv 140,220,60
WL 0
LN 1,36,60,2048
Mvvv 140,224,200
ds_write_b128 v15,v[180:183] offset:58368
Nvav 144,248,24,84
AR 12,106
AR 253,107
Mvav 144,252,44
v_mul_f32 v12,v3,v12
v_mul_f32 v253,v3,v253
Mvvv 144,220,64
LN 1,36,61,3072
Mvvv 144,224,204
s_add_u32 m0,s76,90112
s_cmp_eq_u32 s88,8
s_cselect_b32 s86,0x20000,0
Nvav 148,248,28,88
v_subrev_u32 v2,s86,v2
v_add_u32 v2,8192,v2
Mvav 148,252,48
EX 140
Mvvv 148,220,68
s_nop 0
Ta 240,8,16384
Ta 242,8,20480
Mvvv 148,224,208
LN 2,96,62,0
Nvav 152,248,32,92
EX 141
Mvav 152,252,52
EX 142
Mvvv 152,220,72
EX 143
Mvvv 152,224,212
s_nop 0
Ta 244,252,16384
Ta 246,252,20480
Nvav 156,248,36,96
LN 2,96,63,1024
Mvav 156,252,56
CV 140,140,141
CV 141,142,143
Mvvv 156,220,76
EX 144
Mvvv 156,224,216
EX 145
Mvvv 228,248,160
s_nop 0
Ta 248,8,16896
Ta 250,8,20992
Mvvv 232,248,164
LN 2,96,62,2048
Mvvv 236,248,168
EX 146
Mvvv 240,248,172
EX 147
Mvvv 244,248,176
WL 2
CV 144,144,145
CV 145,146,147
Maav 160,240,160
s_nop 0
Ta 252,252,16896
Ta 254,252,20992
Maav 164,240,164
LN 2,96,63,3072
Maav 168,240,168
EX 148
Maav 172,240,172
EX 149
Maav 176,240,176
EX 150
Maav 180,244,160
Rv 220,6,18432
CV 12,12,253
Maav 184,244,164
Rv 224,7,18432
AW 87,12
Maav 188,244,168
Ra 240,6,19456
AR 254,88
Maav 192,244,172
EX 151
Maav 196,244,176
WL 3
Ra 244,7,19456
CV 148,148,149
Maav 200,248,160
CV 149,150,151
AR 253,89
Maav 204,248,164
EX 152
Maav 208,248,168
EX 153
Maav 212,248,172
EX 154
Maav 216,248,176
EX 155
Maav 220,252,160
CV 152,152,153
CV 153,154,155
Maav 224,252,164
EX 156
Maav 228,252,168
EX 157
Maav 232,252,172
EX 158
Maav 236,252,176
WL 0
EX 159
Nvvv 100,220,20,80
CV 156,156,157
CV 157,158,159
Mvvv 100,224,40
ds_write_b128 v15,v[184:187] offset:59392
Mvav 100,240,60
v_mul_f32 v254,v9,v254
v_mul_f32 v253,v9,v253
Mvav 100,244,200
CV 254,254,253
AW 88,254
Nvvv 104,220,24,84
AR 12,90
AR 253,91
Mvvv 104,224,44
v_mul_f32 v12,v9,v12
v_mul_f32 v253,v9,v253
Mvav 104,240,64
Ta 248,8,17408
CV 12,12,253
Mvav 104,244,204
Ta 250,8,21504
AW 89,12
Nvvv 108,220,28,88
EX 100
Mvvv 108,224,48
EX 101
Mvav 108,240,68
s_nop 0
Ta 252,252,17408
Ta 254,252,21504
Mvav 108,244,208
EX 102
Nvvv 112,220,32,92
EX 103
Mvvv 112,224,52
CV 142,100,101
CV 143,102,103
Mvav 112,240,72
EX 104
Mvav 112,244,212
EX 105
Nvvv 116,220,36,96
s_nop 0
Tv 220,8,17920
Tv 222,8,22016
Mvvv 116,224,56
EX 106
Mvav 116,240,76
EX 107
Mvav 116,244,216
WL 2
Tv 224,252,17920
Tv 226,252,22016
Maav 0,248,160
CV 146,104,105
CV 147,106,107
Maav 4,248,164
EX 108
Maav 8,248,168
EX 109
Maav 12,248,172
EX 110
Maav 16,248,176
EX 111
Maav 20,252,160
CV 150,108,109
CV 151,110,111
Maav 24,252,164
EX 112
Maav 28,252,168
EX 113
Maav 32,252,172
EX 114
Maav 36,252,176
s_waitcnt vmcnt(12) lgkmcnt(0)
s_barrier
Mavv 40,220,160
s_add_u32 m0,s76,24576
Ra 240,6,32768
Ra 244,7,32768
Mavv 44,220,164
s_cmp_eq_u32 s88,9
s_cselect_b32 s86,0x20000,0
Ra 248,6,33792
Mavv 48,220,168
Ra 252,7,33792
v_subrev_u32 v1,s86,v1
Mavv 52,220,172
v_add_u32 v1,8192,v1
AR 254,108
Mavv 56,220,176
LN 1,36,58,0
Mavv 60,224,160
EX 115
Mavv 64,224,164
CV 154,112,113
CV 155,114,115
Mavv 68,224,168
EX 116
Mavv 72,224,172
LN 1,36,59,1024
Mavv 76,224,176
WL 1
EX 117
Nvav 160,240,20,80
EX 118
Mvav 160,244,40
EX 119
Mvav 160,248,60
WL 0
LN 1,36,60,2048
Mvav 160,252,200
CV 158,116,117
CV 159,118,119
Nvav 164,240,24,84
ds_write_b128 v15,v[188:191] offset:60416
Mvav 164,244,44
AR 253,109
v_mul_f32 v254,v9,v254
Mvav 164,248,64
LN 1,36,61,3072
Mvav 164,252,204
s_add_u32 m0,s76,73728
s_cmp_eq_u32 s88,9
s_cselect_b32 s86,0x20000,0
Nvav 168,240,28,88
v_subrev_u32 v2,s86,v2
v_add_u32 v2,8192,v2
Mvav 168,244,48
ds_write_b128 v15,v[192:195] offset:61440
Mvav 168,248,68
LN 2,96,62,0
Mvav 168,252,208
EX 160
Nvav 172,240,32,92
EX 161
Mvav 172,244,52
EX 162
Mvav 172,248,72
LN 2,96,63,1024
Mvav 172,252,212
Tv 220,8,0
v_mul_f32 v253,v9,v253
Nvav 176,240,36,96
s_nop 0
Tv 222,8,4096
Tv 224,252,0
Mvav 176,244,56
Tv 226,252,4096
CV 254,254,253
Mvav 176,248,76
LN 2,96,62,2048
Mvav 176,252,216
ds_write_b128 v15,v[196:199] offset:62464
Mvvv 228,248,140
EX 163
Mvvv 232,248,144
CV 160,160,161
CV 161,162,163
Mvvv 236,248,148
LN 2,96,63,3072
Mvvv 240,248,152
EX 164
Mvvv 244,248,156
WL 2
Ta 240,8,512
AW 90,254
Mavv 160,220,140
s_nop 0
Ta 242,8,4608
Ta 244,252,512
Mavv 164,220,144
Ta 246,252,4608
AR 12,110
Mavv 168,220,148
EX 165
Mavv 172,220,152
EX 166
Mavv 176,220,156
WL 4
EX 167
Mavv 180,224,140
Ra 248,6,34816
CV 164,164,165
Mavv 184,224,144
Ra 252,7,34816
CV 165,166,167
Mavv 188,224,148
Rv 220,6,35840
AR 253,111
Mavv 192,224,152
EX 168
Mavv 196,224,156
WL 3
Rv 224,7,35840
v_mul_f32 v12,v9,v12
Maav 200,240,140
EX 169
Maav 204,240,144
EX 170
Maav 208,240,148
EX 171
Maav 212,240,152
CV 168,168,169
CV 169,170,171
Maav 216,240,156
EX 172
Maav 220,244,140
EX 173
Maav 224,244,144
EX 174
Maav 228,244,148
EX 175
Maav 232,244,152
CV 172,172,173
CV 173,174,175
Maav 236,244,156
WL 0
EX 176
Nvav 100,248,20,80
EX 177
Mvav 100,252,40
EX 178
Mvvv 100,220,60
EX 179
Mvvv 100,224,200
CV 176,176,177
CV 177,178,179
Nvav 104,248,24,84
v_mul_f32 v253,v9,v253
CV 12,12,253
Mvav 104,252,44
s_nop 0
Ta 240,8,1024
Ta 242,8,5120
Mvvv 104,220,64
AW 91,12
AR 254,92
Mvvv 104,224,204
EX 100
Nvav 108,248,28,88
EX 101
Mvav 108,252,48
EX 102
Mvvv 108,220,68
s_nop 0
Ta 244,252,1024
Ta 246,252,5120
Mvvv 108,224,208
EX 103
Nvav 112,248,32,92
CV 162,100,101
CV 163,102,103
Mvav 112,252,52
EX 104
Mvvv 112,220,72
EX 105
Mvvv 112,224,212
EX 106
Nvav 116,248,36,96
s_nop 0
Ta 248,8,1536
Ta 250,8,5632
Mvav 116,252,56
EX 107
Mvvv 116,220,76
CV 166,104,105
CV 167,106,107
Mvvv 116,224,216
WL 2
Ta 252,252,1536
Ta 254,252,5632
Maav 0,240,140
EX 108
Maav 4,240,144
EX 109
Maav 8,240,148
EX 110
Maav 12,240,152
EX 111
Maav 16,240,156
CV 170,108,109
CV 171,110,111
Maav 20,244,140
EX 112
Maav 24,244,144
EX 113
Maav 28,244,148
EX 114
Maav 32,244,152
EX 115
Maav 36,244,156
s_waitcnt vmcnt(12) lgkmcnt(0)
s_barrier
Maav 40,248,140
s_add_u32 m0,s76,40960
Rv 220,6,0
Rv 224,7,0
Maav 44,248,144
s_cmp_eq_u32 s88,10
s_cselect_b32 s86,0x20000,0
Ra 240,6,1024
Maav 48,248,148
s_nop 0
Ra 244,7,1024
v_subrev_u32 v1,s86,v1
Maav 52,248,152
s_nop 0
v_add_u32 v1,8192,v1
CV 174,112,113
Maav 56,248,156
LN 1,36,58,0
Maav 60,252,140
s_nop 0
CV 175,114,115
AR 253,93
Maav 64,252,144
s_nop 0
EX 116
Maav 68,252,148
EX 117
Maav 72,252,152
LN 1,36,59,1024
Maav 76,252,156
WL 1
EX 118
Nvvv 140,220,20,80
EX 119
Mvvv 140,224,40
CV 178,116,117
CV 179,118,119
Mvav 140,240,60
WL 0
LN 1,36,60,2048
Mvav 140,244,200
v_mul_f32 v254,v10,v254
v_mul_f32 v253,v10,v253
Nvvv 144,220,24,84
CV 254,254,253
AW 92,254
Mvvv 144,224,44
AR 12,94
AR 253,95
Mvav 144,240,64
LN 1,36,61,3072
Mvav 144,244,204
s_add_u32 m0,s76,57344
s_cmp_eq_u32 s88,10
s_cselect_b32 s86,0x20000,0
Nvvv 148,220,28,88
v_subrev_u32 v2,s86,v2
v_add_u32 v2,8192,v2
Mvvv 148,224,48
EX 140
Mvav 148,240,68
s_nop 0
Ta 248,8,49152
Ta 250,8,53248
Mvav 148,244,208
LN 2,96,62,0
Nvvv 152,220,32,92
EX 141
Mvvv 152,224,52
EX 142
Mvav 152,240,72
EX 143
Mvav 152,244,212
s_nop 0
Ta 252,252,49152
Ta 254,252,53248
Nvvv 156,220,36,96
LN 2,96,63,1024
Mvvv 156,224,56
CV 140,140,141
CV 141,142,143
Mvav 156,240,76
EX 144
Mvav 156,244,216
EX 145
Mvvv 228,248,160
s_nop 0
Tv 220,8,49664
Tv 222,8,53760
Mvvv 232,248,164
LN 2,96,62,2048
Mvvv 236,248,168
EX 146
Mvvv 240,248,172
EX 147
Mvvv 244,248,176
WL 2
CV 144,144,145
CV 145,146,147
Maav 160,248,160
s_nop 0
Tv 224,252,49664
Tv 226,252,53760
Maav 164,248,164
LN 2,96,63,3072
Maav 168,248,168
EX 148
Maav 172,248,172
EX 149
Maav 176,248,176
EX 150
Maav 180,252,160
Ra 240,6,2048
v_mul_f32 v12,v10,v12
Maav 184,252,164
Ra 244,7,2048
v_mul_f32 v253,v10,v253
Maav 188,252,168
Ra 248,6,3072
CV 12,12,253
Maav 192,252,172
EX 151
Maav 196,252,176
WL 3
Ra 252,7,3072
CV 148,148,149
Mavv 200,220,160
CV 149,150,151
AW 93,12
Mavv 204,220,164
EX 152
Mavv 208,220,168
EX 153
Mavv 212,220,172
EX 154
Mavv 216,220,176
EX 155
Mavv 220,224,160
CV 152,152,153
CV 153,154,155
Mavv 224,224,164
EX 156
Mavv 228,224,168
EX 157
Mavv 232,224,172
EX 158
Mavv 236,224,176
WL 0
EX 159
Nvav 100,240,20,80
CV 156,156,157
CV 157,158,159
Mvav 100,244,40
AR 254,112
AR 253,113
Mvav 100,248,60
v_mul_f32 v254,v10,v254
v_mul_f32 v253,v10,v253
Mvav 100,252,200
CV 254,254,253
AW 94,254
Nvav 104,240,24,84
AR 12,114
AR 253,115
Mvav 104,244,44
s_nop 0
Tv 220,8,50176
Tv 222,8,54272
Mvav 104,248,64
v_mul_f32 v12,v10,v12
v_mul_f32 v253,v10,v253
Mvav 104,252,204
EX 100
Nvav 108,240,28,88
EX 101
Mvav 108,244,48
EX 102
Mvav 108,248,68
s_nop 0
Tv 224,252,50176
Tv 226,252,54272
Mvav 108,252,208
EX 103
Nvav 112,240,32,92
CV 142,100,101
CV 143,102,103
Mvav 112,244,52
EX 104
Mvav 112,248,72
EX 105
Mvav 112,252,212
EX 106
Nvav 116,240,36,96
s_nop 0
Ta 240,8,50688
Ta 242,8,54784
Mvav 116,244,56
EX 107
Mvav 116,248,76
CV 146,104,105
CV 147,106,107
Mvav 116,252,216
WL 2
Ta 244,252,50688
Ta 246,252,54784
Mavv 0,220,160
EX 108
Mavv 4,220,164
EX 109
Mavv 8,220,168
EX 110
Mavv 12,220,172
EX 111
Mavv 16,220,176
CV 150,108,109
CV 151,110,111
Mavv 20,224,160
EX 112
Mavv 24,224,164
EX 113
Mavv 28,224,168
EX 114
Mavv 32,224,172
EX 115
Mavv 36,224,176
s_waitcnt vmcnt(12) lgkmcnt(0)
s_barrier
Maav 40,240,160
s_add_u32 m0,s76,8192
Ra 248,6,16384
Ra 252,7,16384
Maav 44,240,164
s_cmp_eq_u32 s88,11
s_cselect_b32 s86,0x20000,0
Rv 220,6,17408
Maav 48,240,168
Rv 224,7,17408
v_subrev_u32 v1,s86,v1
Maav 52,240,172
v_add_u32 v1,8192,v1
CV 154,112,113
Maav 56,240,176
LN 1,36,58,0
Maav 60,244,160
CV 155,114,115
CV 12,12,253
Maav 64,244,164
EX 116
Maav 68,244,168
EX 117
Maav 72,244,172
LN 1,36,59,1024
Maav 76,244,176
WL 1
EX 118
Nvav 160,248,20,80
EX 119
Mvav 160,252,40
CV 158,116,117
CV 159,118,119
Mvvv 160,220,60
WL 0
LN 1,36,60,2048
Mvvv 160,224,200
AW 95,12
AR 254,96
Nvav 164,248,24,84
AR 253,97
v_mul_f32 v254,v11,v254
Mvav 164,252,44
v_mul_f32 v253,v11,v253
CV 254,254,253
Mvvv 164,220,64
LN 1,36,61,3072
Mvvv 164,224,204
s_add_u32 m0,s76,106496
s_cmp_eq_u32 s88,11
s_cselect_b32 s86,0x20000,0
Nvav 168,248,28,88
v_subrev_u32 v2,s86,v2
v_add_u32 v2,8192,v2
Mvav 168,252,48
EX 160
Mvvv 168,220,68
s_nop 0
Ta 240,8,32768
Ta 242,8,36864
Mvvv 168,224,208
LN 2,96,62,0
Nvav 172,248,32,92
EX 161
Mvav 172,252,52
EX 162
Mvvv 172,220,72
EX 163
Mvvv 172,224,212
s_nop 0
Ta 244,252,32768
Ta 246,252,36864
Nvav 176,248,36,96
LN 2,96,63,1024
Mvav 176,252,56
CV 160,160,161
CV 161,162,163
Mvvv 176,220,76
EX 164
Mvvv 176,224,216
EX 165
Mvvv 228,248,140
s_nop 0
Ta 248,8,33280
Ta 250,8,37376
Mvvv 232,248,144
LN 2,96,62,2048
Mvvv 236,248,148
EX 166
Mvvv 240,248,152
EX 167
Mvvv 244,248,156
WL 2
CV 164,164,165
CV 165,166,167
Maav 160,240,140
s_nop 0
Ta 252,252,33280
Ta 254,252,37376
Maav 164,240,144
LN 2,96,63,3072
Maav 168,240,148
EX 168
Maav 172,240,152
EX 169
Maav 176,240,156
EX 170
Maav 180,244,140
Rv 220,6,18432
AW 96,254
Maav 184,244,144
Rv 224,7,18432
AR 12,98
Maav 188,244,148
Ra 240,6,19456
AR 253,99
Maav 192,244,152
EX 171
Maav 196,244,156
WL 3
Ra 244,7,19456
CV 168,168,169
Maav 200,248,140
CV 169,170,171
v_mul_f32 v12,v11,v12
Maav 204,248,144
EX 172
Maav 208,248,148
EX 173
Maav 212,248,152
EX 174
Maav 216,248,156
EX 175
Maav 220,252,140
CV 172,172,173
CV 173,174,175
Maav 224,252,144
EX 176
Maav 228,252,148
EX 177
Maav 232,252,152
EX 178
Maav 236,252,156
WL 0
EX 179
Nvvv 100,220,20,80
CV 176,176,177
CV 177,178,179
Mvvv 100,224,40
v_mul_f32 v253,v11,v253
CV 12,12,253
Mvav 100,240,60
AW 97,12
AR 254,116
Mvav 100,244,200
AR 253,117
v_mul_f32 v254,v11,v254
Nvvv 104,220,24,84
v_mul_f32 v253,v11,v253
CV 254,254,253
Mvvv 104,224,44
s_nop 0
Ta 248,8,33792
Ta 250,8,37888
Mvav 104,240,64
AW 98,254
AR 12,118
Mvav 104,244,204
EX 100
Nvvv 108,220,28,88
EX 101
Mvvv 108,224,48
EX 102
Mvav 108,240,68
s_nop 0
Ta 252,252,33792
Ta 254,252,37888
Mvav 108,244,208
EX 103
Nvvv 112,220,32,92
CV 162,100,101
CV 163,102,103
Mvvv 112,224,52
EX 104
Mvav 112,240,72
EX 105
Mvav 112,244,212
EX 106
Nvvv 116,220,36,96
s_nop 0
Tv 220,8,34304
Tv 222,8,38400
Mvvv 116,224,56
EX 107
Mvav 116,240,76
CV 166,104,105
CV 167,106,107
Mvav 116,244,216
WL 2
Tv 224,252,34304
Tv 226,252,38400
Maav 0,248,140
EX 108
Maav 4,248,144
EX 109
Maav 8,248,148
EX 110
Maav 12,248,152
EX 111
Maav 16,248,156
CV 170,108,109
CV 171,110,111
Maav 20,252,140
EX 112
Maav 24,252,144
EX 113
Maav 28,252,148
EX 114
Maav 32,252,152
EX 115
Maav 36,252,156
s_waitcnt vmcnt(12) lgkmcnt(0)
s_barrier
Mavv 40,220,140
s_add_u32 m0,s76,24576
Ra 240,6,32768
Ra 244,7,32768
Mavv 44,220,144
s_cmp_eq_u32 s88,12
s_cselect_b32 s86,0x20000,0
Ra 248,6,33792
Mavv 48,220,148
Ra 252,7,33792
v_subrev_u32 v1,s86,v1
Mavv 52,220,152
v_add_u32 v1,8192,v1
CV 174,112,113
Mavv 56,220,156
LN 1,36,58,0
Mavv 60,224,140
CV 175,114,115
AR 253,119
Mavv 64,224,144
EX 116
Mavv 68,224,148
EX 117
Mavv 72,224,152
LN 1,36,59,1024
Mavv 76,224,156
WL 1
EX 118
Nvav 140,240,20,80
EX 119
Mvav 140,244,40
CV 178,116,117
CV 179,118,119
Mvav 140,248,60
WL 0
LN 1,36,60,2048
Mvav 140,252,200
v_mul_f32 v12,v11,v12
v_mul_f32 v253,v11,v253
Nvav 144,240,24,84
CV 12,12,253
AW 99,12
Mvav 144,244,44
AR 254,120
AR 253,121
Mvav 144,248,64
LN 1,36,61,3072
Mvav 144,252,204
s_add_u32 m0,s76,90112
s_cmp_eq_u32 s88,12
s_cselect_b32 s86,0x20000,0
Nvav 148,240,28,88
v_subrev_u32 v2,s86,v2
v_add_u32 v2,8192,v2
Mvav 148,244,48
EX 140
Mvav 148,248,68
s_nop 0
Tv 220,8,16384
Tv 222,8,20480
Mvav 148,252,208
LN 2,96,62,0
Nvav 152,240,32,92
EX 141
Mvav 152,244,52
EX 142
Mvav 152,248,72
EX 143
Mvav 152,252,212
s_nop 0
Tv 224,252,16384
Tv 226,252,20480
Nvav 156,240,36,96
LN 2,96,63,1024
Mvav 156,244,56
CV 140,140,141
CV 141,142,143
Mvav 156,248,76
EX 144
Mvav 156,252,216
EX 145
Mvvv 228,248,160
s_nop 0
Ta 240,8,16896
Ta 242,8,20992
Mvvv 232,248,164
LN 2,96,62,2048
Mvvv 236,248,168
EX 146
Mvvv 240,248,172
EX 147
Mvvv 244,248,176
WL 2
CV 144,144,145
CV 145,146,147
Mavv 160,220,160
s_nop 0
Ta 244,252,16896
Ta 246,252,20992
Mavv 164,220,164
LN 2,96,63,3072
Mavv 168,220,168
EX 148
Mavv 172,220,172
EX 149
Mavv 176,220,176
EX 150
Mavv 180,224,160
Ra 248,6,34816
v_mul_f32 v254,v0,v254
Mavv 184,224,164
Ra 252,7,34816
v_mul_f32 v253,v0,v253
Mavv 188,224,168
Rv 220,6,35840
CV 254,254,253
Mavv 192,224,172
EX 151
Mavv 196,224,176
WL 3
Rv 224,7,35840
CV 148,148,149
Maav 200,240,160
CV 149,150,151
AW 120,254
Maav 204,240,164
EX 152
Maav 208,240,168
EX 153
Maav 212,240,172
EX 154
Maav 216,240,176
EX 155
Maav 220,244,160
CV 152,152,153
CV 153,154,155
Maav 224,244,164
EX 156
Maav 228,244,168
EX 157
Maav 232,244,172
EX 158
Maav 236,244,176
WL 0
EX 159
Nvav 100,248,20,80
CV 156,156,157
CV 157,158,159
Mvav 100,252,40
AR 12,122
AR 253,123
Mvvv 100,220,60
v_mul_f32 v12,v0,v12
v_mul_f32 v253,v0,v253
Mvvv 100,224,200
CV 12,12,253
AW 121,12
Nvav 104,248,24,84
AR 254,140
AR 253,141
Mvav 104,252,44
s_nop 0
Ta 240,8,17408
Ta 242,8,21504
Mvvv 104,220,64
v_mul_f32 v254,v0,v254
v_mul_f32 v253,v0,v253
Mvvv 104,224,204
EX 100
Nvav 108,248,28,88
EX 101
Mvav 108,252,48
EX 102
Mvvv 108,220,68
s_nop 0
Ta 244,252,17408
Ta 246,252,21504
Mvvv 108,224,208
EX 103
Nvav 112,248,32,92
CV 142,100,101
CV 143,102,103
Mvav 112,252,52
EX 104
Mvvv 112,220,72
EX 105
Mvvv 112,224,212
EX 106
Nvav 116,248,36,96
s_nop 0
Ta 248,8,17920
Ta 250,8,22016
Mvav 116,252,56
EX 107
Mvvv 116,220,76
CV 146,104,105
CV 147,106,107
Mvvv 116,224,216
WL 2
Ta 252,252,17920
Ta 254,252,22016
Maav 0,240,160
EX 108
Maav 4,240,164
EX 109
Maav 8,240,168
EX 110
Maav 12,240,172
EX 111
Maav 16,240,176
CV 150,108,109
CV 151,110,111
Maav 20,244,160
EX 112
Maav 24,244,164
EX 113
Maav 28,244,168
EX 114
Maav 32,244,172
EX 115
Maav 36,244,176
s_waitcnt vmcnt(12) lgkmcnt(0)
s_barrier
Maav 40,248,160
s_add_u32 m0,s76,40960
Rv 220,6,0
Rv 224,7,0
Maav 44,248,164
s_cmp_eq_u32 s88,13
s_cselect_b32 s86,0x20000,0
Ra 240,6,1024
Maav 48,248,168
s_nop 0
Ra 244,7,1024
v_subrev_u32 v1,s86,v1
Maav 52,248,172
s_nop 0
v_add_u32 v1,8192,v1
CV 154,112,113
Maav 56,248,176
LN 1,36,58,0
Maav 60,252,160
s_nop 0
CV 155,114,115
CV 254,254,253
Maav 64,252,164
EX 116
Maav 68,252,168
EX 117
Maav 72,252,172
LN 1,36,59,1024
Maav 76,252,176
WL 1
EX 118
Nvvv 160,220,20,80
EX 119
Mvvv 160,224,40
CV 158,116,117
CV 159,118,119
Mvav 160,240,60
WL 0
LN 1,36,60,2048
Mvav 160,244,200
AW 122,254
AR 12,142
Nvvv 164,220,24,84
AR 253,143
v_mul_f32 v12,v0,v12
Mvvv 164,224,44
v_mul_f32 v253,v0,v253
CV 12,12,253
Mvav 164,240,64
LN 1,36,61,3072
Mvav 164,244,204
s_add_u32 m0,s76,73728
s_cmp_eq_u32 s88,13
s_cselect_b32 s86,0x20000,0
Nvvv 168,220,28,88
v_subrev_u32 v2,s86,v2
v_add_u32 v2,8192,v2
Mvvv 168,224,48
EX 160
Mvav 168,240,68
s_nop 0
Ta 248,8,0
Ta 250,8,4096
Mvav 168,244,208
LN 2,96,62,0
Nvvv 172,220,32,92
EX 161
Mvvv 172,224,52
EX 162
Mvav 172,240,72
EX 163
Mvav 172,244,212
s_nop 0
Ta 252,252,0
Ta 254,252,4096
Nvvv 176,220,36,96
LN 2,96,63,1024
Mvvv 176,224,56
CV 160,160,161
CV 161,162,163
Mvav 176,240,76
EX 164
Mvav 176,244,216
EX 165
Mvvv 228,248,140
s_nop 0
Tv 220,8,512
Tv 222,8,4608
Mvvv 232,248,144
LN 2,96,62,2048
Mvvv 236,248,148
EX 166
Mvvv 240,248,152
EX 167
Mvvv 244,248,156
WL 2
CV 164,164,165
CV 165,166,167
Maav 160,248,140
s_nop 0
Tv 224,252,512
Tv 226,252,4608
Maav 164,248,144
LN 2,96,63,3072
Maav 168,248,148
EX 168
Maav 172,248,152
EX 169
Maav 176,248,156
EX 170
Maav 180,252,140
Ra 240,6,2048
AW 123,12
Maav 184,252,144
Ra 244,7,2048
AR 254,124
Maav 188,252,148
Ra 248,6,3072
AR 253,125
Maav 192,252,152
EX 171
Maav 196,252,156
WL 3
Ra 252,7,3072
CV 168,168,169
Mavv 200,220,140
CV 169,170,171
v_mul_f32 v254,v3,v254
Mavv 204,220,144
EX 172
Mavv 208,220,148
EX 173
Mavv 212,220,152
EX 174
Mavv 216,220,156
EX 175
Mavv 220,224,140
CV 172,172,173
CV 173,174,175
Mavv 224,224,144
EX 176
Mavv 228,224,148
EX 177
Mavv 232,224,152
EX 178
Mavv 236,224,156
WL 0
EX 179
Nvav 100,240,20,80
CV 176,176,177
CV 177,178,179
Mvav 100,244,40
v_mul_f32 v253,v3,v253
CV 254,254,253
Mvav 100,248,60
AW 124,254
AR 12,126
Mvav 100,252,200
AR 253,127
v_mul_f32 v12,v3,v12
Nvav 104,240,24,84
v_mul_f32 v253,v3,v253
CV 12,12,253
Mvav 104,244,44
s_nop 0
Tv 220,8,1024
Tv 222,8,5120
Mvav 104,248,64
AW 125,12
AR 254,144
Mvav 104,252,204
EX 100
Nvav 108,240,28,88
EX 101
Mvav 108,244,48
EX 102
Mvav 108,248,68
s_nop 0
Tv 224,252,1024
Tv 226,252,5120
Mvav 108,252,208
EX 103
Nvav 112,240,32,92
CV 162,100,101
CV 163,102,103
Mvav 112,244,52
EX 104
Mvav 112,248,72
EX 105
Mvav 112,252,212
EX 106
Nvav 116,240,36,96
s_nop 0
Ta 240,8,1536
Ta 242,8,5632
Mvav 116,244,56
EX 107
Mvav 116,248,76
CV 166,104,105
CV 167,106,107
Mvav 116,252,216
WL 2
Ta 244,252,1536
Ta 246,252,5632
Mavv 0,220,140
EX 108
Mavv 4,220,144
EX 109
Mavv 8,220,148
EX 110
Mavv 12,220,152
EX 111
Mavv 16,220,156
CV 170,108,109
CV 171,110,111
Mavv 20,224,140
EX 112
Mavv 24,224,144
EX 113
Mavv 28,224,148
EX 114
Mavv 32,224,152
EX 115
Mavv 36,224,156
s_waitcnt vmcnt(12) lgkmcnt(0)
s_barrier
Maav 40,240,140
s_add_u32 m0,s76,8192
Ra 248,6,16384
Ra 252,7,16384
Maav 44,240,144
s_cmp_eq_u32 s88,14
s_cselect_b32 s86,0x20000,0
Rv 220,6,17408
Maav 48,240,148
s_nop 0
Rv 224,7,17408
v_subrev_u32 v1,s86,v1
Maav 52,240,152
s_nop 0
v_add_u32 v1,8192,v1
CV 174,112,113
Maav 56,240,156
LN 1,36,58,0
Maav 60,244,140
s_nop 0
CV 175,114,115
AR 253,145
Maav 64,244,144
s_nop 0
EX 116
Maav 68,244,148
EX 117
Maav 72,244,152
LN 1,36,59,1024
Maav 76,244,156
WL 1
EX 118
Nvav 140,248,20,80
s_nop 0
EX 119
Mvav 140,252,40
CV 178,116,117
CV 179,118,119
Mvvv 140,220,60
WL 0
LN 1,36,60,2048
Mvvv 140,224,200
s_nop 0
v_mul_f32 v254,v3,v254
v_mul_f32 v253,v3,v253
Nvav 144,248,24,84
s_nop 0
CV 254,254,253
AW 126,254
Mvav 144,252,44
s_nop 0
AR 12,146
AR 253,147
Mvvv 144,220,64
LN 1,36,61,3072
Mvvv 144,224,204
s_nop 0
v_mul_f32 v12,v3,v12
v_mul_f32 v253,v3,v253
Nvav 148,248,28,88
s_nop 0
CV 12,12,253
AW 127,12
Mvav 148,252,48
s_nop 0
AR 254,128
AR 253,129
Mvvv 148,220,68
s_nop 0
Ta 240,8,49152
Ta 242,8,53248
Mvvv 148,224,208
s_nop 0
v_mul_f32 v254,v9,v254
v_mul_f32 v253,v9,v253
Nvav 152,248,32,92
s_nop 0
CV 254,254,253
AW 128,254
Mvav 152,252,52
s_nop 0
AR 12,130
AR 253,131
Mvvv 152,220,72
s_nop 0
v_mul_f32 v12,v9,v12
v_mul_f32 v253,v9,v253
Mvvv 152,224,212
s_nop 0
Ta 244,252,49152
Ta 246,252,53248
Nvav 156,248,36,96
CV 12,12,253
AW 129,12
Mvav 156,252,56
AR 254,148
AR 253,149
Mvvv 156,220,76
v_mul_f32 v254,v9,v254
v_mul_f32 v253,v9,v253
Mvvv 156,224,216
CV 254,254,253
AW 130,254
Mvvv 228,248,160
s_nop 0
Ta 248,8,49664
Ta 250,8,53760
Mvvv 232,248,164
AR 12,150
AR 253,151
Mvvv 236,248,168
v_mul_f32 v12,v9,v12
v_mul_f32 v253,v9,v253
Mvvv 240,248,172
CV 12,12,253
AW 131,12
Mvvv 244,248,176
WL 2
AR 254,132
AR 253,133
Maav 160,240,160
s_nop 0
Ta 252,252,49664
Ta 254,252,53760
Maav 164,240,164
v_mul_f32 v254,v10,v254
v_mul_f32 v253,v10,v253
Maav 168,240,168
CV 254,254,253
AW 132,254
Maav 172,240,172
AR 12,134
AR 253,135
Maav 176,240,176
v_mul_f32 v12,v10,v12
v_mul_f32 v253,v10,v253
Maav 180,244,160
Rv 220,6,18432
CV 12,12,253
Maav 184,244,164
Rv 224,7,18432
AW 133,12
Maav 188,244,168
Ra 240,6,19456
AR 254,152
Maav 192,244,172
AR 253,153
v_mul_f32 v254,v10,v254
Maav 196,244,176
WL 3
Ra 244,7,19456
v_mul_f32 v253,v10,v253
Maav 200,248,160
CV 254,254,253
AW 134,254
Maav 204,248,164
AR 12,154
AR 253,155
Maav 208,248,168
v_mul_f32 v12,v10,v12
v_mul_f32 v253,v10,v253
Maav 212,248,172
CV 12,12,253
AW 135,12
Maav 216,248,176
AR 254,136
AR 253,137
Maav 220,252,160
v_mul_f32 v254,v11,v254
v_mul_f32 v253,v11,v253
Maav 224,252,164
CV 254,254,253
AW 136,254
Maav 228,252,168
AR 12,138
AR 253,139
Maav 232,252,172
v_mul_f32 v12,v11,v12
v_mul_f32 v253,v11,v253
Maav 236,252,176
WL 0
CV 12,12,253
AW 137,12
Nvvv 100,220,20,80
AR 254,156
AR 253,157
Mvvv 100,224,40
v_mul_f32 v254,v11,v254
v_mul_f32 v253,v11,v253
Mvav 100,240,60
CV 254,254,253
AW 138,254
Mvav 100,244,200
AR 12,158
AR 253,159
Nvvv 104,220,24,84
v_mul_f32 v12,v11,v12
v_mul_f32 v253,v11,v253
Mvvv 104,224,44
s_nop 0
Ta 248,8,50176
Ta 250,8,54272
Mvav 104,240,64
CV 12,12,253
AW 139,12
Mvav 104,244,204
v_add_u32 v253,0x10000,v13
v_add_u32 v254,0x10000,v14
Nvvv 108,220,28,88
EX 140
Mvvv 108,224,48
EX 141
Mvav 108,240,68
s_nop 0
Ta 252,252,50176
Ta 254,252,54272
Mvav 108,244,208
EX 142
Nvvv 112,220,32,92
EX 143
Mvvv 112,224,52
CV 140,140,141
CV 141,142,143
Mvav 112,240,72
EX 144
Mvav 112,244,212
EX 145
Nvvv 116,220,36,96
s_nop 0
Tv 220,8,50688
Tv 222,8,54784
Mvvv 116,224,56
EX 146
Mvav 116,240,76
EX 147
Mvav 116,244,216
WL 2
Tv 224,252,50688
Tv 226,252,54784
Maav 0,248,160
CV 144,144,145
CV 145,146,147
Maav 4,248,164
EX 148
Maav 8,248,168
EX 149
Maav 12,248,172
EX 150
Maav 16,248,176
EX 151
Maav 20,252,160
CV 148,148,149
CV 149,150,151
Maav 24,252,164
EX 152
Maav 28,252,168
EX 153
Maav 32,252,172
EX 154
Maav 36,252,176
s_waitcnt vmcnt(8) lgkmcnt(0)
s_barrier
Mavv 40,220,160
s_add_u32 m0,s76,24576
Ra 240,6,32768
Ra 244,7,32768
Mavv 44,220,164
s_cmp_eq_u32 s88,15
s_cselect_b32 s86,0x20000,0
Ra 248,6,33792
Mavv 48,220,168
s_nop 0
Ra 252,7,33792
v_subrev_u32 v1,s86,v1
Mavv 52,220,172
s_nop 0
Ra 100,16,53248
v_add_u32 v1,8192,v1
Mavv 56,220,176
LN 1,36,58,0
Mavv 60,224,160
s_nop 0
EX 155
Mavv 64,224,164
CV 152,152,153
CV 153,154,155
Mavv 68,224,168
s_nop 0
EX 156
Mavv 72,224,172
LN 1,36,59,1024
Mavv 76,224,176
WL 2
EX 157
Nvav 160,240,20,80
s_nop 0
EX 158
Mvav 160,244,40
EX 159
Mvav 160,248,60
WL 0
LN 1,36,60,2048
Mvav 160,252,200
s_nop 0
CV 156,156,157
CV 157,158,159
Nvav 164,240,24,84
s_nop 0
EX 100
Mvav 164,244,44
EX 101
Mvav 164,248,64
LN 1,36,61,3072
Mvav 164,252,204
s_add_u32 m0,s76,106496
s_cmp_eq_u32 s88,14
s_cselect_b32 s86,0x20000,0
Nvav 168,240,28,88
s_nop 0
v_subrev_u32 v2,s86,v2
v_add_u32 v2,8192,v2
Mvav 168,244,48
s_nop 0
EX 102
Mvav 168,248,68
s_nop 0
Tv 220,8,32768
Tv 222,8,36864
Mvav 168,252,208
LN 2,96,62,0
Nvav 172,240,32,92
s_nop 0
EX 103
Mvav 172,244,52
CV 142,100,101
CV 143,102,103
Mvav 172,248,72
s_nop 0
EX 104
Mvav 172,252,212
s_nop 0
Tv 224,252,32768
Tv 226,252,36864
Nvav 176,240,36,96
LN 2,96,63,1024
Mvav 176,244,56
EX 105
Mvav 176,248,76
EX 106
EX 107
CV 146,104,105
CV 147,106,107
Mvav 176,252,216
EX 108
EX 109
EX 110
EX 111
CV 150,108,109
CV 151,110,111
Mvvv 228,248,140
EX 112
EX 113
EX 114
EX 115
CV 154,112,113
CV 155,114,115
Mvvv 232,248,144
EX 116
EX 117
EX 118
EX 119
CV 158,116,117
CV 159,118,119
Mvvv 236,248,148
s_nop 0
Ta 240,8,33280
Ta 242,8,37376
Mvvv 240,248,152
LN 2,96,62,2048
Mvvv 244,248,156
WL 2
EX 160
Mavv 160,220,140
s_nop 0
Ta 244,252,33280
Ta 246,252,37376
Mavv 164,220,144
EX 161
Mavv 168,220,148
LN 2,96,63,3072
Mavv 172,220,152
EX 162
Mavv 176,220,156
EX 163
Mavv 180,224,140
Ra 248,6,34816
CV 160,160,161
Mavv 184,224,144
Ra 252,7,34816
CV 161,162,163
Mavv 188,224,148
s_nop 0
Rv 220,6,35840
Ra 104,16,54272
Mavv 192,224,152
EX 164
Mavv 196,224,156
WL 4
Rv 224,7,35840
Ra 108,16,55296
Maav 200,240,140
EX 165
Maav 204,240,144
EX 166
Maav 208,240,148
EX 167
Maav 212,240,152
CV 164,164,165
CV 165,166,167
Maav 216,240,156
EX 168
Maav 220,244,140
EX 169
Maav 224,244,144
EX 170
Maav 228,244,148
EX 171
Maav 232,244,152
CV 168,168,169
CV 169,170,171
Maav 236,244,156
WL 0
EX 172
Nvav 100,248,20,80
EX 173
Mvav 100,252,40
EX 174
Mvvv 100,220,60
EX 175
Mvvv 100,224,200
CV 172,172,173
CV 173,174,175
Nvav 104,248,24,84
EX 176
Mvav 104,252,44
s_nop 0
Ta 240,8,33792
Ta 242,8,37888
Mvvv 104,220,64
EX 177
Mvvv 104,224,204
EX 178
Nvav 108,248,28,88
EX 179
Mvav 108,252,48
CV 176,176,177
CV 177,178,179
Mvvv 108,220,68
s_nop 0
Ta 244,252,33792
Ta 246,252,37888
Mvvv 108,224,208
EX 100
Nvav 112,248,32,92
EX 101
Mvav 112,252,52
EX 102
Mvvv 112,220,72
EX 103
Mvvv 112,224,212
CV 162,100,101
CV 163,102,103
Nvav 116,248,36,96
s_nop 0
Ta 248,8,34304
Ta 250,8,38400
Mvav 116,252,56
EX 104
Mvvv 116,220,76
EX 105
Mvvv 116,224,216
WL 2
Ta 252,252,34304
Ta 254,252,38400
Maav 0,240,140
EX 106
Maav 4,240,144
EX 107
Maav 8,240,148
CV 166,104,105
CV 167,106,107
Maav 12,240,152
EX 108
Maav 16,240,156
EX 109
Maav 20,244,140
EX 110
Maav 24,244,144
EX 111
Maav 28,244,148
CV 170,108,109
CV 171,110,111
Maav 32,244,152
EX 112
Maav 36,244,156
s_waitcnt vmcnt(8) lgkmcnt(0)
s_barrier
Maav 40,248,140
s_add_u32 m0,s76,90112
Rv 220,6,0
Rv 224,7,0
Maav 44,248,144
s_cmp_eq_u32 s88,15
s_cselect_b32 s86,0x20000,0
Ra 240,6,1024
Maav 48,248,148
s_nop 0
Ra 244,7,1024
v_subrev_u32 v2,s86,v2
Maav 52,248,152
s_nop 0
Ra 112,16,56320
v_add_u32 v2,8192,v2
Maav 56,248,156
LN 2,96,62,0
Maav 60,252,140
s_nop 0
EX 113
Maav 64,252,144
EX 114
Maav 68,252,148
s_nop 0
EX 115
Maav 72,252,152
LN 2,96,63,1024
Maav 76,252,156
WL 2
CV 174,112,113
CV 175,114,115
Nvvv 140,220,20,80
s_nop 0
EX 116
Mvvv 140,224,40
EX 117
Mvav 140,240,60
WL 0
LN 2,96,62,2048
Mvav 140,244,200
s_nop 0
EX 118
Nvvv 144,220,24,84
EX 119
Mvvv 144,224,44
s_nop 0
CV 178,116,117
CV 179,118,119
Mvav 144,240,64
LN 2,96,63,3072
Mvav 144,244,204
s_add_u32 m0,s78,32768
Ra 116,16,57344
Ra 140,16,58368
Nvvv 148,220,28,88
s_nop 0
Ra 144,16,59392
Mvvv 148,224,48
Ra 148,16,60416
Mvav 148,240,68
Ta 248,8,16384
Mvav 148,244,208
s_nop 0
Ta 250,8,20480
Ra 152,16,61440
Nvvv 152,220,32,92
LD 4,40,68,0
Mvvv 152,224,52
Ra 156,16,62464
Mvav 152,240,72
EX 140
Mvav 152,244,212
s_nop 0
Ta 252,252,16384
Ta 254,252,20480
Nvvv 156,220,36,96
LD 4,40,69,1024
Mvvv 156,224,56
EX 141
Mvav 156,240,76
EX 142
Mvav 156,244,216
EX 143
Mvvv 228,248,160
s_nop 0
Tv 220,8,16896
Tv 222,8,20992
Mvvv 232,248,164
LD 4,40,70,2048
Mvvv 236,248,168
CV 140,140,141
CV 141,142,143
Mvvv 240,248,172
EX 144
Mvvv 244,248,176
WL 2
EX 145
Maav 160,248,160
s_nop 0
Tv 224,252,16896
Tv 226,252,20992
Maav 164,248,164
LD 4,40,71,3072
Maav 168,248,168
s_add_u32 m0,s78,36864
EX 146
Maav 172,248,172
EX 147
Maav 176,248,176
CV 144,144,145
CV 145,146,147
Maav 180,252,160
Ra 240,6,2048
Maav 184,252,164
Ra 244,7,2048
Maav 188,252,168
)ASM"
R"ASM(Ra 248,6,3072
Maav 192,252,172
LD 4,40,72,0
Maav 196,252,176
WL 3
Ra 252,7,3072
Mavv 200,220,160
EX 148
Mavv 204,220,164
EX 149
Mavv 208,220,168
LD 4,40,73,1024
Mavv 212,220,172
EX 150
Mavv 216,220,176
EX 151
Mavv 220,224,160
CV 148,148,149
CV 149,150,151
Mavv 224,224,164
LD 4,40,74,2048
Mavv 228,224,168
EX 152
Mavv 232,224,172
EX 153
Mavv 236,224,176
WL 0
EX 154
Nvav 100,240,20,80
LD 4,40,75,3072
Mvav 100,244,40
EX 155
Mvav 100,248,60
CV 152,152,153
CV 153,154,155
Mvav 100,252,200
EX 156
Nvav 104,240,24,84
EX 157
Mvav 104,244,44
s_nop 0
Tv 220,8,17408
Tv 222,8,21504
Mvav 104,248,64
EX 158
Mvav 104,252,204
EX 159
Nvav 108,240,28,88
CV 156,156,157
CV 157,158,159
Mvav 108,244,48
EX 100
Mvav 108,248,68
s_nop 0
Tv 224,252,17408
Tv 226,252,21504
Mvav 108,252,208
EX 101
Nvav 112,240,32,92
EX 102
Mvav 112,244,52
EX 103
Mvav 112,248,72
CV 142,100,101
CV 143,102,103
Mvav 112,252,212
EX 104
Nvav 116,240,36,96
s_nop 0
Ta 240,8,17920
Ta 242,8,22016
Mvav 116,244,56
EX 105
Mvav 116,248,76
EX 106
Mvav 116,252,216
WL 2
Ta 244,252,17920
Ta 246,252,22016
Mavv 0,220,160
EX 107
Mavv 4,220,164
CV 146,104,105
CV 147,106,107
Mavv 8,220,168
EX 108
Mavv 12,220,172
EX 109
Mavv 16,220,176
EX 110
Mavv 20,224,160
EX 111
Mavv 24,224,164
CV 150,108,109
CV 151,110,111
Mavv 28,224,168
EX 112
Mavv 32,224,172
EX 113
Mavv 36,224,176
s_waitcnt vmcnt(12) lgkmcnt(0)
s_barrier
Maav 40,240,160
s_nop 0
Ra 248,6,16384
Ra 252,7,16384
Maav 44,240,164
s_nop 0
Rv 220,6,17408
Maav 48,240,168
Rv 224,7,17408
Maav 52,240,172
s_nop 0
EX 114
Maav 56,240,176
EX 115
Maav 60,244,160
s_nop 0
CV 154,112,113
CV 155,114,115
Maav 64,244,164
s_nop 0
EX 116
Maav 68,244,168
EX 117
Maav 72,244,172
EX 118
Maav 76,244,176
WL 1
EX 119
Nvav 160,248,20,80
CV 158,116,117
CV 159,118,119
Mvav 160,252,40
ds_write_b128 v15,a[80:83] offset:53248
Mvvv 160,220,60
WL 1
Mvvv 160,224,200
Nvav 164,248,24,84
Mvav 164,252,44
Mvvv 164,220,64
ds_write_b128 v15,a[84:87] offset:54272
Mvvv 164,224,204
s_nop 2
EX 160
Nvav 168,248,28,88
EX 161
Mvav 168,252,48
EX 162
Mvvv 168,220,68
EX 163
Mvvv 168,224,208
CV 160,160,161
CV 161,162,163
Nvav 172,248,32,92
Ta 240,8,49152
Mvav 172,252,52
Ta 242,8,53248
Mvvv 172,220,72
EX 164
Mvvv 172,224,212
s_nop 0
Ta 244,252,49152
Ta 246,252,53248
Nvav 176,248,36,96
EX 165
Mvav 176,252,56
EX 166
Mvvv 176,220,76
EX 167
Mvvv 176,224,216
CV 164,164,165
CV 165,166,167
Mvvv 228,248,140
s_nop 0
Ta 248,8,49664
Ta 250,8,53760
Mvvv 232,248,144
EX 168
Mvvv 236,248,148
EX 169
Mvvv 240,248,152
EX 170
Mvvv 244,248,156
WL 2
EX 171
Maav 160,240,140
s_nop 0
Ta 252,252,49664
Ta 254,252,53760
Maav 164,240,144
CV 168,168,169
CV 169,170,171
Maav 168,240,148
EX 172
Maav 172,240,152
EX 173
Maav 176,240,156
EX 174
Maav 180,244,140
Rv 220,6,18432
Maav 184,244,144
Rv 224,7,18432
Maav 188,244,148
Ra 240,6,19456
Maav 192,244,152
EX 175
Maav 196,244,156
WL 3
Ra 244,7,19456
CV 172,172,173
Maav 200,248,140
CV 173,174,175
Maav 204,248,144
EX 176
Maav 208,248,148
EX 177
Maav 212,248,152
EX 178
Maav 216,248,156
EX 179
Maav 220,252,140
CV 176,176,177
CV 177,178,179
Maav 224,252,144
ds_write_b128 v15,a[88:91] offset:55296
Maav 228,252,148
Maav 232,252,152
Maav 236,252,156
WL 1
Nvvv 100,220,20,80
Mvvv 100,224,40
ds_write_b128 v15,a[92:95] offset:56320
Mvav 100,240,60
Mvav 100,244,200
Nvvv 104,220,24,84
Mvvv 104,224,44
Mvav 104,240,64
Mvav 104,244,204
Ta 248,8,50176
Nvvv 108,220,28,88
Ta 250,8,54272
Mvvv 108,224,48
EX 100
Mvav 108,240,68
s_nop 0
Ta 252,252,50176
Ta 254,252,54272
Mvav 108,244,208
EX 101
Nvvv 112,220,32,92
EX 102
Mvvv 112,224,52
EX 103
Mvav 112,240,72
CV 162,100,101
CV 163,102,103
Mvav 112,244,212
EX 104
Nvvv 116,220,36,96
s_nop 0
Tv 220,8,50688
Tv 222,8,54784
Mvvv 116,224,56
EX 105
Mvav 116,240,76
EX 106
Mvav 116,244,216
WL 2
Tv 224,252,50688
Tv 226,252,54784
Maav 0,248,140
EX 107
Maav 4,248,144
CV 166,104,105
CV 167,106,107
Maav 8,248,148
EX 108
Maav 12,248,152
EX 109
Maav 16,248,156
EX 110
Maav 20,252,140
EX 111
Maav 24,252,144
CV 170,108,109
CV 171,110,111
Maav 28,252,148
EX 112
Maav 32,252,152
EX 113
Maav 36,252,156
s_waitcnt vmcnt(8) lgkmcnt(0)
s_barrier
Mavv 40,220,140
s_add_u32 m0,s78,0
Ta 240,8,32768
Ta 242,8,36864
Mavv 44,220,144
s_cmp_le_u32 s90,1
s_cselect_b64 s[40:41],s[28:29],%6
v_add_u32 v4,128,v4
Mavv 48,220,148
LD 4,40,68,0
Mavv 52,220,152
EX 114
Mavv 56,220,156
EX 115
Mavv 60,224,140
s_nop 0
Ta 244,252,32768
Ta 246,252,36864
Mavv 64,224,144
LD 4,40,69,1024
Mavv 68,224,148
CV 174,112,113
CV 175,114,115
Mavv 72,224,152
EX 116
Mavv 76,224,156
EX 117
Mvvv 228,248,160
s_nop 0
Ta 248,8,33280
Ta 250,8,37376
Mvvv 232,248,164
EX 118
EX 119
CV 178,116,117
CV 179,118,119
Mvvv 236,248,168
LD 4,40,70,2048
Mvvv 240,248,172
ds_write_b128 v15,a[96:99] offset:57344
Mvvv 244,248,176
WL 3
Maav 160,240,160
Maav 164,240,164
LD 4,40,71,3072
Maav 168,240,168
s_add_u32 m0,s78,4096
Maav 172,240,172
Ta 252,252,33280
v_mul_f32 v229,0x4d800000,v229
Maav 176,240,176
Ta 254,252,37376
Maav 180,244,160
s_nop 0
Tv 220,8,33792
Tv 222,8,37888
Maav 184,244,164
LD 4,40,72,0
Maav 188,244,168
ds_write_b128 v15,a[120:123] offset:58368
Maav 192,244,172
v_rcp_f32 v228,v228
Maav 196,244,176
WL 5
v_mul_f32 v233,0x4d800000,v233
Maav 200,248,160
LD 4,40,73,1024
Maav 204,248,164
v_rcp_f32 v232,v232
Maav 208,248,168
Tv 224,252,33792
v_mul_f32 v237,0x4d800000,v237
Maav 212,248,172
Tv 226,252,37888
Maav 216,248,176
WL 3
LD 4,40,74,2048
Maav 220,252,160
s_nop 0
Ta 240,8,34304
Ta 242,8,38400
Maav 224,252,164
ds_write_b128 v15,a[124:127] offset:59392
Maav 228,252,168
v_rcp_f32 v236,v236
Maav 232,252,172
LD 4,40,75,3072
Maav 236,252,176
v_mul_f32 v241,0x4d800000,v241
Mavv 0,220,160
v_rcp_f32 v240,v240
Mavv 4,220,164
Ta 244,252,34304
v_mul_f32 v245,0x4d800000,v245
Mavv 8,220,168
Ta 246,252,38400
Mavv 12,220,172
ds_write_b128 v15,a[128:131] offset:60416
Mavv 16,220,176
WL 3
v_rcp_f32 v244,v244
Mavv 20,224,160
ds_write_b128 v15,a[132:135] offset:61440
ds_write_b128 v15,a[136:139] offset:62464
Mavv 24,224,164
v_sub_f32 v229,v229,v229
v_sub_f32 v233,v233,v233
Mavv 28,224,168
v_sub_f32 v237,v237,v237
v_sub_f32 v241,v241,v241
Mavv 32,224,172
v_sub_f32 v245,v245,v245
v_add_f32 v228,v228,v229
Mavv 36,224,176
s_waitcnt vmcnt(8) lgkmcnt(0)
s_barrier
Maav 40,240,160
s_nop 0
v_add_f32 v232,v232,v233
v_add_f32 v236,v236,v237
Maav 44,240,164
s_nop 0
v_add_f32 v240,v240,v241
v_add_f32 v244,v244,v245
Maav 48,240,168
s_nop 0
AR 20,160
AR 21,161
Maav 52,240,172
s_nop 0
Ta 248,13,49152
Ta 250,13,51200
Maav 56,240,176
s_nop 0
Ta 252,14,49152
AR 22,162
Maav 60,244,160
s_nop 0
Ta 254,14,51200
Tv 220,13,53248
Maav 64,244,164
s_nop 0
Tv 222,13,55296
AR 23,163
Maav 68,244,168
AR 248,180
AR 249,181
Maav 72,244,172
AR 250,182
AR 251,183
Maav 76,244,176
WL 4
v_mul_f32 v20,v228,v20
v_mul_f32 v21,v228,v21
Zvav 100,248,120
s_nop 0
Tv 224,14,53248
Tv 226,14,55296
Zvav 104,248,124
v_mul_f32 v22,v228,v22
v_mul_f32 v23,v228,v23
Zvav 108,248,128
v_mul_f32 v248,v228,v248
v_mul_f32 v249,v228,v249
Zvav 112,248,132
v_mul_f32 v250,v228,v250
v_mul_f32 v251,v228,v251
Zvav 116,248,136
WL 2
CV 20,20,21
CV 21,22,23
Zvav 80,252,120
s_nop 0
Ta 240,13,57344
Ta 242,13,59392
Zvav 84,252,124
CV 22,248,249
CV 23,250,251
Zvav 88,252,128
ds_write_b128 v15,v[20:23] offset:0
Zvav 92,252,132
AR 24,164
AR 25,165
Zvav 96,252,136
AR 26,166
AR 27,167
Mvvv 100,220,180
AR 229,184
AR 230,185
Mvvv 104,220,184
AR 231,186
AR 233,187
Mvvv 108,220,188
Ta 244,14,57344
v_mul_f32 v24,v232,v24
Mvvv 112,220,192
Ta 246,14,59392
v_mul_f32 v25,v232,v25
Mvvv 116,220,196
WL 3
v_mul_f32 v26,v232,v26
v_mul_f32 v27,v232,v27
Mvvv 80,224,180
s_nop 0
Ta 248,13,61440
Ta 250,13,63488
Mvvv 84,224,184
v_mul_f32 v229,v232,v229
v_mul_f32 v230,v232,v230
Mvvv 88,224,188
v_mul_f32 v231,v232,v231
v_mul_f32 v233,v232,v233
Mvvv 92,224,192
CV 24,24,25
CV 25,26,27
Mvvv 96,224,196
CV 26,229,230
CV 27,231,233
Mvaa 100,240,80
s_nop 0
Ta 252,14,61440
Ta 254,14,63488
Mvaa 104,240,84
ds_write_b128 v15,v[24:27] offset:1024
Mvaa 108,240,88
AR 28,168
AR 29,169
Mvaa 112,240,92
AR 30,170
AR 31,171
Mvaa 116,240,96
WL 3
AR 248,188
AR 249,189
Mvaa 80,244,80
AR 250,190
AR 251,191
Mvaa 84,244,84
Tv 220,13,32768
v_mul_f32 v28,v236,v28
Mvaa 88,244,88
Tv 222,13,34816
v_mul_f32 v29,v236,v29
Mvaa 92,244,92
v_mul_f32 v30,v236,v30
v_mul_f32 v31,v236,v31
Mvaa 96,244,96
s_barrier
Mvaa 100,248,120
s_nop 0
Tv 224,14,32768
Tv 226,14,34816
Mvaa 104,248,124
s_nop 0
v_mul_f32 v248,v236,v248
v_mul_f32 v249,v236,v249
Mvaa 108,248,128
s_nop 0
v_mul_f32 v250,v236,v250
v_mul_f32 v251,v236,v251
Mvaa 112,248,132
s_nop 0
CV 28,28,29
CV 29,30,31
Mvaa 116,248,136
WL 3
CV 30,248,249
CV 31,250,251
Mvaa 80,252,120
s_nop 0
Ta 240,13,36864
Ta 242,13,38912
Mvaa 84,252,124
s_nop 0
ds_write_b128 v15,v[28:31] offset:2048
Mvaa 88,252,128
AR 32,172
AR 33,173
Mvaa 92,252,132
s_nop 0
AR 34,174
AR 35,175
Mvaa 96,252,136
WL 3
AR 229,192
AR 230,193
Mvva 100,220,100
s_nop 0
AR 231,194
AR 233,195
Mvva 104,220,104
s_nop 0
Ta 244,14,36864
v_mul_f32 v32,v240,v32
Mvva 108,220,108
s_nop 0
Ta 246,14,38912
v_mul_f32 v33,v240,v33
Mvva 112,220,112
s_nop 0
v_mul_f32 v34,v240,v34
v_mul_f32 v35,v240,v35
Mvva 116,220,116
s_nop 0
v_mul_f32 v229,v240,v229
v_mul_f32 v230,v240,v230
Mvva 80,224,100
s_nop 0
Ta 248,13,40960
Ta 250,13,43008
Mvva 84,224,104
s_nop 0
v_mul_f32 v231,v240,v231
v_mul_f32 v233,v240,v233
Mvva 88,224,108
CV 32,32,33
CV 33,34,35
Mvva 92,224,112
CV 34,229,230
CV 35,231,233
Mvva 96,224,116
WL 3
ds_write_b128 v15,v[32:35] offset:3072
Mvaa 100,240,140
AR 36,176
AR 37,177
Mvaa 104,240,144
AR 38,178
AR 39,179
Mvaa 108,240,148
AR 248,196
AR 249,197
Mvaa 112,240,152
AR 250,198
AR 251,199
Ra 160,16,53248
Mvaa 116,240,156
WL 2
Ra 164,16,54272
v_mul_f32 v36,v244,v36
Mvaa 80,244,140
Ra 168,16,55296
v_mul_f32 v37,v244,v37
Mvaa 84,244,144
Ra 172,16,56320
v_mul_f32 v38,v244,v38
Mvaa 88,244,148
Ra 176,16,57344
v_mul_f32 v39,v244,v39
Mvaa 92,244,152
s_nop 0
Ta 252,14,40960
Ta 254,14,43008
Mvaa 96,244,156
WL 6
Tv 220,13,45056
v_mul_f32 v248,v244,v248
Mvaa 100,248,160
WL 6
Tv 222,13,47104
Tv 224,14,45056
Mvaa 104,248,164
WL 7
Tv 226,14,47104
Ra 180,16,58368
Mvaa 108,248,168
WL 8
Ra 184,16,59392
v_mul_f32 v249,v244,v249
Mvaa 112,248,172
WL 8
Ra 188,16,60416
Ra 192,16,61440
Ra 196,16,62464
Mvaa 116,248,176
WL 9
v_mul_f32 v250,v244,v250
v_mul_f32 v251,v244,v251
Mvaa 80,252,160
CV 36,36,37
CV 37,38,39
Mvaa 84,252,164
CV 38,248,249
CV 39,250,251
Mvaa 88,252,168
ds_write_b128 v15,v[36:39] offset:4096
Mvaa 92,252,172
AR 40,200
AR 41,201
Mvaa 96,252,176
s_waitcnt vmcnt(0) lgkmcnt(1)
s_barrier
Mvva 100,220,180
s_add_u32 m0,s78,32768
s_cmp_le_u32 s90,0
s_cselect_b64 s[40:41],s[28:29],%6
Mvva 104,220,184
v_add_u32 v4,294784,v4
AR 42,202
Mvva 108,220,188
Ta 240,13,16384
AR 43,203
Mvva 112,220,192
s_nop 0
Ta 242,13,18432
Ta 244,14,16384
Mvva 116,220,196
Ta 246,14,18432
AR 229,220
Mvva 80,224,180
s_nop 0
Ta 248,13,20480
Ta 250,13,22528
Mvva 84,224,184
LD 4,40,68,0
Mvva 88,224,188
AR 230,221
AR 231,222
Mvva 92,224,192
AR 233,223
v_mul_f32 v40,v228,v40
Mvva 96,224,196
WL 4
v_mul_f32 v41,v228,v41
v_mul_f32 v42,v228,v42
Zvav 140,240,120
s_nop 0
Ta 252,14,20480
Ta 254,14,22528
Zvav 144,240,124
LD 4,40,69,1024
Zvav 148,240,128
v_mul_f32 v43,v228,v43
v_mul_f32 v229,v228,v229
Zvav 152,240,132
v_mul_f32 v230,v228,v230
v_mul_f32 v231,v228,v231
Zvav 156,240,136
WL 2
v_mul_f32 v233,v228,v233
CV 40,40,41
Zvav 160,244,120
s_nop 0
Tv 220,13,24576
Tv 222,13,26624
Zvav 164,244,124
LD 4,40,70,2048
Zvav 168,244,128
CV 41,42,43
CV 42,229,230
Zvav 172,244,132
CV 43,231,233
AR 44,204
Zvav 176,244,136
ds_write_b128 v15,v[40:43] offset:5120
Mvav 140,248,180
LD 4,40,71,3072
Mvav 144,248,184
s_add_u32 m0,s78,36864
AR 45,205
AR 46,206
Mvav 148,248,188
AR 47,207
AR 248,224
Mvav 152,248,192
AR 249,225
AR 250,226
Mvav 156,248,196
WL 1
Tv 224,14,24576
AR 251,227
Mvav 160,252,180
s_nop 0
Tv 226,14,26624
Ta 240,13,28672
Mvav 164,252,184
Ta 242,13,30720
v_mul_f32 v44,v232,v44
Mvav 168,252,188
LD 4,40,72,0
Mvav 172,252,192
v_mul_f32 v45,v232,v45
v_mul_f32 v46,v232,v46
Mvav 176,252,196
v_mul_f32 v47,v232,v47
v_mul_f32 v248,v232,v248
Mvva 140,220,80
s_nop 0
Ta 244,14,28672
Ta 246,14,30720
Mvva 144,220,84
LD 4,40,73,1024
Mvva 148,220,88
v_mul_f32 v249,v232,v249
v_mul_f32 v250,v232,v250
Mvva 152,220,92
v_mul_f32 v251,v232,v251
CV 44,44,45
CV 45,46,47
CV 46,248,249
CV 47,250,251
AR 48,208
AR 49,209
AR 50,210
AR 51,211
AR 229,228
AR 230,229
AR 231,230
AR 233,231
v_mul_f32 v48,v236,v48
v_mul_f32 v49,v236,v49
v_mul_f32 v50,v236,v50
v_mul_f32 v51,v236,v51
v_mul_f32 v229,v236,v229
v_mul_f32 v230,v236,v230
v_mul_f32 v231,v236,v231
v_mul_f32 v233,v236,v233
CV 48,48,49
CV 49,50,51
CV 50,229,230
CV 51,231,233
AR 52,212
AR 53,213
AR 54,214
AR 55,215
AR 248,232
AR 249,233
AR 250,234
AR 251,235
v_mul_f32 v52,v240,v52
v_mul_f32 v53,v240,v53
v_mul_f32 v54,v240,v54
v_mul_f32 v55,v240,v55
v_mul_f32 v248,v240,v248
v_mul_f32 v249,v240,v249
v_mul_f32 v250,v240,v250
v_mul_f32 v251,v240,v251
CV 52,52,53
CV 53,54,55
CV 54,248,249
CV 55,250,251
AR 56,216
AR 57,217
AR 58,218
AR 59,219
AR 229,236
AR 230,237
AR 231,238
AR 233,239
v_mul_f32 v56,v244,v56
v_mul_f32 v57,v244,v57
v_mul_f32 v58,v244,v58
v_mul_f32 v59,v244,v59
v_mul_f32 v229,v244,v229
v_mul_f32 v230,v244,v230
v_mul_f32 v231,v244,v231
v_mul_f32 v233,v244,v233
CV 56,56,57
CV 57,58,59
CV 58,229,230
CV 59,231,233
Mvva 156,220,96
WL 2
ds_write_b128 v15,v[44:47] offset:6144
Mvva 160,224,80
ds_write_b128 v15,v[48:51] offset:7168
ds_write_b128 v15,v[52:55] offset:8192
ds_write_b128 v15,v[56:59] offset:9216
Mvva 164,224,84
LD 4,40,74,2048
Mvva 168,224,88
AR 60,0
AR 61,1
Mvva 172,224,92
AR 62,2
AR 63,3
Mvva 176,224,96
WL 0
s_barrier
Mvaa 140,240,120
LD 4,40,75,3072
Mvaa 144,240,124
AR 248,20
AR 249,21
Mvaa 148,240,128
AR 250,22
AR 251,23
Mvaa 152,240,132
s_nop 0
Ta 248,13,0
Ta 250,13,2048
Mvaa 156,240,136
Ta 252,14,0
v_mul_f32 v60,v228,v60
Mvaa 160,244,120
s_nop 0
Ta 254,14,2048
Tv 220,13,4096
Mvaa 164,244,124
Tv 222,13,6144
v_mul_f32 v61,v228,v61
Mvaa 168,244,128
v_mul_f32 v62,v228,v62
v_mul_f32 v63,v228,v63
Mvaa 172,244,132
v_mul_f32 v248,v228,v248
v_mul_f32 v249,v228,v249
Mvaa 176,244,136
WL 4
v_mul_f32 v250,v228,v250
v_mul_f32 v251,v228,v251
Mvaa 140,248,100
s_nop 0
Tv 224,14,4096
Tv 226,14,6144
Mvaa 144,248,104
CV 60,60,61
CV 61,62,63
Mvaa 148,248,108
CV 62,248,249
CV 63,250,251
Mvaa 152,248,112
ds_write_b128 v15,v[60:63] offset:53248
Mvaa 156,248,116
WL 3
AR 64,4
AR 65,5
Mvaa 160,252,100
AR 66,6
AR 67,7
Mvaa 164,252,104
AR 229,24
AR 230,25
Mvaa 168,252,108
AR 231,26
AR 233,27
Mvaa 172,252,112
Ta 240,13,8192
v_mul_f32 v64,v232,v64
Mvaa 176,252,116
Ta 242,13,10240
v_mul_f32 v65,v232,v65
Mvva 140,220,140
s_nop 0
Ta 244,14,8192
Ta 246,14,10240
Mvva 144,220,144
v_mul_f32 v66,v232,v66
v_mul_f32 v67,v232,v67
Mvva 148,220,148
v_mul_f32 v229,v232,v229
v_mul_f32 v230,v232,v230
Mvva 152,220,152
v_mul_f32 v231,v232,v231
v_mul_f32 v233,v232,v233
Mvva 156,220,156
WL 4
CV 64,64,65
CV 65,66,67
Mvva 160,224,140
s_nop 0
Ta 248,13,12288
Ta 250,13,14336
Mvva 164,224,144
CV 66,229,230
CV 67,231,233
Mvva 168,224,148
ds_write_b128 v15,v[64:67] offset:54272
Mvva 172,224,152
AR 68,8
AR 69,9
Mvva 176,224,156
WL 3
AR 70,10
AR 71,11
Mvaa 140,240,160
AR 248,28
AR 249,29
Mvaa 144,240,164
AR 250,30
AR 251,31
Mvaa 148,240,168
Ta 252,14,12288
v_mul_f32 v68,v236,v68
Mvaa 152,240,172
Ta 254,14,14336
v_mul_f32 v69,v236,v69
Mvaa 156,240,176
v_mul_f32 v70,v236,v70
v_mul_f32 v71,v236,v71
Mvaa 160,244,160
v_mul_f32 v248,v236,v248
v_mul_f32 v249,v236,v249
Mvaa 164,244,164
v_mul_f32 v250,v236,v250
v_mul_f32 v251,v236,v251
Mvaa 168,244,168
CV 68,68,69
CV 69,70,71
Mvaa 172,244,172
CV 70,248,249
CV 71,250,251
Mvaa 176,244,176
s_waitcnt vmcnt(0) lgkmcnt(0)
s_barrier
Mvaa 140,248,180
s_add_u32 m0,s78,0
Tv 220,13,49152
Tv 222,13,51200
Mvaa 144,248,184
s_nop 0
Tv 224,14,49152
Tv 226,14,51200
Mvaa 148,248,188
s_cmp_le_u32 s90,1
s_cselect_b64 s[40:41],s[28:29],%6
v_add_u32 v4,128,v4
Mvaa 152,248,192
LD 4,40,68,0
Mvaa 156,248,196
s_nop 0
ds_write_b128 v15,v[68:71] offset:55296
Mvaa 160,252,180
AR 72,12
AR 73,13
Mvaa 164,252,184
s_nop 0
AR 74,14
AR 75,15
Mvaa 168,252,188
LD 4,40,69,1024
Mvaa 172,252,192
s_nop 0
AR 229,32
AR 230,33
Mvaa 176,252,196
WL 1
Ta 240,13,53248
AR 231,34
Mvvv 100,220,20
s_nop 0
Ta 242,13,55296
Ta 244,14,53248
Mvvv 104,220,24
s_nop 0
Ta 246,14,55296
AR 233,35
Mvvv 108,220,28
LD 4,40,70,2048
Mvvv 112,220,32
s_nop 0
v_mul_f32 v72,v240,v72
v_mul_f32 v73,v240,v73
Mvvv 116,220,36
s_nop 0
v_mul_f32 v74,v240,v74
v_mul_f32 v75,v240,v75
Mvvv 80,224,20
s_nop 0
Ta 248,13,57344
Ta 250,13,59392
Mvvv 84,224,24
LD 4,40,71,3072
Mvvv 88,224,28
s_add_u32 m0,s78,4096
v_mul_f32 v229,v240,v229
v_mul_f32 v230,v240,v230
Mvvv 92,224,32
s_nop 0
v_mul_f32 v231,v240,v231
v_mul_f32 v233,v240,v233
Mvvv 96,224,36
WL 2
CV 72,72,73
CV 73,74,75
Mvav 100,240,40
s_nop 0
Ta 252,14,57344
Ta 254,14,59392
Mvav 104,240,44
LD 4,40,72,0
Mvav 108,240,48
s_nop 0
CV 74,229,230
CV 75,231,233
Mvav 112,240,52
ds_write_b128 v15,v[72:75] offset:56320
Mvav 116,240,56
AR 76,16
AR 77,17
Mvav 80,244,40
LD 4,40,73,1024
Mvav 84,244,44
AR 78,18
AR 79,19
Mvav 88,244,48
AR 248,36
AR 249,37
AR 250,38
AR 251,39
v_mul_f32 v76,v244,v76
v_mul_f32 v77,v244,v77
v_mul_f32 v78,v244,v78
v_mul_f32 v79,v244,v79
v_mul_f32 v248,v244,v248
v_mul_f32 v249,v244,v249
v_mul_f32 v250,v244,v250
v_mul_f32 v251,v244,v251
CV 76,76,77
CV 77,78,79
CV 78,248,249
CV 79,250,251
Mvav 92,244,52
Tv 220,13,61440
AR 200,40
Mvav 96,244,56
WL 2
Tv 222,13,63488
AR 201,41
Mvav 100,248,60
s_nop 0
Tv 224,14,61440
Tv 226,14,63488
Mvav 104,248,64
LD 4,40,74,2048
Mvav 108,248,68
ds_write_b128 v15,v[76:79] offset:57344
Mvav 112,248,72
s_nop 0
Ra 200,16,0
Ra 204,16,1024
Ra 208,16,2048
Ra 212,16,3072
Ra 216,16,4096
Ra 220,16,5120
Ra 224,16,6144
Ra 228,16,7168
Ra 232,16,8192
Ra 236,16,9216
AR 202,42
AR 203,43
AR 229,60
AR 230,61
AR 231,62
AR 233,63
v_mul_f32 v200,v228,v200
v_mul_f32 v201,v228,v201
v_mul_f32 v202,v228,v202
v_mul_f32 v203,v228,v203
v_mul_f32 v229,v228,v229
v_mul_f32 v230,v228,v230
v_mul_f32 v231,v228,v231
v_mul_f32 v233,v228,v233
CV 200,200,201
CV 201,202,203
CV 202,229,230
CV 203,231,233
AR 204,44
AR 205,45
AR 206,46
AR 207,47
AR 248,64
AR 249,65
AR 250,66
AR 251,67
v_mul_f32 v204,v232,v204
v_mul_f32 v205,v232,v205
v_mul_f32 v206,v232,v206
v_mul_f32 v207,v232,v207
v_mul_f32 v248,v232,v248
v_mul_f32 v249,v232,v249
v_mul_f32 v250,v232,v250
v_mul_f32 v251,v232,v251
CV 204,204,205
CV 205,206,207
CV 206,248,249
CV 207,250,251
AR 208,48
AR 209,49
AR 210,50
AR 211,51
AR 229,68
AR 230,69
AR 231,70
AR 233,71
v_mul_f32 v208,v236,v208
v_mul_f32 v209,v236,v209
v_mul_f32 v210,v236,v210
v_mul_f32 v211,v236,v211
v_mul_f32 v229,v236,v229
v_mul_f32 v230,v236,v230
v_mul_f32 v231,v236,v231
v_mul_f32 v233,v236,v233
CV 208,208,209
CV 209,210,211
CV 210,229,230
CV 211,231,233
AR 212,52
AR 213,53
AR 214,54
AR 215,55
AR 248,72
AR 249,73
AR 250,74
AR 251,75
v_mul_f32 v212,v240,v212
v_mul_f32 v213,v240,v213
v_mul_f32 v214,v240,v214
v_mul_f32 v215,v240,v215
v_mul_f32 v248,v240,v248
v_mul_f32 v249,v240,v249
v_mul_f32 v250,v240,v250
v_mul_f32 v251,v240,v251
CV 212,212,213
CV 213,214,215
CV 214,248,249
CV 215,250,251
AR 216,56
AR 217,57
AR 218,58
AR 219,59
AR 229,76
AR 230,77
AR 231,78
AR 233,79
v_mul_f32 v216,v244,v216
v_mul_f32 v217,v244,v217
v_mul_f32 v218,v244,v218
v_mul_f32 v219,v244,v219
v_mul_f32 v229,v244,v229
v_mul_f32 v230,v244,v230
v_mul_f32 v231,v244,v231
v_mul_f32 v233,v244,v233
CV 216,216,217
CV 217,218,219
CV 218,229,230
CV 219,231,233
Mvav 116,248,76
Mvav 80,252,60
ds_write_b128 v15,v[200:203] offset:58368
ds_write_b128 v15,v[204:207] offset:59392
ds_write_b128 v15,v[208:211] offset:60416
ds_write_b128 v15,v[212:215] offset:61440
ds_write_b128 v15,v[216:219] offset:62464
Mvav 84,252,64
LD 4,40,75,3072
Mvav 88,252,68
Mvav 92,252,72
Mvav 96,252,76
WL 0
s_barrier
Mvvv 100,220,200
s_add_u32 m0,s78,65536
s_cmp_le_u32 s90,2
s_cselect_b64 s[40:41],s[28:29],%6
Mvvv 104,220,204
s_nop 0
v_subrev_u32 v4,294784,v4
Mvvv 108,220,208
LD 4,40,68,0
Mvvv 112,220,212
s_nop 0
Ta 240,13,32768
Ta 242,13,34816
Mvvv 116,220,216
Ta 244,14,32768
Mvvv 80,224,200
s_nop 0
Ta 246,14,34816
Ta 248,13,36864
Mvvv 84,224,204
s_nop 0
Ta 250,13,38912
Ra 0,16,53248
Mvvv 88,224,208
LD 4,40,69,1024
Mvvv 92,224,212
s_nop 0
Ra 4,16,54272
Ra 8,16,55296
Mvvv 96,224,216
WL 7
Ra 12,16,56320
Mvaa 100,240,200
Ta 252,14,36864
Mvaa 104,240,204
s_nop 0
Ta 254,14,38912
Ra 16,16,57344
Mvaa 108,240,208
LD 4,40,70,2048
Mvaa 112,240,212
s_nop 0
Ra 20,16,58368
Ra 24,16,59392
Mvaa 116,240,216
WL 8
Ra 28,16,60416
Mvaa 80,244,200
Tv 220,13,40960
Mvaa 84,244,204
s_nop 0
Tv 222,13,43008
Ra 32,16,61440
Mvaa 88,244,208
LD 4,40,71,3072
Mvaa 92,244,212
s_add_u32 m0,s78,69632
Ra 36,16,62464
Mvaa 96,244,216
Mvaa 100,248,220
s_nop 0
Tv 224,14,40960
Tv 226,14,43008
Mvaa 104,248,224
LD 4,40,72,0
Mvaa 108,248,228
Mvaa 112,248,232
Mvaa 116,248,236
WL 3
Mvaa 80,252,220
s_nop 0
Ta 240,13,45056
Ta 242,13,47104
Mvaa 84,252,224
LD 4,40,73,1024
Mvaa 88,252,228
Mvaa 92,252,232
Mvaa 96,252,236
s_nop 0
Ta 244,14,45056
Ta 246,14,47104
Mvva 100,220,0
LD 4,40,74,2048
Mvva 104,220,4
Mvva 108,220,8
Mvva 112,220,12
Mvva 116,220,16
WL 2
LD 4,40,75,3072
Mvva 80,224,0
Mvva 84,224,4
Mvva 88,224,8
Mvva 92,224,12
Mvva 96,224,16
s_waitcnt vmcnt(8) lgkmcnt(0)
s_barrier
Mvaa 100,240,20
s_add_u32 m0,s78,32768
Ta 248,13,16384
Ta 250,13,18432
Mvaa 104,240,24
s_nop 0
Ta 252,14,16384
Ta 254,14,18432
Mvaa 108,240,28
v_add_u32 v4,294912,v4
Mvaa 112,240,32
LD 4,40,68,0
Mvaa 116,240,36
Mvaa 80,244,20
s_nop 0
Tv 220,13,20480
Tv 222,13,22528
Mvaa 84,244,24
Mvaa 88,244,28
LD 4,40,69,1024
Mvaa 92,244,32
Mvaa 96,244,36
WL 2
CV 100,100,101
CV 101,102,103
Mvav 140,248,20
s_bfe_u32 s76,%12,0x30008
Tv 224,14,20480
Tv 226,14,22528
Mvav 144,248,24
LD 4,40,70,2048
Mvav 148,248,28
s_mul_i32 s77,s76,184320
CV 102,80,81
CV 103,82,83
Mvav 152,248,32
s_mov_b64 s[36:37],%7
CV 104,104,105
CV 105,106,107
Mvav 156,248,36
CV 106,84,85
CV 107,86,87
Mvav 160,252,20
s_sub_u32 s36,s36,s77
s_subb_u32 s37,s37,0
Ta 240,13,24576
Ta 242,13,26624
Mvav 164,252,24
LD 4,40,71,3072
Mvav 168,252,28
s_add_u32 m0,s78,36864
CV 108,108,109
CV 109,110,111
Mvav 172,252,32
s_and_b32 s37,s37,0xffff
CV 110,88,89
CV 111,90,91
Mvav 176,252,36
WL 2
CV 112,112,113
CV 113,114,115
Mvvv 140,220,40
s_mov_b32 s38,1474560
Ta 244,14,24576
Ta 246,14,26624
Mvvv 144,220,44
LD 4,40,72,0
Mvvv 148,220,48
s_mov_b64 s[96:97],%13
CV 114,92,93
CV 115,94,95
Mvvv 152,220,52
s_and_b32 s97,s97,0xffff
CV 116,116,117
CV 117,118,119
Mvvv 156,220,56
CV 118,96,97
CV 119,98,99
Mvvv 160,224,40
s_mov_b32 s98,184320
Ta 248,13,28672
Ta 250,13,30720
Mvvv 164,224,44
LD 4,40,73,1024
Mvvv 168,224,48
s_mov_b64 s[24:25],%11
s_and_b32 s25,s25,0xffff
s_mov_b32 s26,2048
Mvvv 172,224,52
s_lshl_b32 s66,s76,7
s_add_u32 s66,s66,128
s_mov_b32 s67,0x207
Mvvv 176,224,56
WL 2
s_lshl_b32 s76,s76,2
s_add_u32 s76,s76,%8
Mvav 140,240,60
s_add_u32 s77,s76,0
Ta 252,14,28672
Ta 254,14,30720
Mvav 144,240,64
LD 4,40,74,2048
Mvav 148,240,68
s_lshl_b32 s48,s77,10
s_mul_i32 s85,s77,205
s_lshr_b32 s85,s85,10
Mvav 152,240,72
s_mul_i32 s89,s85,5
s_sub_u32 s89,s77,s89
s_lshl_b32 s60,s89,4
Mvav 156,240,76
s_and_b32 s89,s85,1
s_mul_i32 s89,s89,80
Mvav 160,244,60
s_add_u32 s60,s60,s89
Tv 220,13,0
Tv 222,13,2048
Mvav 164,244,64
LD 4,40,75,3072
Mvav 168,244,68
s_lshr_b32 s85,s85,1
s_lshl_b32 s85,s85,6
s_lshl_b32 s89,s90,7
Mvav 172,244,72
s_sub_u32 s85,s85,s89
s_cselect_b32 s89,1152,0
s_add_u32 s85,s85,s89
s_mul_i32 s54,s60,1152
Mvav 176,244,76
WL 2
s_add_u32 s54,s54,s85
s_add_u32 s77,s76,32
Mvav 140,248,200
s_lshl_b32 s49,s77,10
Tv 224,14,0
Tv 226,14,2048
Mvav 144,248,204
buffer_store_dwordx4 v[100:103],v5,s[44:47],s79 offen offset:0 sc1
Mvav 148,248,208
s_mul_i32 s85,s77,205
s_lshr_b32 s85,s85,10
s_mul_i32 s89,s85,5
Mvav 152,248,212
s_sub_u32 s89,s77,s89
s_lshl_b32 s61,s89,4
s_and_b32 s89,s85,1
Mvav 156,248,216
s_mul_i32 s89,s89,80
s_add_u32 s61,s61,s89
Mvav 160,252,200
s_lshr_b32 s85,s85,1
Ta 240,13,4096
Ta 242,13,6144
Mvav 164,252,204
buffer_store_dwordx4 v[104:107],v5,s[44:47],s79 offen offset:1024 sc1
Mvav 168,252,208
s_lshl_b32 s85,s85,6
s_lshl_b32 s89,s90,7
s_sub_u32 s85,s85,s89
s_cselect_b32 s89,1152,0
Mvav 172,252,212
s_add_u32 s85,s85,s89
s_mul_i32 s55,s61,1152
s_add_u32 s55,s55,s85
Mvav 176,252,216
WL 2
s_add_u32 s77,s76,64
s_lshl_b32 s50,s77,10
Mvva 140,220,200
s_mul_i32 s85,s77,205
Ta 244,14,4096
Ta 246,14,6144
Mvva 144,220,204
buffer_store_dwordx4 v[108:111],v5,s[44:47],s79 offen offset:2048 sc1
Mvva 148,220,208
s_lshr_b32 s85,s85,10
s_mul_i32 s89,s85,5
s_sub_u32 s89,s77,s89
Mvva 152,220,212
s_lshl_b32 s62,s89,4
s_and_b32 s89,s85,1
s_mul_i32 s89,s89,80
Mvva 156,220,216
s_add_u32 s62,s62,s89
s_lshr_b32 s85,s85,1
Mvva 160,224,200
s_lshl_b32 s85,s85,6
Ta 248,13,8192
Ta 250,13,10240
Mvva 164,224,204
buffer_store_dwordx4 v[112:115],v5,s[44:47],s79 offen offset:3072 sc1
Mvva 168,224,208
s_lshl_b32 s89,s90,7
s_sub_u32 s85,s85,s89
s_cselect_b32 s89,1152,0
s_add_u32 s85,s85,s89
Mvva 172,224,212
s_mul_i32 s56,s62,1152
s_add_u32 s56,s56,s85
s_add_u32 s77,s76,96
Mvva 176,224,216
WL 2
s_lshl_b32 s51,s77,10
s_mul_i32 s85,s77,205
Mvaa 140,240,220
s_lshr_b32 s85,s85,10
Ta 252,14,8192
Ta 254,14,10240
Mvaa 144,240,224
buffer_store_dwordx4 v[116:119],v5,s[44:47],s84 offen offset:0 sc1
Mvaa 148,240,228
s_add_u32 s79,s79,20480
s_add_u32 s84,s84,20480
s_mul_i32 s89,s85,5
Mvaa 152,240,232
s_sub_u32 s89,s77,s89
s_lshl_b32 s63,s89,4
s_and_b32 s89,s85,1
Mvaa 156,240,236
s_mul_i32 s89,s89,80
s_add_u32 s63,s63,s89
Mvaa 160,244,220
s_lshr_b32 s85,s85,1
Tv 220,13,12288
Tv 222,13,14336
Mvaa 164,244,224
s_lshl_b32 s85,s85,6
s_lshl_b32 s89,s90,7
s_sub_u32 s85,s85,s89
s_cselect_b32 s89,1152,0
Mvaa 168,244,228
s_add_u32 s85,s85,s89
s_mul_i32 s57,s63,1152
s_add_u32 s57,s57,s85
Mvaa 172,244,232
s_add_u32 s77,s76,128
s_lshl_b32 s52,s77,10
s_mul_i32 s85,s77,205
Mvaa 176,244,236
WL 2
Tv 224,14,12288
Tv 226,14,14336
Mvaa 140,248,0
s_lshr_b32 s85,s85,10
s_mul_i32 s89,s85,5
s_sub_u32 s89,s77,s89
Mvaa 144,248,4
s_lshl_b32 s64,s89,4
s_and_b32 s89,s85,1
s_mul_i32 s89,s89,80
Mvaa 148,248,8
s_add_u32 s64,s64,s89
s_lshr_b32 s85,s85,1
s_lshl_b32 s85,s85,6
Mvaa 152,248,12
s_lshl_b32 s89,s90,7
s_sub_u32 s85,s85,s89
s_cselect_b32 s89,1152,0
s_add_u32 s85,s85,s89
Mvaa 156,248,16
s_mul_i32 s58,s64,1152
s_add_u32 s58,s58,s85
Mvaa 160,252,0
s_add_u32 s77,s76,160
s_lshl_b32 s53,s77,10
s_mul_i32 s85,s77,205
Mvaa 164,252,4
s_lshr_b32 s85,s85,10
s_mul_i32 s89,s85,5
s_sub_u32 s89,s77,s89
Mvaa 168,252,8
s_lshl_b32 s65,s89,4
s_and_b32 s89,s85,1
s_mul_i32 s89,s89,80
Mvaa 172,252,12
s_add_u32 s65,s65,s89
s_lshr_b32 s85,s85,1
s_lshl_b32 s85,s85,6
Mvaa 176,252,16
s_waitcnt vmcnt(13) lgkmcnt(0)
s_barrier
Mvva 140,220,20
s_add_u32 m0,s78,0
Ta 76,253,16384
Ta 78,253,18432
Mvva 144,220,24
s_lshl_b32 s89,s90,7
Ta 72,253,20480
Ta 74,253,22528
Mvva 148,220,28
s_sub_u32 s85,s85,s89
s_cselect_b32 s89,1152,0
Ta 240,253,24576
Ta 242,253,26624
Mvva 152,220,32
s_add_u32 s85,s85,s89
Ta 244,253,28672
Ta 246,253,30720
Mvva 156,220,36
s_cmp_le_u32 s90,3
s_cselect_b64 s[40:41],s[28:29],%6
Mvva 160,224,20
s_mul_i32 s59,s65,1152
s_add_u32 s59,s59,s85
v_subrev_u32 v4,294784,v4
Mvva 164,224,24
LD 4,40,68,0
Mvva 168,224,28
s_cmp_ge_u32 s77,180
s_cselect_b32 s85,0x40000000,0
s_or_b32 s53,s53,s85
s_or_b32 s59,s59,s85
Mvva 172,224,32
s_nop 0
v_bfe_u32 v9,v5,4,4
v_mul_u32_u24 v6,1152,v9
Mvva 176,224,36
WL 4
v_lshrrev_b32 v7,8,v5
v_lshl_add_u32 v6,v7,4,v6
Zvav 100,76,120
LD 4,40,69,1024
Mvav 100,72,180
WL 0
v_lshrrev_b32 v8,2,v9
v_xor_b32 v8,v8,v7
Mvaa 100,240,80
s_nop 0
v_and_b32 v7,1,v9
v_lshlrev_b32 v7,4,v7
Mvaa 100,244,120
s_nop 0
v_mov_b32 v228,0x3f80
v_lshlrev_b32 v7,v7,v228
Zvav 104,76,124
LD 4,40,70,2048
Mvav 104,72,184
s_nop 0
Ta 248,254,16384
Ta 250,254,18432
Mvaa 104,240,84
s_nop 0
Ta 252,254,20480
Ta 254,254,22528
Mvaa 104,244,124
s_nop 0
Tv 220,254,24576
Tv 222,254,26624
Zvav 108,76,128
s_nop 0
Tv 224,254,28672
Tv 226,254,30720
Mvav 108,72,188
LD 4,40,71,3072
Mvaa 108,240,88
s_add_u32 m0,s78,4096
v_bfe_u32 v9,v9,1,1
v_lshl_or_b32 v8,v8,1,v9
Mvaa 108,244,128
s_nop 0
v_cmp_eq_u32 vcc,0,v8
s_nop 1
v_cndmask_b32 v230,0,v7,vcc
v_cmp_eq_u32 vcc,1,v8
s_nop 1
v_cndmask_b32 v231,0,v7,vcc
Zvav 112,76,132
s_nop 0
v_mov_b32 v228,0
v_mov_b32 v229,0
Mvav 112,72,192
LD 4,40,72,0
Mvaa 112,240,92
s_nop 0
v_mov_b32 v232,0
v_mov_b32 v233,0
Mvaa 112,244,132
s_nop 0
v_bfe_u32 v7,v5,4,3
v_lshlrev_b32 v7,7,v7
Zvav 116,76,136
s_nop 0
v_or_b32 v8,s87,v5
v_min_u32 v8,1,v8
Mvav 116,72,196
LD 4,40,73,1024
Mvaa 116,240,96
s_nop 0
v_lshlrev_b32 v8,30,v8
v_mov_b32 v3,%1
Mvaa 116,244,136
WL 0
CV 140,140,141
CV 141,142,143
Zvav 80,248,120
s_nop 0
CV 142,160,161
CV 143,162,163
Mvav 80,252,180
LD 4,40,74,2048
Mvva 80,220,80
CV 144,144,145
CV 145,146,147
Mvva 80,224,120
CV 146,164,165
CV 147,166,167
Zvav 84,248,124
CV 148,148,149
CV 149,150,151
Mvav 84,252,184
s_nop 0
Ta 76,253,0
Ta 78,253,2048
Mvva 84,220,84
s_nop 0
Ta 72,253,4096
Ta 74,253,6144
Mvva 84,224,124
s_nop 0
Ta 240,253,8192
Ta 242,253,10240
Zvav 88,248,128
s_nop 0
Ta 244,253,12288
Ta 246,253,14336
Mvav 88,252,188
LD 4,40,75,3072
Mvva 88,220,88
CV 150,168,169
CV 151,170,171
Mvva 88,224,128
CV 152,152,153
CV 153,154,155
Zvav 92,248,132
CV 154,172,173
CV 155,174,175
Mvav 92,252,192
buffer_store_dwordx4 v[140:143],v5,s[44:47],s79 offen offset:0 sc1
Mvva 92,220,92
CV 156,156,157
CV 157,158,159
Mvva 92,224,132
CV 158,176,177
CV 159,178,179
Zvav 96,248,136
Mvav 96,252,196
Mvva 96,220,96
buffer_store_dwordx4 v[144:147],v5,s[44:47],s79 offen offset:1024 sc1
Mvva 96,224,136
WL 0
Mvaa 100,76,100
Mvaa 100,72,140
Mvaa 100,240,160
Mvaa 100,244,180
buffer_store_dwordx4 v[148:151],v5,s[44:47],s79 offen offset:2048 sc1
Mvaa 104,76,104
Mvaa 104,72,144
s_nop 0
Ta 248,254,0
Ta 250,254,2048
Mvaa 104,240,164
s_nop 0
Ta 252,254,4096
Ta 254,254,6144
Mvaa 104,244,184
s_nop 0
Tv 220,254,8192
Tv 222,254,10240
Mvaa 108,76,108
s_nop 0
Tv 224,254,12288
Tv 226,254,14336
Mvaa 108,72,148
buffer_store_dwordx4 v[152:155],v5,s[44:47],s79 offen offset:3072 sc1
Mvaa 108,240,168
Mvaa 108,244,188
Mvaa 112,76,112
Mvaa 112,72,152
Mvaa 112,240,172
buffer_store_dwordx4 v[156:159],v5,s[44:47],s84 offen offset:0 sc1
Mvaa 112,244,192
s_add_u32 s79,s79,20480
s_add_u32 s84,s84,20480
Mvaa 116,76,116
Mvaa 116,72,156
Mvaa 116,240,176
Mvaa 116,244,196
WL 0
Mvaa 80,248,100
Mvaa 80,252,140
Mvva 80,220,160
Mvva 80,224,180
Mvaa 84,248,104
Mvaa 84,252,144
Mvva 84,220,164
Mvva 84,224,184
Mvaa 88,248,108
Mvaa 88,252,148
s_waitcnt vmcnt(18)
s_barrier
Mvva 88,220,168
s_add_u32 m0,s78,65536
Ta 76,13,49152
Ta 78,13,51200
Mvva 88,224,188
s_nop 0
Ta 72,13,53248
Ta 74,13,55296
Mvaa 92,248,112
s_nop 0
Ta 240,13,57344
Ta 242,13,59392
Mvaa 92,252,152
s_nop 0
Ta 244,13,61440
Ta 246,13,63488
Mvva 92,220,172
v_add_u32 v4,294912,v4
Mvva 92,224,192
LD 4,40,68,0
Mvaa 96,248,116
Mvaa 96,252,156
Mvva 96,220,176
Mvva 96,224,196
WL 4
LD 4,40,69,1024
Mvav 100,76,20
Mvav 100,72,40
WL 0
Mvav 100,240,60
Mvav 100,244,200
LD 4,40,70,2048
Mvav 104,76,24
Mvav 104,72,44
s_nop 0
Ta 248,14,49152
Ta 250,14,51200
Mvav 104,240,64
s_nop 0
Ta 252,14,53248
Ta 254,14,55296
Mvav 104,244,204
s_nop 0
Tv 220,14,57344
Tv 222,14,59392
Mvav 108,76,28
s_nop 0
Tv 224,14,61440
Tv 226,14,63488
Mvav 108,72,48
LD 4,40,71,3072
Mvav 108,240,68
s_add_u32 m0,s78,69632
Mvav 108,244,208
Mvav 112,76,32
Mvav 112,72,52
LD 4,40,72,0
Mvav 112,240,72
Mvav 112,244,212
Mvav 116,76,36
Mvav 116,72,56
LD 4,40,73,1024
Mvav 116,240,76
Mvav 116,244,216
WL 0
Mvav 80,248,20
Mvav 80,252,40
LD 4,40,74,2048
Mvvv 80,220,60
Mvvv 80,224,200
Mvav 84,248,24
Mvav 84,252,44
s_nop 0
Ta 76,13,32768
Ta 78,13,34816
Mvvv 84,220,64
s_nop 0
Ta 72,13,36864
Ta 74,13,38912
Mvvv 84,224,204
s_nop 0
Ta 240,13,40960
Ta 242,13,43008
Mvav 88,248,28
s_nop 0
Ta 244,13,45056
Ta 246,13,47104
Mvav 88,252,48
LD 4,40,75,3072
Mvvv 88,220,68
Mvvv 88,224,208
Mvav 92,248,32
Mvav 92,252,52
Mvvv 92,220,72
Mvvv 92,224,212
Mvav 96,248,36
Mvav 96,252,56
Mvvv 96,220,76
Mvvv 96,224,216
WL 0
Mvaa 100,76,200
Mvaa 100,72,220
Mvaa 100,240,0
Mvaa 100,244,20
Mvaa 104,76,204
Mvaa 104,72,224
s_nop 0
Ta 248,14,32768
Ta 250,14,34816
Mvaa 104,240,4
s_nop 0
Ta 252,14,36864
Ta 254,14,38912
Mvaa 104,244,24
s_nop 0
Tv 220,14,40960
Tv 222,14,43008
Mvaa 108,76,208
s_nop 0
Tv 224,14,45056
Tv 226,14,47104
Mvaa 108,72,228
Mvaa 108,240,8
Mvaa 108,244,28
Mvaa 112,76,212
Mvaa 112,72,232
Mvaa 112,240,12
Mvaa 112,244,32
Mvaa 116,76,216
Mvaa 116,72,236
Mvaa 116,240,16
Mvaa 116,244,36
WL 0
Mvaa 80,248,200
Mvaa 80,252,220
Mvva 80,220,0
Mvva 80,224,20
Mvaa 84,248,204
Mvaa 84,252,224
Mvva 84,220,4
Mvva 84,224,24
CV 100,100,101
CV 101,102,103
Mvaa 88,248,208
s_nop 0
CV 102,80,81
CV 103,82,83
Mvaa 88,252,228
s_waitcnt vmcnt(0)
s_barrier
Mvva 88,220,8
s_add_u32 m0,s78,32768
Ta 76,13,16384
Ta 78,13,18432
Mvva 88,224,28
s_nop 0
Ta 72,13,20480
Ta 74,13,22528
Mvaa 92,248,212
s_nop 0
Ta 240,13,24576
Ta 242,13,26624
Mvaa 92,252,232
s_nop 0
Ta 244,13,28672
Ta 246,13,30720
Mvva 92,220,12
s_cmp_le_u32 s90,4
s_cselect_b64 s[40:41],s[28:29],%6
v_subrev_u32 v4,294784,v4
Mvva 92,224,32
LD 4,40,68,0
Mvaa 96,248,216
s_nop 0
CV 104,104,105
CV 105,106,107
Mvaa 96,252,236
)ASM"
R"ASM(s_nop 0
CV 106,84,85
CV 107,86,87
Mvva 96,220,16
s_nop 0
CV 108,108,109
CV 109,110,111
Mvva 96,224,36
WL 4
LD 4,40,69,1024
Zvav 140,76,120
s_nop 0
CV 110,88,89
CV 111,90,91
Mvav 140,72,180
WL 0
CV 112,112,113
CV 113,114,115
Mvaa 140,240,80
s_nop 0
CV 114,92,93
CV 115,94,95
Mvaa 140,244,120
LD 4,40,70,2048
Zvav 144,76,124
s_nop 0
CV 116,116,117
CV 117,118,119
Mvav 144,72,184
s_nop 0
Ta 248,14,16384
Ta 250,14,18432
Mvaa 144,240,84
s_nop 0
Ta 252,14,20480
Ta 254,14,22528
Mvaa 144,244,124
s_nop 0
Tv 220,14,24576
Tv 222,14,26624
Zvav 148,76,128
s_nop 0
Tv 224,14,28672
Tv 226,14,30720
Mvav 148,72,188
LD 4,40,71,3072
Mvaa 148,240,88
s_add_u32 m0,s78,36864
CV 118,96,97
CV 119,98,99
Mvaa 148,244,128
Zvav 152,76,132
Mvav 152,72,192
LD 4,40,72,0
Mvaa 152,240,92
Mvaa 152,244,132
Zvav 156,76,136
Mvav 156,72,196
LD 4,40,73,1024
Mvaa 156,240,96
Mvaa 156,244,136
WL 0
Zvav 160,248,120
Mvav 160,252,180
LD 4,40,74,2048
Mvva 160,220,80
Mvva 160,224,120
Zvav 164,248,124
Mvav 164,252,184
s_nop 0
Ta 76,13,0
Ta 78,13,2048
Mvva 164,220,84
s_nop 0
Ta 72,13,4096
Ta 74,13,6144
Mvva 164,224,124
s_nop 0
Ta 240,13,8192
Ta 242,13,10240
Zvav 168,248,128
s_nop 0
Ta 244,13,12288
Ta 246,13,14336
Mvav 168,252,188
LD 4,40,75,3072
Mvva 168,220,88
Mvva 168,224,128
Zvav 172,248,132
Mvav 172,252,192
buffer_store_dwordx4 v[100:103],v5,s[44:47],s79 offen offset:0 sc1
Mvva 172,220,92
Mvva 172,224,132
Zvav 176,248,136
Mvav 176,252,196
Mvva 176,220,96
buffer_store_dwordx4 v[104:107],v5,s[44:47],s79 offen offset:1024 sc1
Mvva 176,224,136
WL 0
Mvaa 140,76,100
Mvaa 140,72,140
Mvaa 140,240,160
Mvaa 140,244,180
buffer_store_dwordx4 v[108:111],v5,s[44:47],s79 offen offset:2048 sc1
Mvaa 144,76,104
Mvaa 144,72,144
s_nop 0
Ta 248,14,0
Ta 250,14,2048
Mvaa 144,240,164
s_nop 0
Ta 252,14,4096
Ta 254,14,6144
Mvaa 144,244,184
s_nop 0
Tv 220,14,8192
Tv 222,14,10240
Mvaa 148,76,108
s_nop 0
Tv 224,14,12288
Tv 226,14,14336
Mvaa 148,72,148
buffer_store_dwordx4 v[112:115],v5,s[44:47],s79 offen offset:3072 sc1
Mvaa 148,240,168
Mvaa 148,244,188
Mvaa 152,76,112
Mvaa 152,72,152
Mvaa 152,240,172
buffer_store_dwordx4 v[116:119],v5,s[44:47],s84 offen offset:0 sc1
Mvaa 152,244,192
s_add_u32 s79,s79,20480
s_add_u32 s84,s84,20480
Mvaa 156,76,116
Mvaa 156,72,156
Mvaa 156,240,176
Mvaa 156,244,196
WL 0
Mvaa 160,248,100
Mvaa 160,252,140
Mvva 160,220,160
Mvva 160,224,180
Mvaa 164,248,104
Mvaa 164,252,144
Mvva 164,220,164
Mvva 164,224,184
Mvaa 168,248,108
Mvaa 168,252,148
s_barrier
Mvva 168,220,168
s_add_u32 m0,s78,0
Ta 76,253,16384
Ta 78,253,18432
Mvva 168,224,188
s_nop 0
Ta 72,253,20480
Ta 74,253,22528
Mvaa 172,248,112
s_nop 0
Ta 240,253,24576
Ta 242,253,26624
Mvaa 172,252,152
s_nop 0
Ta 244,253,28672
Ta 246,253,30720
Mvva 172,220,172
s_nop 0
v_add_u32 v4,294912,v4
Mvva 172,224,192
LD 4,40,68,0
Mvaa 176,248,116
s_nop 0
Mvaa 176,252,156
Mvva 176,220,176
Mvva 176,224,196
WL 4
LD 4,40,69,1024
Mvav 140,76,20
s_nop 0
Mvav 140,72,40
WL 0
Mvav 140,240,60
s_nop 0
Mvav 140,244,200
LD 4,40,70,2048
Mvav 144,76,24
s_nop 0
Mvav 144,72,44
s_nop 0
Ta 248,254,16384
Ta 250,254,18432
Mvav 144,240,64
s_nop 0
Ta 252,254,20480
Ta 254,254,22528
Mvav 144,244,204
s_nop 0
Tv 220,254,24576
Tv 222,254,26624
Mvav 148,76,28
s_nop 0
Tv 224,254,28672
Tv 226,254,30720
Mvav 148,72,48
LD 4,40,71,3072
Mvav 148,240,68
s_add_u32 m0,s78,4096
Mvav 148,244,208
s_nop 0
Mvav 152,76,32
Mvav 152,72,52
LD 4,40,72,0
Mvav 152,240,72
s_nop 0
Mvav 152,244,212
Mvav 156,76,36
Mvav 156,72,56
buffer_load_dwordx2 v[0:1],v7,s[24:27],0 offen offset:128 sc1
Mvav 156,240,76
s_nop 0
Mvav 156,244,216
WL 0
Mvav 160,248,20
s_nop 0
Mvav 160,252,40
LD 4,40,73,1024
Mvvv 160,220,60
Mvvv 160,224,200
Mvav 164,248,24
Mvav 164,252,44
s_nop 0
Ta 76,253,0
Ta 78,253,2048
Mvvv 164,220,64
s_nop 0
Ta 72,253,4096
Ta 74,253,6144
Mvvv 164,224,204
s_nop 0
Ta 240,253,8192
Ta 242,253,10240
Mvav 168,248,28
s_nop 0
Ta 244,253,12288
Ta 246,253,14336
Mvav 168,252,48
LD 4,40,74,2048
Mvvv 168,220,68
Mvvv 168,224,208
Mvav 172,248,32
Mvav 172,252,52
LD 4,40,75,3072
Mvvv 172,220,72
Mvvv 172,224,212
Mvav 176,248,36
Mvav 176,252,56
Mvvv 176,220,76
Mvvv 176,224,216
WL 0
Mvaa 140,76,200
Mvaa 140,72,220
Mvaa 140,240,0
Mvaa 140,244,20
Mvaa 144,76,204
Mvaa 144,72,224
s_nop 0
Ta 248,254,0
Ta 250,254,2048
Mvaa 144,240,4
s_nop 0
Ta 252,254,4096
Ta 254,254,6144
Mvaa 144,244,24
s_nop 0
Tv 220,254,8192
Tv 222,254,10240
Mvaa 148,76,208
s_nop 0
Tv 224,254,12288
Tv 226,254,14336
Mvaa 148,72,228
Mvaa 148,240,8
Mvaa 148,244,28
Mvaa 152,76,212
Mvaa 152,72,232
Mvaa 152,240,12
Mvaa 152,244,32
Mvaa 156,76,216
Mvaa 156,72,236
Mvaa 156,240,16
Mvaa 156,244,36
WL 0
Mvaa 160,248,200
Mvaa 160,252,220
Mvva 160,220,0
Mvva 160,224,20
Mvaa 164,248,204
Mvaa 164,252,224
Mvva 164,220,4
Mvva 164,224,24
CV 140,140,141
CV 141,142,143
Mvaa 168,248,208
s_nop 0
CV 142,160,161
CV 143,162,163
Mvaa 168,252,228
s_waitcnt vmcnt(14)
s_barrier
Mvva 168,220,8
s_add_u32 m0,s78,65536
Ta 76,13,49152
Ta 78,13,51200
Mvva 168,224,28
s_nop 0
Ta 72,13,53248
Ta 74,13,55296
Mvaa 172,248,212
s_nop 0
Ta 240,13,57344
Ta 242,13,59392
Mvaa 172,252,232
s_nop 0
Ta 244,13,61440
Ta 246,13,63488
Mvva 172,220,12
s_cmp_le_u32 s90,5
s_cselect_b64 s[40:41],s[28:29],%6
v_subrev_u32 v4,294784,v4
Mvva 172,224,32
LD 4,40,68,0
Mvaa 176,248,216
CV 144,144,145
CV 145,146,147
Mvaa 176,252,236
CV 146,164,165
CV 147,166,167
Mvva 176,220,16
CV 148,148,149
CV 149,150,151
Mvva 176,224,36
WL 4
LD 4,40,69,1024
Zvav 100,76,120
CV 150,168,169
CV 151,170,171
Mvav 100,72,180
WL 0
CV 152,152,153
CV 153,154,155
Mvaa 100,240,80
CV 154,172,173
CV 155,174,175
Mvaa 100,244,120
LD 4,40,70,2048
Zvav 104,76,124
CV 156,156,157
CV 157,158,159
Mvav 104,72,184
s_nop 0
Ta 248,14,49152
Ta 250,14,51200
Mvaa 104,240,84
s_nop 0
Ta 252,14,53248
Ta 254,14,55296
Mvaa 104,244,124
s_nop 0
Tv 220,14,57344
Tv 222,14,59392
Zvav 108,76,128
s_nop 0
Tv 224,14,61440
Tv 226,14,63488
Mvav 108,72,188
LD 4,40,71,3072
Mvaa 108,240,88
s_add_u32 m0,s78,69632
CV 158,176,177
CV 159,178,179
Mvaa 108,244,128
Zvav 112,76,132
Mvav 112,72,192
LD 4,40,72,0
Mvaa 112,240,92
Mvaa 112,244,132
Zvav 116,76,136
Mvav 116,72,196
LD 4,40,73,1024
Mvaa 116,240,96
Mvaa 116,244,136
s_waitcnt vmcnt(9) lgkmcnt(0)
s_add_u32 s80,%0,2
s_mov_b32 s81,0x40000
1:
v_subrev_u32 v9,s80,v0
v_cmp_eq_u32 s[82:83],%1,v1
v_cmp_ge_u32 vcc,13,v9
s_nop 1
s_and_b64 vcc,vcc,s[82:83]
s_cmp_eq_u64 vcc,-1
s_cbranch_scc1 2f
s_sleep 1
buffer_load_dwordx2 v[0:1],v7,s[24:27],0 offen offset:128 sc1
s_sub_u32 s81,s81,1
s_waitcnt vmcnt(0)
s_cmp_lg_u32 s81,0
s_cbranch_scc1 1b
2:
Zvav 80,248,120
s_add_u32 s30,s48,0
Mvav 80,252,180
LD 4,40,74,2048
Mvva 80,220,80
Mvva 80,224,120
Zvav 84,248,124
Mvav 84,252,184
s_nop 0
Ta 76,13,32768
Ta 78,13,34816
Mvva 84,220,84
s_nop 0
Ta 72,13,36864
Ta 74,13,38912
Mvva 84,224,124
s_nop 0
Ta 240,13,40960
Ta 242,13,43008
Zvav 88,248,128
s_nop 0
Ta 244,13,45056
Ta 246,13,47104
Mvav 88,252,188
LD 4,40,75,3072
Mvva 88,220,88
Mvva 88,224,128
Zvav 92,248,132
Mvav 92,252,192
buffer_load_dwordx4 a[40:43],v5,s[36:39],s30 offen sc1
Mvva 92,220,92
s_add_u32 s30,s48,184320
Mvva 92,224,132
Zvav 96,248,136
Mvav 96,252,196
buffer_load_dwordx4 a[44:47],v5,s[36:39],s30 offen sc1
Mvva 96,220,96
s_add_u32 s30,s48,368640
Mvva 96,224,136
WL 0
Mvaa 100,76,100
Mvaa 100,72,140
buffer_load_dwordx4 a[48:51],v5,s[36:39],s30 offen sc1
Mvaa 100,240,160
s_add_u32 s30,s48,552960
Mvaa 100,244,180
Mvaa 104,76,104
Mvaa 104,72,144
s_nop 0
Ta 248,14,32768
Ta 250,14,34816
Mvaa 104,240,164
s_nop 0
Ta 252,14,36864
Ta 254,14,38912
Mvaa 104,244,184
s_nop 0
Tv 220,14,40960
Tv 222,14,43008
Mvaa 108,76,108
s_nop 0
Tv 224,14,45056
Tv 226,14,47104
Mvaa 108,72,148
buffer_load_dwordx4 a[52:55],v5,s[36:39],s30 offen sc1
Mvaa 108,240,168
s_add_u32 s30,s48,737280
Mvaa 108,244,188
Mvaa 112,76,112
Mvaa 112,72,152
buffer_load_dwordx4 a[56:59],v5,s[36:39],s30 offen sc1
Mvaa 112,240,172
s_add_u32 s30,s48,921600
Mvaa 112,244,192
Mvaa 116,76,116
Mvaa 116,72,156
buffer_load_dwordx4 a[60:63],v5,s[36:39],s30 offen sc1
Mvaa 116,240,176
s_add_u32 s30,s48,1105920
Mvaa 116,244,196
WL 0
Mvaa 80,248,100
Mvaa 80,252,140
buffer_load_dwordx4 a[64:67],v5,s[36:39],s30 offen sc1
Mvva 80,220,160
s_add_u32 s30,s48,1290240
Mvva 80,224,180
Mvaa 84,248,104
Mvaa 84,252,144
buffer_load_dwordx4 a[68:71],v5,s[36:39],s30 offen sc1
Mvva 84,220,164
Mvva 84,224,184
Mvaa 88,248,108
Mvaa 88,252,148
s_waitcnt vmcnt(16)
s_barrier
Mvva 88,220,168
s_add_u32 m0,s78,32768
Ta 76,13,16384
Ta 78,13,18432
Mvva 88,224,188
s_nop 0
Ta 72,13,20480
Ta 74,13,22528
Mvaa 92,248,112
s_nop 0
Ta 240,13,24576
Ta 242,13,26624
Mvaa 92,252,152
s_nop 0
Ta 244,13,28672
Ta 246,13,30720
Mvva 92,220,172
v_add_u32 v4,294912,v4
Mvva 92,224,192
LD 4,40,68,0
Mvaa 96,248,116
Mvaa 96,252,156
Mvva 96,220,176
Mvva 96,224,196
WL 4
LD 4,40,69,1024
Mvav 100,76,20
Mvav 100,72,40
WL 0
Mvav 100,240,60
Mvav 100,244,200
LD 4,40,70,2048
Mvav 104,76,24
Mvav 104,72,44
s_nop 0
Ta 248,14,16384
Ta 250,14,18432
Mvav 104,240,64
s_nop 0
Ta 252,14,20480
Ta 254,14,22528
Mvav 104,244,204
s_nop 0
Tv 220,14,24576
Tv 222,14,26624
Mvav 108,76,28
s_nop 0
Tv 224,14,28672
Tv 226,14,30720
Mvav 108,72,48
LD 4,40,71,3072
Mvav 108,240,68
s_add_u32 m0,s78,36864
Mvav 108,244,208
Mvav 112,76,32
Mvav 112,72,52
LD 4,40,72,0
Mvav 112,240,72
Mvav 112,244,212
Mvav 116,76,36
Mvav 116,72,56
LD 4,40,73,1024
Mvav 116,240,76
Mvav 116,244,216
WL 0
Mvav 80,248,20
Mvav 80,252,40
LD 4,40,74,2048
Mvvv 80,220,60
Mvvv 80,224,200
Mvav 84,248,24
Mvav 84,252,44
s_nop 0
Ta 76,13,0
Ta 78,13,2048
Mvvv 84,220,64
s_nop 0
Ta 72,13,4096
Ta 74,13,6144
Mvvv 84,224,204
s_nop 0
Ta 240,13,8192
Ta 242,13,10240
Mvav 88,248,28
s_nop 0
Ta 244,13,12288
Ta 246,13,14336
Mvav 88,252,48
LD 4,40,75,3072
Mvvv 88,220,68
Mvvv 88,224,208
Mvav 92,248,32
Mvav 92,252,52
buffer_store_dwordx4 v[140:143],v5,s[44:47],s79 offen offset:0 sc1
Mvvv 92,220,72
Mvvv 92,224,212
Mvav 96,248,36
Mvav 96,252,56
Mvvv 96,220,76
buffer_store_dwordx4 v[144:147],v5,s[44:47],s79 offen offset:1024 sc1
Mvvv 96,224,216
WL 0
Mvaa 100,76,200
Mvaa 100,72,220
Mvaa 100,240,0
Mvaa 100,244,20
buffer_store_dwordx4 v[148:151],v5,s[44:47],s79 offen offset:2048 sc1
Mvaa 104,76,204
Mvaa 104,72,224
s_nop 0
Ta 248,14,0
Ta 250,14,2048
Mvaa 104,240,4
s_nop 0
Ta 252,14,4096
Ta 254,14,6144
Mvaa 104,244,24
s_nop 0
Tv 220,14,8192
Tv 222,14,10240
Mvaa 108,76,208
s_nop 0
Tv 224,14,12288
Tv 226,14,14336
Mvaa 108,72,228
buffer_store_dwordx4 v[152:155],v5,s[44:47],s79 offen offset:3072 sc1
Mvaa 108,240,8
Mvaa 108,244,28
Mvaa 112,76,212
Mvaa 112,72,232
Mvaa 112,240,12
buffer_store_dwordx4 v[156:159],v5,s[44:47],s84 offen offset:0 sc1
Mvaa 112,244,32
s_add_u32 s79,s79,20480
s_add_u32 s84,s84,20480
Mvaa 116,76,216
Mvaa 116,72,236
Mvaa 116,240,16
Mvaa 116,244,36
WL 0
Mvaa 80,248,200
Mvaa 80,252,220
Mvva 80,220,0
Mvva 80,224,20
Mvaa 84,248,204
Mvaa 84,252,224
Mvva 84,220,4
Mvva 84,224,24
CV 100,100,101
CV 101,102,103
Mvaa 88,248,208
s_nop 0
CV 102,80,81
CV 103,82,83
Mvaa 88,252,228
s_waitcnt vmcnt(21)
s_barrier
Mvva 88,220,8
s_add_u32 m0,s78,0
Ta 76,253,16384
Ta 78,253,18432
Mvva 88,224,28
s_nop 0
Ta 72,253,20480
Ta 74,253,22528
Mvaa 92,248,212
s_nop 0
Ta 240,253,24576
Ta 242,253,26624
Mvaa 92,252,232
s_nop 0
Ta 244,253,28672
Ta 246,253,30720
Mvva 92,220,12
s_cmp_le_u32 s90,6
s_cselect_b64 s[40:41],s[28:29],%6
v_subrev_u32 v4,294784,v4
Mvva 92,224,32
LD 4,40,68,0
Mvaa 96,248,216
CV 104,104,105
CV 105,106,107
Mvaa 96,252,236
CV 106,84,85
CV 107,86,87
Mvva 96,220,16
CV 108,108,109
CV 109,110,111
Mvva 96,224,36
WL 4
LD 4,40,69,1024
Zvav 140,76,120
CV 110,88,89
CV 111,90,91
Mvav 140,72,180
WL 0
CV 112,112,113
CV 113,114,115
Mvaa 140,240,80
CV 114,92,93
CV 115,94,95
Mvaa 140,244,120
LD 4,40,70,2048
Zvav 144,76,124
CV 116,116,117
CV 117,118,119
Mvav 144,72,184
s_nop 0
Ta 248,254,16384
Ta 250,254,18432
Mvaa 144,240,84
s_nop 0
Ta 252,254,20480
Ta 254,254,22528
Mvaa 144,244,124
s_nop 0
Tv 220,254,24576
Tv 222,254,26624
Zvav 148,76,128
s_nop 0
Tv 224,254,28672
Tv 226,254,30720
Mvav 148,72,188
LD 4,40,71,3072
Mvaa 148,240,88
s_add_u32 m0,s78,4096
CV 118,96,97
CV 119,98,99
Mvaa 148,244,128
Zvav 152,76,132
Mvav 152,72,192
LD 4,40,72,0
Mvaa 152,240,92
Mvaa 152,244,132
Zvav 156,76,136
Mvav 156,72,196
LD 4,40,73,1024
Mvaa 156,240,96
Mvaa 156,244,136
WL 0
Zvav 160,248,120
Mvav 160,252,180
LD 4,40,74,2048
Mvva 160,220,80
Mvva 160,224,120
Zvav 164,248,124
Mvav 164,252,184
s_nop 0
Ta 76,253,0
Ta 78,253,2048
Mvva 164,220,84
s_nop 0
Ta 72,253,4096
Ta 74,253,6144
Mvva 164,224,124
s_nop 0
Ta 240,253,8192
Ta 242,253,10240
Zvav 168,248,128
s_nop 0
Ta 244,253,12288
Ta 246,253,14336
Mvav 168,252,188
LD 4,40,75,3072
Mvva 168,220,88
Mvva 168,224,128
Zvav 172,248,132
Mvav 172,252,192
buffer_store_dwordx4 v[100:103],v5,s[44:47],s79 offen offset:0 sc1
Mvva 172,220,92
Mvva 172,224,132
Zvav 176,248,136
Mvav 176,252,196
Mvva 176,220,96
buffer_store_dwordx4 v[104:107],v5,s[44:47],s79 offen offset:1024 sc1
Mvva 176,224,136
WL 0
Mvaa 140,76,100
Mvaa 140,72,140
Mvaa 140,240,160
Mvaa 140,244,180
buffer_store_dwordx4 v[108:111],v5,s[44:47],s79 offen offset:2048 sc1
Mvaa 144,76,104
Mvaa 144,72,144
s_nop 0
Ta 248,254,0
Ta 250,254,2048
Mvaa 144,240,164
s_nop 0
Ta 252,254,4096
Ta 254,254,6144
Mvaa 144,244,184
s_nop 0
Tv 220,254,8192
Tv 222,254,10240
Mvaa 148,76,108
s_nop 0
Tv 224,254,12288
Tv 226,254,14336
Mvaa 148,72,148
buffer_store_dwordx4 v[112:115],v5,s[44:47],s79 offen offset:3072 sc1
Mvaa 148,240,168
Mvaa 148,244,188
Mvaa 152,76,112
Mvaa 152,72,152
Mvaa 152,240,172
buffer_store_dwordx4 v[116:119],v5,s[44:47],s84 offen offset:0 sc1
Mvaa 152,244,192
s_add_u32 s79,s79,20480
s_add_u32 s84,s84,20480
Mvaa 156,76,116
Mvaa 156,72,156
Mvaa 156,240,176
Mvaa 156,244,196
WL 0
Mvaa 160,248,100
Mvaa 160,252,140
Mvva 160,220,160
Mvva 160,224,180
Mvaa 164,248,104
Mvaa 164,252,144
Mvva 164,220,164
Mvva 164,224,184
Mvaa 168,248,108
Mvaa 168,252,148
s_waitcnt vmcnt(18)
s_barrier
Mvva 168,220,168
s_add_u32 m0,s78,65536
v_add_u32 v4,294912,v4
Mvva 168,224,188
LD 4,40,68,0
Mvaa 172,248,112
s_nop 0
Mvaa 172,252,152
Mvva 172,220,172
Mvva 172,224,192
LD 4,40,69,1024
Mvaa 176,248,116
s_nop 0
Mvaa 176,252,156
Mvva 176,220,176
Mvva 176,224,196
LD 4,40,70,2048
Zvva 236,230,40
s_nop 0
Zvva 240,228,40
s_nop 0
Ta 76,13,49152
Ta 78,13,51200
Mvva 236,230,44
s_nop 0
Ta 72,13,53248
Ta 74,13,55296
Mvva 240,228,44
s_nop 0
Ta 240,13,57344
Ta 242,13,59392
Mvva 236,230,48
s_nop 0
Ta 244,13,61440
Ta 246,13,63488
Mvva 240,228,48
LD 4,40,71,3072
Mvva 236,230,52
s_add_u32 m0,s78,69632
Mvva 240,228,52
s_nop 0
Mvva 236,230,56
Mvva 240,228,56
LD 4,40,72,0
Mvva 236,230,60
s_nop 0
Mvva 240,228,60
Mvva 236,230,64
Mvva 240,228,64
LD 4,40,73,1024
Mvva 236,230,68
s_nop 0
Mvva 240,228,68
WL 0
Mvav 140,76,20
s_nop 0
Mvav 140,72,40
LD 4,40,74,2048
Mvav 140,240,60
s_nop 0
Mvav 140,244,200
s_nop 0
v_cmp_class_f32 vcc,v236,s67
s_nop 1
s_cmp_eq_u64 vcc,0
s_cbranch_scc1 3f
s_lshl_b32 s80,s60,2
s_add_u32 s80,s80,%9
s_add_u32 s80,s80,159744
v_bfe_u32 v9,v5,4,4
v_lshl_add_u32 v9,v9,2,s80
v_mov_b32 v0,1
s_mov_b64 exec,vcc
ds_write_b32 v9,v0
s_add_u32 s80,%9,160384
v_mov_b32 v9,s80
ds_write_b32 v9,v0
s_mov_b64 exec,-1
s_waitcnt lgkmcnt(0)
s_nop 4
3:
Mvav 144,76,24
s_nop 0
CV 236,236,237
CV 237,238,239
Mvav 144,72,44
s_nop 0
Ta 248,14,49152
Ta 250,14,51200
Mvav 144,240,64
s_nop 0
Ta 252,14,53248
Ta 254,14,55296
Mvav 144,244,204
s_nop 0
Tv 220,14,57344
Tv 222,14,59392
Mvav 148,76,28
s_nop 0
Tv 224,14,61440
Tv 226,14,63488
Mvav 148,72,48
LD 4,40,75,3072
Mvav 148,240,68
s_nop 0
CV 238,240,241
CV 239,242,243
Mvav 148,244,208
v_permlane32_swap_b32 v236,v238
Mvav 152,76,32
v_permlane32_swap_b32 v237,v239
Mvav 152,72,52
v_permlane16_swap_b32 v236,v238
Mvav 152,240,72
v_permlane16_swap_b32 v237,v239
Mvav 152,244,212
buffer_store_dwordx4 v[236:239],v6,s[96:99],s54 offen
Mvav 156,76,36
Mvav 156,72,56
Mvav 156,240,76
Mvav 156,244,216
WL 0
Mvav 160,248,20
Mvav 160,252,40
Mvvv 160,220,60
Mvvv 160,224,200
Mvav 164,248,24
Mvav 164,252,44
s_nop 0
Ta 76,13,32768
Ta 78,13,34816
Mvvv 164,220,64
s_nop 0
Ta 72,13,36864
Ta 74,13,38912
Mvvv 164,224,204
s_nop 0
Ta 240,13,40960
Ta 242,13,43008
Mvav 168,248,28
s_nop 0
Ta 244,13,45056
Ta 246,13,47104
Mvav 168,252,48
Mvvv 168,220,68
Mvvv 168,224,208
Mvav 172,248,32
Mvav 172,252,52
Mvvv 172,220,72
Mvvv 172,224,212
Mvav 176,248,36
Mvav 176,252,56
Mvvv 176,220,76
Mvvv 176,224,216
WL 0
Mvaa 140,76,200
Mvaa 140,72,220
Mvaa 140,240,0
Mvaa 140,244,20
Mvaa 144,76,204
Mvaa 144,72,224
s_nop 0
Ta 248,14,32768
Ta 250,14,34816
Mvaa 144,240,4
s_nop 0
Ta 252,14,36864
Ta 254,14,38912
Mvaa 144,244,24
s_nop 0
Tv 220,14,40960
Tv 222,14,43008
Mvaa 148,76,208
s_nop 0
Tv 224,14,45056
Tv 226,14,47104
Mvaa 148,72,228
Mvaa 148,240,8
Mvaa 148,244,28
Mvaa 152,76,212
Mvaa 152,72,232
Mvaa 152,240,12
Mvaa 152,244,32
Mvaa 156,76,216
Mvaa 156,72,236
Mvaa 156,240,16
Mvaa 156,244,36
WL 0
Mvaa 160,248,200
Mvaa 160,252,220
Mvva 160,220,0
Mvva 160,224,20
Mvaa 164,248,204
Mvaa 164,252,224
Mvva 164,220,4
Mvva 164,224,24
CV 140,140,141
CV 141,142,143
Mvaa 168,248,208
s_nop 0
CV 142,160,161
CV 143,162,163
Mvaa 168,252,228
s_waitcnt vmcnt(0)
s_barrier
Mvva 168,220,8
s_add_u32 m0,s78,32768
Ta 76,13,16384
Ta 78,13,18432
Mvva 168,224,28
s_nop 0
Ta 72,13,20480
Ta 74,13,22528
Mvaa 172,248,212
s_nop 0
Ta 240,13,24576
Ta 242,13,26624
Mvaa 172,252,232
s_nop 0
Ta 244,13,28672
Ta 246,13,30720
Mvva 172,220,12
s_cmp_le_u32 s90,7
s_cselect_b64 s[40:41],s[28:29],%6
v_subrev_u32 v4,294784,v4
Mvva 172,224,32
LD 4,40,68,0
Mvaa 176,248,216
s_nop 0
CV 144,144,145
CV 145,146,147
Mvaa 176,252,236
s_nop 0
CV 146,164,165
CV 147,166,167
Mvva 176,220,16
s_nop 0
CV 148,148,149
CV 149,150,151
Mvva 176,224,36
WL 4
LD 4,40,69,1024
Zvav 100,76,120
s_nop 0
CV 150,168,169
CV 151,170,171
Mvav 100,72,180
WL 0
CV 152,152,153
CV 153,154,155
Mvaa 100,240,80
s_nop 0
CV 154,172,173
CV 155,174,175
Mvaa 100,244,120
LD 4,40,70,2048
Zvav 104,76,124
s_nop 0
CV 156,156,157
CV 157,158,159
Mvav 104,72,184
s_nop 0
Ta 248,14,16384
Ta 250,14,18432
Mvaa 104,240,84
s_nop 0
Ta 252,14,20480
Ta 254,14,22528
Mvaa 104,244,124
s_nop 0
Tv 220,14,24576
Tv 222,14,26624
Zvav 108,76,128
s_nop 0
Tv 224,14,28672
Tv 226,14,30720
Mvav 108,72,188
LD 4,40,71,3072
Mvaa 108,240,88
s_add_u32 m0,s78,36864
CV 158,176,177
CV 159,178,179
Mvaa 108,244,128
Zvav 112,76,132
Mvav 112,72,192
LD 4,40,72,0
Mvaa 112,240,92
Mvaa 112,244,132
Zvav 116,76,136
Mvav 116,72,196
LD 4,40,73,1024
Mvaa 116,240,96
Mvaa 116,244,136
WL 0
Zvav 80,248,120
Mvav 80,252,180
LD 4,40,74,2048
Mvva 80,220,80
Mvva 80,224,120
Zvav 84,248,124
Mvav 84,252,184
s_nop 0
Ta 76,13,0
Ta 78,13,2048
Mvva 84,220,84
s_nop 0
Ta 72,13,4096
Ta 74,13,6144
Mvva 84,224,124
s_nop 0
Ta 240,13,8192
Ta 242,13,10240
Zvav 88,248,128
s_nop 0
Ta 244,13,12288
Ta 246,13,14336
Mvav 88,252,188
LD 4,40,75,3072
Mvva 88,220,88
Mvva 88,224,128
Zvav 92,248,132
Mvav 92,252,192
buffer_store_dwordx4 v[140:143],v5,s[44:47],s79 offen offset:0 sc1
Mvva 92,220,92
Mvva 92,224,132
Zvav 96,248,136
Mvav 96,252,196
Mvva 96,220,96
buffer_store_dwordx4 v[144:147],v5,s[44:47],s79 offen offset:1024 sc1
Mvva 96,224,136
WL 0
Mvaa 100,76,100
Mvaa 100,72,140
Mvaa 100,240,160
Mvaa 100,244,180
buffer_store_dwordx4 v[148:151],v5,s[44:47],s79 offen offset:2048 sc1
Mvaa 104,76,104
Mvaa 104,72,144
s_nop 0
Ta 248,14,0
Ta 250,14,2048
Mvaa 104,240,164
s_nop 0
Ta 252,14,4096
Ta 254,14,6144
Mvaa 104,244,184
s_nop 0
Tv 220,14,8192
Tv 222,14,10240
Mvaa 108,76,108
s_nop 0
Tv 224,14,12288
Tv 226,14,14336
Mvaa 108,72,148
buffer_store_dwordx4 v[152:155],v5,s[44:47],s79 offen offset:3072 sc1
Mvaa 108,240,168
Mvaa 108,244,188
Mvaa 112,76,112
Mvaa 112,72,152
Mvaa 112,240,172
buffer_store_dwordx4 v[156:159],v5,s[44:47],s84 offen offset:0 sc1
Mvaa 112,244,192
s_add_u32 s79,s79,20480
s_add_u32 s84,s84,20480
Mvaa 116,76,116
Mvaa 116,72,156
Mvaa 116,240,176
Mvaa 116,244,196
WL 0
Mvaa 80,248,100
Mvaa 80,252,140
Mvva 80,220,160
Mvva 80,224,180
Mvaa 84,248,104
Mvaa 84,252,144
Mvva 84,220,164
Mvva 84,224,184
Mvaa 88,248,108
Mvaa 88,252,148
s_barrier
Mvva 88,220,168
s_add_u32 m0,s78,0
Ta 76,253,16384
Ta 78,253,18432
Mvva 88,224,188
s_nop 0
Ta 72,253,20480
Ta 74,253,22528
Mvaa 92,248,112
s_nop 0
Ta 240,253,24576
Ta 242,253,26624
Mvaa 92,252,152
s_nop 0
Ta 244,253,28672
Ta 246,253,30720
Mvva 92,220,172
v_add_u32 v4,294912,v4
Mvva 92,224,192
LD 4,40,68,0
Mvaa 96,248,116
Mvaa 96,252,156
Mvva 96,220,176
Mvva 96,224,196
WL 4
LD 4,40,69,1024
Mvav 100,76,20
Mvav 100,72,40
WL 0
Mvav 100,240,60
Mvav 100,244,200
LD 4,40,70,2048
Mvav 104,76,24
Mvav 104,72,44
s_nop 0
Ta 248,254,16384
Ta 250,254,18432
Mvav 104,240,64
s_nop 0
Ta 252,254,20480
Ta 254,254,22528
Mvav 104,244,204
s_nop 0
Tv 220,254,24576
Tv 222,254,26624
Mvav 108,76,28
s_nop 0
Tv 224,254,28672
Tv 226,254,30720
Mvav 108,72,48
LD 4,40,71,3072
Mvav 108,240,68
s_add_u32 m0,s78,4096
Mvav 108,244,208
Mvav 112,76,32
Mvav 112,72,52
LD 4,40,72,0
Mvav 112,240,72
Mvav 112,244,212
Mvav 116,76,36
Mvav 116,72,56
buffer_load_dwordx2 v[0:1],v7,s[24:27],0 offen offset:128 sc1
Mvav 116,240,76
Mvav 116,244,216
WL 0
Mvav 80,248,20
Mvav 80,252,40
LD 4,40,73,1024
Mvvv 80,220,60
Mvvv 80,224,200
Mvav 84,248,24
Mvav 84,252,44
s_nop 0
Ta 76,253,0
Ta 78,253,2048
Mvvv 84,220,64
s_nop 0
Ta 72,253,4096
Ta 74,253,6144
Mvvv 84,224,204
s_nop 0
Ta 240,253,8192
Ta 242,253,10240
Mvav 88,248,28
s_nop 0
Ta 244,253,12288
Ta 246,253,14336
Mvav 88,252,48
LD 4,40,74,2048
Mvvv 88,220,68
Mvvv 88,224,208
Mvav 92,248,32
Mvav 92,252,52
LD 4,40,75,3072
Mvvv 92,220,72
Mvvv 92,224,212
Mvav 96,248,36
Mvav 96,252,56
Mvvv 96,220,76
Mvvv 96,224,216
WL 0
Mvaa 100,76,200
Mvaa 100,72,220
Mvaa 100,240,0
Mvaa 100,244,20
Mvaa 104,76,204
Mvaa 104,72,224
s_nop 0
Ta 248,254,0
Ta 250,254,2048
Mvaa 104,240,4
s_nop 0
Ta 252,254,4096
Ta 254,254,6144
Mvaa 104,244,24
s_nop 0
Tv 220,254,8192
Tv 222,254,10240
Mvaa 108,76,208
s_nop 0
Tv 224,254,12288
Tv 226,254,14336
Mvaa 108,72,228
Mvaa 108,240,8
Mvaa 108,244,28
Mvaa 112,76,212
Mvaa 112,72,232
Mvaa 112,240,12
Mvaa 112,244,32
Mvaa 116,76,216
Mvaa 116,72,236
Mvaa 116,240,16
Mvaa 116,244,36
WL 0
Mvaa 80,248,200
Mvaa 80,252,220
Mvva 80,220,0
Mvva 80,224,20
Mvaa 84,248,204
Mvaa 84,252,224
Mvva 84,220,4
Mvva 84,224,24
CV 100,100,101
CV 101,102,103
Mvaa 88,248,208
s_nop 0
CV 102,80,81
CV 103,82,83
Mvaa 88,252,228
s_waitcnt vmcnt(14)
s_barrier
Mvva 88,220,8
s_add_u32 m0,s78,65536
Ta 76,13,49152
Ta 78,13,51200
Mvva 88,224,28
s_nop 0
Ta 72,13,53248
Ta 74,13,55296
Mvaa 92,248,212
s_nop 0
Ta 240,13,57344
Ta 242,13,59392
Mvaa 92,252,232
s_nop 0
Ta 244,13,61440
Ta 246,13,63488
Mvva 92,220,12
s_cmp_le_u32 s90,8
s_cselect_b64 s[40:41],s[28:29],%6
v_subrev_u32 v4,294784,v4
Mvva 92,224,32
LD 4,40,68,0
Mvaa 96,248,216
s_nop 0
CV 104,104,105
CV 105,106,107
Mvaa 96,252,236
s_nop 0
CV 106,84,85
CV 107,86,87
Mvva 96,220,16
s_nop 0
CV 108,108,109
CV 109,110,111
Mvva 96,224,36
WL 4
LD 4,40,69,1024
Zvav 140,76,120
s_nop 0
CV 110,88,89
CV 111,90,91
Mvav 140,72,180
WL 0
CV 112,112,113
CV 113,114,115
Mvaa 140,240,80
CV 114,92,93
CV 115,94,95
Mvaa 140,244,120
LD 4,40,70,2048
Zvav 144,76,124
CV 116,116,117
CV 117,118,119
Mvav 144,72,184
s_nop 0
Ta 248,14,49152
Ta 250,14,51200
Mvaa 144,240,84
s_nop 0
Ta 252,14,53248
Ta 254,14,55296
Mvaa 144,244,124
s_nop 0
Tv 220,14,57344
Tv 222,14,59392
Zvav 148,76,128
s_nop 0
Tv 224,14,61440
Tv 226,14,63488
Mvav 148,72,188
s_waitcnt vmcnt(6)
s_add_u32 s80,%0,5
s_mov_b32 s81,0x40000
1:
v_subrev_u32 v9,s80,v0
v_cmp_eq_u32 s[82:83],%1,v1
v_cmp_ge_u32 vcc,10,v9
s_nop 1
s_and_b64 vcc,vcc,s[82:83]
s_cmp_eq_u64 vcc,-1
s_cbranch_scc1 2f
s_sleep 1
buffer_load_dwordx2 v[0:1],v7,s[24:27],0 offen offset:128 sc1
s_sub_u32 s81,s81,1
s_waitcnt vmcnt(0)
s_cmp_lg_u32 s81,0
s_cbranch_scc1 1b
2:
Mvaa 148,240,88
LD 4,40,71,3072
Mvaa 148,244,128
s_add_u32 m0,s78,69632
s_add_u32 s30,s49,0
CV 118,96,97
Zvav 152,76,132
CV 119,98,99
Mvav 152,72,192
Mvaa 152,240,92
LD 4,40,72,0
Mvaa 152,244,132
Zvav 156,76,136
Mvav 156,72,196
Mvaa 156,240,96
LD 4,40,73,1024
Mvaa 156,244,136
WL 0
Zvav 160,248,120
Mvav 160,252,180
Mvva 160,220,80
LD 4,40,74,2048
Mvva 160,224,120
Zvav 164,248,124
Mvav 164,252,184
s_nop 0
Ta 76,13,32768
Ta 78,13,34816
Mvva 164,220,84
s_nop 0
Ta 72,13,36864
Ta 74,13,38912
Mvva 164,224,124
s_nop 0
Ta 240,13,40960
Ta 242,13,43008
Zvav 168,248,128
s_nop 0
Ta 244,13,45056
Ta 246,13,47104
Mvav 168,252,188
LD 4,40,75,3072
Mvva 168,220,88
Mvva 168,224,128
Zvav 172,248,132
Mvav 172,252,192
buffer_load_dwordx4 a[40:43],v5,s[36:39],s30 offen sc1
Mvva 172,220,92
s_add_u32 s30,s49,184320
Mvva 172,224,132
Zvav 176,248,136
Mvav 176,252,196
buffer_load_dwordx4 a[44:47],v5,s[36:39],s30 offen sc1
Mvva 176,220,96
s_add_u32 s30,s49,368640
Mvva 176,224,136
WL 0
Mvaa 140,76,100
Mvaa 140,72,140
buffer_load_dwordx4 a[48:51],v5,s[36:39],s30 offen sc1
Mvaa 140,240,160
s_add_u32 s30,s49,552960
Mvaa 140,244,180
Mvaa 144,76,104
Mvaa 144,72,144
s_nop 0
Ta 248,14,32768
Ta 250,14,34816
Mvaa 144,240,164
s_nop 0
Ta 252,14,36864
Ta 254,14,38912
Mvaa 144,244,184
s_nop 0
Tv 220,14,40960
Tv 222,14,43008
Mvaa 148,76,108
s_nop 0
Tv 224,14,45056
Tv 226,14,47104
Mvaa 148,72,148
buffer_load_dwordx4 a[52:55],v5,s[36:39],s30 offen sc1
Mvaa 148,240,168
s_add_u32 s30,s49,737280
Mvaa 148,244,188
Mvaa 152,76,112
Mvaa 152,72,152
buffer_load_dwordx4 a[56:59],v5,s[36:39],s30 offen sc1
Mvaa 152,240,172
s_add_u32 s30,s49,921600
Mvaa 152,244,192
Mvaa 156,76,116
Mvaa 156,72,156
buffer_load_dwordx4 a[60:63],v5,s[36:39],s30 offen sc1
Mvaa 156,240,176
s_add_u32 s30,s49,1105920
Mvaa 156,244,196
WL 0
Mvaa 160,248,100
Mvaa 160,252,140
buffer_load_dwordx4 a[64:67],v5,s[36:39],s30 offen sc1
Mvva 160,220,160
s_add_u32 s30,s49,1290240
Mvva 160,224,180
Mvaa 164,248,104
Mvaa 164,252,144
buffer_load_dwordx4 a[68:71],v5,s[36:39],s30 offen sc1
Mvva 164,220,164
Mvva 164,224,184
Mvaa 168,248,108
Mvaa 168,252,148
s_waitcnt vmcnt(16)
s_barrier
Mvva 168,220,168
s_add_u32 m0,s78,32768
Ta 76,13,16384
Ta 78,13,18432
Mvva 168,224,188
s_nop 0
Ta 72,13,20480
Ta 74,13,22528
Mvaa 172,248,112
s_nop 0
Ta 240,13,24576
Ta 242,13,26624
Mvaa 172,252,152
buffer_store_dwordx4 v[100:103],v5,s[44:47],s79 offen offset:0 sc1
buffer_store_dwordx4 v[104:107],v5,s[44:47],s79 offen offset:1024 sc1
buffer_store_dwordx4 v[108:111],v5,s[44:47],s79 offen offset:2048 sc1
buffer_store_dwordx4 v[112:115],v5,s[44:47],s79 offen offset:3072 sc1
buffer_store_dwordx4 v[116:119],v5,s[44:47],s84 offen offset:0 sc1
s_add_u32 s79,s79,20480
s_add_u32 s84,s84,20480
Mvva 172,220,172
s_nop 0
Ta 244,13,28672
Ta 246,13,30720
Mvva 172,224,192
s_nop 0
v_add_u32 v4,294912,v4
Mvaa 176,248,116
Mvaa 176,252,156
s_nop 0
Mvva 176,220,176
Mvva 176,224,196
WL 4
Mvav 140,76,20
s_nop 0
Mvav 140,72,40
WL 2
Mvav 140,240,60
WL 0
Mvav 140,244,200
s_nop 0
Mvav 144,76,24
Mvav 144,72,44
s_nop 0
Ta 248,14,16384
Ta 250,14,18432
Mvav 144,240,64
s_nop 0
Ta 252,14,20480
Ta 254,14,22528
Mvav 144,244,204
s_nop 0
Tv 220,14,24576
Tv 222,14,26624
Mvav 148,76,28
s_nop 0
Tv 224,14,28672
Tv 226,14,30720
Mvav 148,72,48
s_nop 0
Mvav 148,240,68
Mvav 148,244,208
Mvav 152,76,32
s_nop 0
Mvav 152,72,52
LD 4,40,68,0
Mvav 152,240,72
Mvav 152,244,212
Mvav 156,76,36
Mvav 156,72,56
LD 4,40,69,1024
Mvav 156,240,76
Mvav 156,244,216
WL 0
Mvav 160,248,20
Mvav 160,252,40
LD 4,40,70,2048
Mvvv 160,220,60
Mvvv 160,224,200
Mvav 164,248,24
Mvav 164,252,44
s_nop 0
Ta 76,13,0
Ta 78,13,2048
Mvvv 164,220,64
s_nop 0
Ta 72,13,4096
Ta 74,13,6144
Mvvv 164,224,204
s_nop 0
Ta 240,13,8192
Ta 242,13,10240
Mvav 168,248,28
s_nop 0
Ta 244,13,12288
Ta 246,13,14336
Mvav 168,252,48
LD 4,40,71,3072
Mvvv 168,220,68
s_add_u32 m0,s78,36864
Mvvv 168,224,208
Mvav 172,248,32
Mvav 172,252,52
LD 4,40,72,0
Mvvv 172,220,72
Mvvv 172,224,212
Mvav 176,248,36
Mvav 176,252,56
LD 4,40,73,1024
Mvvv 176,220,76
Mvvv 176,224,216
WL 0
Mvaa 140,76,200
Mvaa 140,72,220
LD 4,40,74,2048
Mvaa 140,240,0
Mvaa 140,244,20
Mvaa 144,76,204
Mvaa 144,72,224
s_nop 0
Ta 248,14,0
Ta 250,14,2048
Mvaa 144,240,4
s_nop 0
Ta 252,14,4096
Ta 254,14,6144
Mvaa 144,244,24
s_nop 0
Tv 220,14,8192
Tv 222,14,10240
Mvaa 148,76,208
s_nop 0
Tv 224,14,12288
Tv 226,14,14336
Mvaa 148,72,228
LD 4,40,75,3072
Mvaa 148,240,8
Mvaa 148,244,28
Mvaa 152,76,212
Mvaa 152,72,232
Mvaa 152,240,12
Mvaa 152,244,32
Mvaa 156,76,216
Mvaa 156,72,236
Mvaa 156,240,16
Mvaa 156,244,36
WL 0
Mvaa 160,248,200
Mvaa 160,252,220
Mvva 160,220,0
Mvva 160,224,20
Mvaa 164,248,204
Mvaa 164,252,224
Mvva 164,220,4
Mvva 164,224,24
CV 140,140,141
CV 141,142,143
Mvaa 168,248,208
s_nop 0
CV 142,160,161
CV 143,162,163
Mvaa 168,252,228
s_waitcnt vmcnt(21)
s_barrier
Mvva 168,220,8
s_nop 0
Ta 76,253,16384
Ta 78,253,18432
Mvva 168,224,28
s_nop 0
Ta 72,253,20480
Ta 74,253,22528
Mvaa 172,248,212
s_nop 0
Ta 240,253,24576
Ta 242,253,26624
Mvaa 172,252,232
s_nop 0
Ta 244,253,28672
Ta 246,253,30720
Mvva 172,220,12
buffer_store_dwordx4 v[140:143],v5,s[44:47],s79 offen offset:0 sc1
Mvva 172,224,32
CV 144,144,145
CV 145,146,147
Mvaa 176,248,216
CV 146,164,165
CV 147,166,167
Mvaa 176,252,236
CV 148,148,149
CV 149,150,151
Mvva 176,220,16
CV 150,168,169
CV 151,170,171
Mvva 176,224,36
WL 4
buffer_store_dwordx4 v[144:147],v5,s[44:47],s79 offen offset:1024 sc1
Zvav 100,76,120
CV 152,152,153
CV 153,154,155
Mvav 100,72,180
WL 0
CV 154,172,173
CV 155,174,175
Mvaa 100,240,80
Mvaa 100,244,120
CV 156,156,157
CV 157,158,159
Zvav 104,76,124
buffer_store_dwordx4 v[148:151],v5,s[44:47],s79 offen offset:2048 sc1
Mvav 104,72,184
s_nop 0
Ta 248,254,16384
Ta 250,254,18432
Mvaa 104,240,84
s_nop 0
Ta 252,254,20480
Ta 254,254,22528
Mvaa 104,244,124
s_nop 0
Tv 220,254,24576
Tv 222,254,26624
Zvav 108,76,128
s_nop 0
Tv 224,254,28672
Tv 226,254,30720
Mvav 108,72,188
buffer_store_dwordx4 v[152:155],v5,s[44:47],s79 offen offset:3072 sc1
Mvaa 108,240,88
CV 158,176,177
CV 159,178,179
Mvaa 108,244,128
Zvav 112,76,132
Mvav 112,72,192
Mvaa 112,240,92
buffer_store_dwordx4 v[156:159],v5,s[44:47],s84 offen offset:0 sc1
Mvaa 112,244,132
s_add_u32 s79,s79,20480
s_add_u32 s84,s84,20480
Zvav 116,76,136
Mvav 116,72,196
Mvaa 116,240,96
Mvaa 116,244,136
WL 0
Zvav 80,248,120
Mvav 80,252,180
Mvva 80,220,80
Mvva 80,224,120
Zvav 84,248,124
Mvav 84,252,184
Mvva 84,220,84
Mvva 84,224,124
Zvav 88,248,128
Mvav 88,252,188
Mvva 88,220,88
Mvva 88,224,128
Zvav 92,248,132
Mvav 92,252,192
Mvva 92,220,92
Mvva 92,224,132
Zvav 96,248,136
Mvav 96,252,196
Mvva 96,220,96
Mvva 96,224,136
s_waitcnt vmcnt(25)
Zvva 236,230,40
Zvva 240,228,40
s_nop 0
Ta 76,253,0
Ta 78,253,2048
s_waitcnt vmcnt(24)
Mvva 236,230,44
s_nop 0
Ta 72,253,4096
Ta 74,253,6144
Mvva 240,228,44
s_nop 0
Ta 240,253,8192
Ta 242,253,10240
s_waitcnt vmcnt(23)
Mvva 236,230,48
s_nop 0
Ta 244,253,12288
Ta 246,253,14336
Mvva 240,228,48
s_waitcnt vmcnt(22)
Mvva 236,230,52
Mvva 240,228,52
s_waitcnt vmcnt(21)
Mvva 236,230,56
Mvva 240,228,56
s_waitcnt vmcnt(20)
Mvva 236,230,60
Mvva 240,228,60
s_waitcnt vmcnt(19)
Mvva 236,230,64
Mvva 240,228,64
s_waitcnt vmcnt(18)
Mvva 236,230,68
Mvva 240,228,68
WL 0
s_add_u32 s30,s50,0
Mvaa 100,76,100
buffer_load_dwordx4 a[40:43],v5,s[36:39],s30 offen sc1
Mvaa 100,72,140
s_add_u32 s30,s50,184320
Mvaa 100,240,160
Mvaa 100,244,180
v_cmp_class_f32 vcc,v236,s67
s_nop 1
s_cmp_eq_u64 vcc,0
s_cbranch_scc1 3f
s_lshl_b32 s80,s61,2
s_add_u32 s80,s80,%9
s_add_u32 s80,s80,159744
v_bfe_u32 v9,v5,4,4
v_lshl_add_u32 v9,v9,2,s80
v_mov_b32 v0,1
s_mov_b64 exec,vcc
ds_write_b32 v9,v0
s_add_u32 s80,%9,160384
v_mov_b32 v9,s80
ds_write_b32 v9,v0
s_mov_b64 exec,-1
s_waitcnt lgkmcnt(0)
s_nop 4
3:
Mvaa 104,76,104
buffer_load_dwordx4 a[44:47],v5,s[36:39],s30 offen sc1
Mvaa 104,72,144
s_add_u32 s30,s50,368640
Ta 248,254,0
Ta 250,254,2048
Mvaa 104,240,164
s_nop 0
Ta 252,254,4096
Ta 254,254,6144
Mvaa 104,244,184
s_nop 0
Tv 220,254,8192
Tv 222,254,10240
Mvaa 108,76,108
s_nop 0
Tv 224,254,12288
Tv 226,254,14336
Mvaa 108,72,148
buffer_load_dwordx4 a[48:51],v5,s[36:39],s30 offen sc1
Mvaa 108,240,168
s_add_u32 s30,s50,552960
CV 236,236,237
CV 237,238,239
Mvaa 108,244,188
CV 238,240,241
CV 239,242,243
Mvaa 112,76,112
v_permlane32_swap_b32 v236,v238
Mvaa 112,72,152
buffer_load_dwordx4 a[52:55],v5,s[36:39],s30 offen sc1
Mvaa 112,240,172
s_add_u32 s30,s50,737280
v_permlane32_swap_b32 v237,v239
Mvaa 112,244,192
v_permlane16_swap_b32 v236,v238
Mvaa 116,76,116
v_permlane16_swap_b32 v237,v239
Mvaa 116,72,156
buffer_load_dwordx4 a[56:59],v5,s[36:39],s30 offen sc1
Mvaa 116,240,176
s_add_u32 s30,s50,921600
Mvaa 116,244,196
WL 0
Mvaa 80,248,100
Mvaa 80,252,140
buffer_load_dwordx4 a[60:63],v5,s[36:39],s30 offen sc1
Mvva 80,220,160
s_add_u32 s30,s50,1105920
Mvva 80,224,180
Mvaa 84,248,104
Mvaa 84,252,144
buffer_load_dwordx4 a[64:67],v5,s[36:39],s30 offen sc1
Mvva 84,220,164
s_add_u32 s30,s50,1290240
Mvva 84,224,184
Mvaa 88,248,108
Mvaa 88,252,148
s_waitcnt vmcnt(12)
s_barrier
Mvva 88,220,168
s_nop 0
Ta 76,13,49152
Ta 78,13,51200
Mvva 88,224,188
s_nop 0
Ta 72,13,53248
Ta 74,13,55296
Mvaa 92,248,112
s_nop 0
Ta 240,13,57344
Ta 242,13,59392
Mvaa 92,252,152
s_nop 0
Ta 244,13,61440
Ta 246,13,63488
Mvva 92,220,172
buffer_load_dwordx4 a[68:71],v5,s[36:39],s30 offen sc1
Mvva 92,224,192
Mvaa 96,248,116
Mvaa 96,252,156
Mvva 96,220,176
buffer_store_dwordx4 v[236:239],v6,s[96:99],s55 offen
Mvva 96,224,196
WL 4
Mvav 100,76,20
Mvav 100,72,40
WL 0
Mvav 100,240,60
Mvav 100,244,200
Mvav 104,76,24
Mvav 104,72,44
s_nop 0
Ta 248,14,49152
Ta 250,14,51200
Mvav 104,240,64
s_nop 0
Ta 252,14,53248
Ta 254,14,55296
Mvav 104,244,204
s_nop 0
Tv 220,14,57344
Tv 222,14,59392
Mvav 108,76,28
s_nop 0
Tv 224,14,61440
Tv 226,14,63488
Mvav 108,72,48
Mvav 108,240,68
Mvav 108,244,208
Mvav 112,76,32
Mvav 112,72,52
Mvav 112,240,72
Mvav 112,244,212
Mvav 116,76,36
Mvav 116,72,56
Mvav 116,240,76
Mvav 116,244,216
WL 0
Mvav 80,248,20
Mvav 80,252,40
Mvvv 80,220,60
Mvvv 80,224,200
Mvav 84,248,24
Mvav 84,252,44
s_nop 0
Ta 76,13,32768
Ta 78,13,34816
Mvvv 84,220,64
s_nop 0
Ta 72,13,36864
Ta 74,13,38912
Mvvv 84,224,204
s_nop 0
Ta 240,13,40960
Ta 242,13,43008
Mvav 88,248,28
s_nop 0
Ta 244,13,45056
Ta 246,13,47104
Mvav 88,252,48
Mvvv 88,220,68
Mvvv 88,224,208
Mvav 92,248,32
Mvav 92,252,52
Mvvv 92,220,72
Mvvv 92,224,212
Mvav 96,248,36
Mvav 96,252,56
Mvvv 96,220,76
Mvvv 96,224,216
WL 0
Mvaa 100,76,200
Mvaa 100,72,220
Mvaa 100,240,0
Mvaa 100,244,20
Mvaa 104,76,204
Mvaa 104,72,224
s_nop 0
Ta 248,14,32768
Ta 250,14,34816
Mvaa 104,240,4
s_nop 0
Ta 252,14,36864
Ta 254,14,38912
Mvaa 104,244,24
s_nop 0
Tv 220,14,40960
Tv 222,14,43008
Mvaa 108,76,208
s_nop 0
Tv 224,14,45056
Tv 226,14,47104
Mvaa 108,72,228
Mvaa 108,240,8
Mvaa 108,244,28
Mvaa 112,76,212
Mvaa 112,72,232
Mvaa 112,240,12
Mvaa 112,244,32
Mvaa 116,76,216
Mvaa 116,72,236
)ASM"
R"ASM(Mvaa 116,240,16
Mvaa 116,244,36
WL 0
Mvaa 80,248,200
Mvaa 80,252,220
Mvva 80,220,0
Mvva 80,224,20
Mvaa 84,248,204
Mvaa 84,252,224
Mvva 84,220,4
Mvva 84,224,24
CV 100,100,101
CV 101,102,103
Mvaa 88,248,208
s_nop 0
CV 102,80,81
CV 103,82,83
Mvaa 88,252,228
s_barrier
Mvva 88,220,8
buffer_store_dwordx4 v[100:103],v5,s[44:47],s79 offen offset:0 sc1
Mvva 88,224,28
CV 104,104,105
CV 105,106,107
Mvaa 92,248,212
CV 106,84,85
CV 107,86,87
Mvaa 92,252,232
Mvva 92,220,12
Mvva 92,224,32
buffer_store_dwordx4 v[104:107],v5,s[44:47],s79 offen offset:1024 sc1
Mvaa 96,248,216
CV 108,108,109
CV 109,110,111
Mvaa 96,252,236
CV 110,88,89
CV 111,90,91
Mvva 96,220,16
Mvva 96,224,36
buffer_store_dwordx4 v[108:111],v5,s[44:47],s79 offen offset:2048 sc1
CV 112,112,113
CV 113,114,115
CV 114,92,93
CV 115,94,95
buffer_store_dwordx4 v[112:115],v5,s[44:47],s79 offen offset:3072 sc1
CV 116,116,117
CV 117,118,119
CV 118,96,97
CV 119,98,99
buffer_store_dwordx4 v[116:119],v5,s[44:47],s84 offen offset:0 sc1
s_add_u32 s79,s79,20480
s_add_u32 s84,s84,20480
s_waitcnt vmcnt(0)
s_barrier
buffer_load_dwordx2 v[0:1],v7,s[24:27],0 offen offset:128 sc1
Zvva 236,230,40
s_nop 0
Zvva 240,228,40
Mvva 236,230,44
Mvva 240,228,44
s_nop 0
Mvva 236,230,48
Mvva 240,228,48
Mvva 236,230,52
s_nop 0
Mvva 240,228,52
Mvva 236,230,56
Mvva 240,228,56
s_nop 0
Mvva 236,230,60
Mvva 240,228,60
Mvva 236,230,64
s_nop 0
Mvva 240,228,64
Mvva 236,230,68
Mvva 240,228,68
s_waitcnt vmcnt(0)
s_add_u32 s80,%0,9
s_mov_b32 s81,0x40000
1:
v_subrev_u32 v9,s80,v0
v_cmp_eq_u32 s[82:83],%1,v1
v_cmp_ge_u32 vcc,6,v9
s_nop 1
s_and_b64 vcc,vcc,s[82:83]
s_cmp_eq_u64 vcc,-1
s_cbranch_scc1 2f
s_sleep 1
buffer_load_dwordx2 v[0:1],v7,s[24:27],0 offen offset:128 sc1
s_sub_u32 s81,s81,1
s_waitcnt vmcnt(0)
s_cmp_lg_u32 s81,0
s_cbranch_scc1 1b
2:
s_add_u32 s30,s51,0
buffer_load_dwordx4 a[80:83],v5,s[36:39],s30 offen sc1
s_add_u32 s30,s51,184320
buffer_load_dwordx4 a[84:87],v5,s[36:39],s30 offen sc1
s_add_u32 s30,s51,368640
buffer_load_dwordx4 a[88:91],v5,s[36:39],s30 offen sc1
s_add_u32 s30,s51,552960
buffer_load_dwordx4 a[92:95],v5,s[36:39],s30 offen sc1
s_add_u32 s30,s51,737280
buffer_load_dwordx4 a[96:99],v5,s[36:39],s30 offen sc1
s_add_u32 s30,s51,921600
buffer_load_dwordx4 a[100:103],v5,s[36:39],s30 offen sc1
s_add_u32 s30,s51,1105920
buffer_load_dwordx4 a[104:107],v5,s[36:39],s30 offen sc1
s_add_u32 s30,s51,1290240
buffer_load_dwordx4 a[108:111],v5,s[36:39],s30 offen sc1
s_add_u32 s30,s52,0
buffer_load_dwordx4 a[112:115],v5,s[36:39],s30 offen sc1
s_add_u32 s30,s52,184320
buffer_load_dwordx4 a[116:119],v5,s[36:39],s30 offen sc1
s_add_u32 s30,s52,368640
buffer_load_dwordx4 a[120:123],v5,s[36:39],s30 offen sc1
s_add_u32 s30,s52,552960
buffer_load_dwordx4 a[124:127],v5,s[36:39],s30 offen sc1
s_add_u32 s30,s52,737280
buffer_load_dwordx4 a[128:131],v5,s[36:39],s30 offen sc1
s_add_u32 s30,s52,921600
buffer_load_dwordx4 a[132:135],v5,s[36:39],s30 offen sc1
s_add_u32 s30,s52,1105920
buffer_load_dwordx4 a[136:139],v5,s[36:39],s30 offen sc1
s_add_u32 s30,s52,1290240
buffer_load_dwordx4 a[140:143],v5,s[36:39],s30 offen sc1
s_add_u32 s30,s53,0
buffer_load_dwordx4 a[144:147],v5,s[36:39],s30 offen sc1
s_add_u32 s30,s53,184320
buffer_load_dwordx4 a[148:151],v5,s[36:39],s30 offen sc1
s_add_u32 s30,s53,368640
buffer_load_dwordx4 a[152:155],v5,s[36:39],s30 offen sc1
s_add_u32 s30,s53,552960
buffer_load_dwordx4 a[156:159],v5,s[36:39],s30 offen sc1
s_add_u32 s30,s53,737280
buffer_load_dwordx4 a[160:163],v5,s[36:39],s30 offen sc1
s_add_u32 s30,s53,921600
buffer_load_dwordx4 a[164:167],v5,s[36:39],s30 offen sc1
s_add_u32 s30,s53,1105920
buffer_load_dwordx4 a[168:171],v5,s[36:39],s30 offen sc1
s_add_u32 s30,s53,1290240
buffer_load_dwordx4 a[172:175],v5,s[36:39],s30 offen sc1
v_cmp_class_f32 vcc,v236,s67
s_nop 1
s_cmp_eq_u64 vcc,0
s_cbranch_scc1 3f
s_lshl_b32 s80,s62,2
s_add_u32 s80,s80,%9
s_add_u32 s80,s80,159744
v_bfe_u32 v9,v5,4,4
v_lshl_add_u32 v9,v9,2,s80
v_mov_b32 v0,1
s_mov_b64 exec,vcc
ds_write_b32 v9,v0
s_add_u32 s80,%9,160384
v_mov_b32 v9,s80
ds_write_b32 v9,v0
s_mov_b64 exec,-1
s_waitcnt lgkmcnt(0)
s_nop 4
3:
CV 236,236,237
CV 237,238,239
CV 238,240,241
CV 239,242,243
s_nop 0
v_permlane32_swap_b32 v236,v238
v_permlane32_swap_b32 v237,v239
s_nop 0
v_permlane16_swap_b32 v236,v238
v_permlane16_swap_b32 v237,v239
buffer_store_dwordx4 v[236:239],v6,s[96:99],s56 offen
s_waitcnt vmcnt(24)
Zvva 20,230,80
s_nop 0
Zvva 24,228,80
s_waitcnt vmcnt(23)
Mvva 20,230,84
s_nop 0
Mvva 24,228,84
s_waitcnt vmcnt(22)
Mvva 20,230,88
s_nop 0
Mvva 24,228,88
s_waitcnt vmcnt(21)
Mvva 20,230,92
s_nop 0
Mvva 24,228,92
s_waitcnt vmcnt(20)
Mvva 20,230,96
s_nop 0
Mvva 24,228,96
s_waitcnt vmcnt(19)
Mvva 20,230,100
s_nop 0
Mvva 24,228,100
s_waitcnt vmcnt(18)
Mvva 20,230,104
Mvva 24,228,104
s_waitcnt vmcnt(17)
Mvva 20,230,108
Mvva 24,228,108
s_waitcnt vmcnt(16)
Zvva 28,230,112
Zvva 32,228,112
s_waitcnt vmcnt(15)
Mvva 28,230,116
Mvva 32,228,116
s_nop 0
v_cmp_class_f32 vcc,v20,s67
s_nop 1
s_cmp_eq_u64 vcc,0
s_cbranch_scc1 3f
s_lshl_b32 s80,s63,2
s_add_u32 s80,s80,%9
s_add_u32 s80,s80,159744
v_bfe_u32 v9,v5,4,4
v_lshl_add_u32 v9,v9,2,s80
v_mov_b32 v0,1
s_mov_b64 exec,vcc
ds_write_b32 v9,v0
s_add_u32 s80,%9,160384
v_mov_b32 v9,s80
ds_write_b32 v9,v0
s_mov_b64 exec,-1
s_waitcnt lgkmcnt(0)
s_nop 4
3:
s_waitcnt vmcnt(14)
Mvva 28,230,120
CV 20,20,21
CV 21,22,23
Mvva 32,228,120
CV 22,24,25
CV 23,26,27
s_waitcnt vmcnt(13)
Mvva 28,230,124
v_permlane32_swap_b32 v20,v22
Mvva 32,228,124
v_permlane32_swap_b32 v21,v23
s_waitcnt vmcnt(12)
Mvva 28,230,128
v_permlane16_swap_b32 v20,v22
Mvva 32,228,128
v_permlane16_swap_b32 v21,v23
s_waitcnt vmcnt(11)
Mvva 28,230,132
buffer_store_dwordx4 v[20:23],v6,s[96:99],s57 offen
Mvva 32,228,132
s_waitcnt vmcnt(11)
Mvva 28,230,136
Mvva 32,228,136
s_waitcnt vmcnt(10)
Mvva 28,230,140
Mvva 32,228,140
s_waitcnt vmcnt(9)
Zvva 36,230,144
Zvva 40,228,144
s_waitcnt vmcnt(8)
Mvva 36,230,148
Mvva 40,228,148
s_nop 0
v_cmp_class_f32 vcc,v28,s67
s_nop 1
s_cmp_eq_u64 vcc,0
s_cbranch_scc1 3f
s_lshl_b32 s80,s64,2
s_add_u32 s80,s80,%9
s_add_u32 s80,s80,159744
v_bfe_u32 v9,v5,4,4
v_lshl_add_u32 v9,v9,2,s80
v_mov_b32 v0,1
s_mov_b64 exec,vcc
ds_write_b32 v9,v0
s_add_u32 s80,%9,160384
v_mov_b32 v9,s80
ds_write_b32 v9,v0
s_mov_b64 exec,-1
s_waitcnt lgkmcnt(0)
s_nop 4
3:
s_waitcnt vmcnt(7)
Mvva 36,230,152
CV 28,28,29
CV 29,30,31
Mvva 40,228,152
CV 30,32,33
CV 31,34,35
s_waitcnt vmcnt(6)
Mvva 36,230,156
v_permlane32_swap_b32 v28,v30
Mvva 40,228,156
v_permlane32_swap_b32 v29,v31
s_waitcnt vmcnt(5)
Mvva 36,230,160
v_permlane16_swap_b32 v28,v30
Mvva 40,228,160
v_permlane16_swap_b32 v29,v31
s_waitcnt vmcnt(4)
Mvva 36,230,164
buffer_store_dwordx4 v[28:31],v6,s[96:99],s58 offen
Mvva 40,228,164
s_waitcnt vmcnt(4)
Mvva 36,230,168
Mvva 40,228,168
s_waitcnt vmcnt(3)
Mvva 36,230,172
Mvva 40,228,172
s_nop 6
v_cmp_class_f32 vcc,v36,s67
s_nop 1
s_cmp_eq_u64 vcc,0
s_cbranch_scc1 3f
s_lshl_b32 s80,s65,2
s_add_u32 s80,s80,%9
s_add_u32 s80,s80,159744
v_bfe_u32 v9,v5,4,4
v_lshl_add_u32 v9,v9,2,s80
v_mov_b32 v0,1
s_mov_b64 exec,vcc
ds_write_b32 v9,v0
s_add_u32 s80,%9,160384
v_mov_b32 v9,s80
ds_write_b32 v9,v0
s_mov_b64 exec,-1
s_waitcnt lgkmcnt(0)
s_nop 4
3:
CV 36,36,37
CV 37,38,39
CV 38,40,41
CV 39,42,43
s_nop 0
v_permlane32_swap_b32 v36,v38
v_permlane32_swap_b32 v37,v39
s_nop 0
v_permlane16_swap_b32 v36,v38
v_permlane16_swap_b32 v37,v39
buffer_store_dwordx4 v[36:39],v6,s[96:99],s59 offen
s_add_u32 s80,%9,160384
v_mov_b32 v9,s80
s_barrier
ds_read_b32 v9,v9
s_waitcnt lgkmcnt(0)
v_readfirstlane_b32 s80,v9
s_nop 3
s_cmp_lg_u32 s80,0
s_cbranch_scc1 4f
s_endpgm
4:
.Lmha_end:
.purgem LD
.purgem Ta
.purgem Rv
.purgem WL
.purgem Zvav
.purgem Tv
.purgem Zvvv
.purgem Mvav
.purgem Mvvv
.purgem LN
.purgem CV
.purgem AW
.purgem Ra
.purgem Zvva
.purgem Mvva
.purgem Mvaa
.purgem Nvaa
.purgem EX
.purgem Zavv
.purgem Zaav
.purgem Mavv
.purgem Maav
.purgem Nvva
.purgem Nvav
.purgem Nvvv
.purgem AR
)ASM"
        : "=&s"(e0), "=&s"(e1)
        : "s"(xp), "s"(wqp), "s"(kp), "s"(vp), "s"(wop), "s"(pp), "s"(wave), "s"(ldsb), "s"(sy), "s"(sy), "s"(b | (G << 8) | (rk << 11) | (idx << 16) | ((unsigned)(9 - rq) << 24) | ((unsigned)(9 - ro) << 28)), "s"(op)
        : "v0","v1","v2","v3","v4","v5","v6","v7","v8","v9","v10","v11","v12","v13","v14","v15","v16","v17","v18","v19","v20","v21","v22","v23","v24","v25","v26","v27","v28","v29","v30","v31","v32","v33","v34","v35","v36","v37","v38","v39","v40","v41","v42","v43","v44","v45","v46","v47","v48","v49","v50","v51","v52","v53","v54","v55","v56","v57","v58","v59","v60","v61","v62","v63","v64","v65","v66","v67","v68","v69","v70","v71","v72","v73","v74","v75","v76","v77","v78","v79","v80","v81","v82","v83","v84","v85","v86","v87","v88","v89","v90","v91","v92","v93","v94","v95","v96","v97","v98","v99","v100","v101","v102","v103","v104","v105","v106","v107","v108","v109","v110","v111","v112","v113","v114","v115","v116","v117","v118","v119","v120","v121","v122","v123","v124","v125","v126","v127","v128","v129","v130","v131","v132","v133","v134","v135","v136","v137","v138","v139","v140","v141","v142","v143","v144","v145","v146","v147","v148","v149","v150","v151","v152","v153","v154","v155","v156","v157","v158","v159","v160","v161","v162","v163","v164","v165","v166","v167","v168","v169","v170","v171","v172","v173","v174","v175","v176","v177","v178","v179","v180","v181","v182","v183","v184","v185","v186","v187","v188","v189","v190","v191","v192","v193","v194","v195","v196","v197","v198","v199","v200","v201","v202","v203","v204","v205","v206","v207","v208","v209","v210","v211","v212","v213","v214","v215","v216","v217","v218","v219","v220","v221","v222","v223","v224","v225","v226","v227","v228","v229","v230","v231","v232","v233","v234","v235","v236","v237","v238","v239","v240","v241","v242","v243","v244","v245","v246","v247","v248","v249","v250","v251","v252","v253","v254","v255","a0","a1","a2","a3","a4","a5","a6","a7","a8","a9","a10","a11","a12","a13","a14","a15","a16","a17","a18","a19","a20","a21","a22","a23","a24","a25","a26","a27","a28","a29","a30","a31","a32","a33","a34","a35","a36","a37","a38","a39","a40","a41","a42","a43","a44","a45","a46","a47","a48","a49","a50","a51","a52","a53","a54","a55","a56","a57","a58","a59","a60","a61","a62","a63","a64","a65","a66","a67","a68","a69","a70","a71","a72","a73","a74","a75","a76","a77","a78","a79","a80","a81","a82","a83","a84","a85","a86","a87","a88","a89","a90","a91","a92","a93","a94","a95","a96","a97","a98","a99","a100","a101","a102","a103","a104","a105","a106","a107","a108","a109","a110","a111","a112","a113","a114","a115","a116","a117","a118","a119","a120","a121","a122","a123","a124","a125","a126","a127","a128","a129","a130","a131","a132","a133","a134","a135","a136","a137","a138","a139","a140","a141","a142","a143","a144","a145","a146","a147","a148","a149","a150","a151","a152","a153","a154","a155","a156","a157","a158","a159","a160","a161","a162","a163","a164","a165","a166","a167","a168","a169","a170","a171","a172","a173","a174","a175","a176","a177","a178","a179","a180","a181","a182","a183","a184","a185","a186","a187","a188","a189","a190","a191","a192","a193","a194","a195","a196","a197","a198","a199","a200","a201","a202","a203","a204","a205","a206","a207","a208","a209","a210","a211","a212","a213","a214","a215","a216","a217","a218","a219","a220","a221","a222","a223","a224","a225","a226","a227","a228","a229","a230","a231","a232","a233","a234","a235","a236","a237","a238","a239","a240","a241","a242","a243","a244","a245","a246","a247","a248","a249","a250","a251","a252","a253","a254","a255","s24","s25","s26","s27","s28","s29","s30","s31","s36","s37","s38","s39","s40","s41","s42","s43","s44","s45","s46","s47","s48","s49","s50","s51","s52","s53","s54","s55","s56","s57","s58","s59","s60","s61","s62","s63","s64","s65","s66","s67","s68","s69","s70","s71","s72","s73","s74","s75","s76","s77","s78","s79","s80","s81","s82","s83","s84","s85","s86","s87","s88","s89","s90","s91","s92","s93","s94","s95","s96","s97","s98","s99","vcc","scc","memory");
  }
  asm volatile("" : "+s"(ka), "+s"(bid), "+s"(wave));
  x = (const char*)ka[0]; kc = (const char*)ka[1]; vc = (const char*)ka[2]; wq = (const char*)ka[3]; wo = (const char*)ka[4];
  ws = (char*)ka[5]; out = (u16*)ka[6];
  const int b = 8 * ((bid & 7) >> 1) + ((bid >> 3) & 7), G = 4 * (bid & 1) + (bid >> 6);
  unsigned t;
  asm volatile("v_mbcnt_lo_u32_b32 %0, -1, 0\n\tv_mbcnt_hi_u32_b32 %0, -1, %0" : "=v"(t));
  const unsigned lane = t;
  const unsigned wv = wave;
  t += wv * 64;
  const int* bad = (const int*)(lds + 159744);
  __syncthreads();
  (void)e0; (void)e1;
  asm volatile("s_waitcnt vmcnt(0)" ::: "memory");
  const u16* xg = (const u16*)x; const u16* kg = (const u16*)kc; const u16* vg = (const u16*)vc;
  const u16* wqg = (const u16*)wq; const u16* wog = (const u16*)wo;
  float* xs = (float*)(lds + 256); float* qv = xs + 576; float* sc = qv + 128; float* ov = sc + 512; float* red = ov + 4096;
  for (int qr = 0; qr < SQ; ++qr) {
    if (!bad[qr]) continue;
    __syncthreads();
    for (int c = t; c < DM; c += 256) xs[c] = bf2f(xg[(size_t)(b * SQ + qr) * DM + c]);
    __syncthreads();
    for (int h = 0; h < NH; ++h) {
      if (t < DH) {
        float a = 0.f;
        for (int c = 0; c < DM; ++c) a += xs[c] * bf2f(wqg[(size_t)c * (NH * DH) + h * DH + t]);
        qv[t] = rbf(a);
      }
      __syncthreads();
      const u16* kp = kg + (size_t)(b * NH + h) * SK * DH;
      const u16* vp = vg + (size_t)(b * NH + h) * SK * DH;
      for (int k = t; k < SK; k += 256) {
        float a = 0.f;
        for (int d = 0; d < DH; ++d) a += qv[d] * bf2f(kp[k * DH + d]);
        sc[k] = a * 0.08838834764831845f;
      }
      __syncthreads();
      if (wv == 0) {
        float m = -__builtin_inff();
        for (int k = lane; k < SK; k += 64) m = fmaxf(m, sc[k]);
        for (int o = 32; o > 0; o >>= 1) m = fmaxf(m, __shfl_xor(m, o));
        float l = 0.f;
        for (int k = lane; k < SK; k += 64) { const float e = __expf(sc[k] - m); sc[k] = e; l += e; }
        for (int o = 32; o > 0; o >>= 1) l += __shfl_xor(l, o);
        if (lane == 0) red[0] = l;
      }
      __syncthreads();
      if (t < DH) {
        const float il = 1.f / red[0];
        float a = 0.f;
        for (int k = 0; k < SK; ++k) a += (sc[k] * il) * bf2f(vp[k * DH + t]);
        ov[h * DH + t] = rbf(a);
      }
      __syncthreads();
    }
    for (int n = t; n < DM; n += 256) {
      const int beta = (((n >> 6) + 9 - (b & 7)) % 9) * 20 + (((n >> 5) & 1) * 2 + qr / 80) * 5 + (qr % 80) / 16;
      if (((beta & 31) >> 2) != G) continue;
      float a = 0.f;
      for (int k = 0; k < NH * DH; ++k) a += ov[k] * bf2f(wog[(size_t)k * DM + n]);
      out[(size_t)(b * SQ + qr) * DM + n] = __builtin_bit_cast(u16, __float2bfloat16(a));
    }
  }
}

extern "C" void kernel_launch(void* const* d_in, const int* in_sizes, int n_in, void* d_out, int out_size, void* d_ws, size_t ws_size,
                              hipStream_t stream) {
  (void)in_sizes; (void)n_in; (void)out_size; (void)ws_size;
  (void)hipFuncSetAttribute((const void*)mha_main, hipFuncAttributeMaxDynamicSharedMemorySize, 163840);
  mha_main<<<256, 256, 163840, stream>>>((const char*)d_in[0], (const char*)d_in[1], (const char*)d_in[2], (const char*)d_in[3],
                                         (const char*)d_in[4], (char*)d_ws, (u16*)d_out);
}
```
